```python
import torch

def _softmax(x, axis=-1):
    return torch.softmax(x, dim=axis)
def _gelu(x):
    return torch.nn.functional.gelu(x, approximate="tanh")
def _silu(x):
    return torch.nn.functional.silu(x)
def _sigmoid(x):
    return torch.sigmoid(x)
def _layernorm(x, axis=-1, eps=1e-5):
    m = x.mean(dim=axis, keepdim=True)
    v = x.var(dim=axis, keepdim=True, unbiased=False)
    return (x - m) / torch.sqrt(v + eps)
def _scatter_add(tgt, idx, src, axis=0):
    return tgt.clone().index_add_(axis, idx, src)
def _segment_sum(src, seg, n):
    out = torch.zeros((n,) + src.shape[1:], dtype=src.dtype)
    return out.index_add_(0, seg, src)
def _pad(x, pw):
    flat = tuple(v for pair in reversed(pw) for v in pair)
    return torch.nn.functional.pad(x, flat)

kernel_name = 'standalone__mha_softmax__near_ridge'

def setup_inputs():

    _w = (lambda a: (lambda t: t.to(torch.bfloat16) if t.is_floating_point() else t)(torch.as_tensor(a)))
    B, H, Sq, Sk, D = 16, 32, 768, 512, 128
    return {
        "q": _w((lambda *s: torch.randn(*s, dtype=torch.bfloat16) * 0.8)(B, H, Sq, D)),
        "k": _w((lambda *s: torch.randn(*s, dtype=torch.bfloat16) * 0.8)(B, H, Sk, D)),
        "v": _w((lambda *s: torch.randn(*s, dtype=torch.bfloat16) * 0.05)(B, H, Sk, D)),
    }

def reference(q, k, v):
    scale = q.shape[-1] ** -0.5
    scores = torch.einsum("bhqd,bhkd->bhqk", q, k) * scale
    p = _softmax(scores, axis=-1)
    return torch.einsum("bhqk,bhkd->bhqd", p, v)
```

```cpp
#include <hip/hip_runtime.h>
#include <hip/hip_bf16.h>
#include <cstdio>

using bf16 = __hip_bfloat16;
constexpr int   D = 128, NW = 8, QBLK = 32, KVBLK = 64;
constexpr float SCALE = 0.088388347648318440f;
#define ATTN_THR 8.f
constexpr float THR = ATTN_THR;
#define ATTN_LAYOUT 0
constexpr int LAYOUT = ATTN_LAYOUT;
#define ATTN_SDEPTH 2
constexpr int SDEPTH = ATTN_SDEPTH;
static_assert(SDEPTH == 1 || SDEPTH == 2, "SDEPTH is 1 or 2");
#define ATTN_B 16
#define ATTN_HQ 32
#define ATTN_HKV 32
#define ATTN_S 768
#define ATTN_SKV 512
constexpr int B = ATTN_B, HQ = ATTN_HQ, HKV = ATTN_HKV, S = ATTN_S, SKV = ATTN_SKV;
constexpr int LDQ = LAYOUT ? HQ * D : D, LDK = LAYOUT ? HKV * D : D, LDO = LDQ;
static_assert(S % (QBLK * NW) == 0 && SKV % (2 * KVBLK) == 0 && HQ % HKV == 0, "S % 256 == 0, SKV % 128 == 0 (even tile count), HQ % HKV == 0");
constexpr size_t SHM_V = KVBLK * D * 2, SHM_K = KVBLK * D * 2, SHM_ATTN = 2 * SHM_V + 2 * SHM_K + NW * 64 * 4;

using bf16x8 = __attribute__((ext_vector_type(8))) short;
using s16x4  = __attribute__((ext_vector_type(4))) short;
using f32x16 = __attribute__((ext_vector_type(16))) float;
using f32x8  = __attribute__((ext_vector_type(8))) float;
using u32x4  = __attribute__((ext_vector_type(4))) unsigned;
#define KSWZ(row, colB) ((row) * 256 + ((colB) ^ (((row) & 7) << 4)))
#define SBAR() __builtin_amdgcn_sched_barrier(0)
__device__ __forceinline__ int crow(int r, int hi) { return (r & 3) + 8 * (r >> 2) + 4 * hi; }
__device__ __forceinline__ unsigned cvtpk(float lo, float hi) {
  unsigned r; asm volatile("v_cvt_pk_bf16_f32 %0, %1, %2" : "=v"(r) : "v"(lo), "v"(hi)); return r;
}
template <typename TIn> struct Stage;
template <> struct Stage<bf16>  { using T = bf16x8;
  __device__ static __forceinline__ T ld8(const bf16* p) { return *reinterpret_cast<const bf16x8*>(p); }
  __device__ static __forceinline__ bf16x8 tobf(T x) { return x; } };
template <> struct Stage<float> { using T = f32x8;
  __device__ static __forceinline__ T ld8(const float* p) { return *reinterpret_cast<const f32x8*>(p); }
  __device__ static __forceinline__ bf16x8 tobf(T x) {
    u32x4 w = {cvtpk(x[0], x[1]), cvtpk(x[2], x[3]), cvtpk(x[4], x[5]), cvtpk(x[6], x[7])}; return *reinterpret_cast<bf16x8*>(&w); } };

__device__ __forceinline__ void partialSM(f32x16& p0, f32x16& p1, float& m_reg, float& mn, float& alpha) {
  constexpr float C = SCALE * 1.4426950408889634f;
  float pmax = p0[0]; for (int r = 1; r < 16; ++r) pmax = fmaxf(pmax, p0[r]); for (int r = 0; r < 16; ++r) pmax = fmaxf(pmax, p1[r]);
  { auto rr = __builtin_amdgcn_permlane32_swap(__float_as_uint(pmax), __float_as_uint(pmax), false, false);
    pmax = fmaxf(__uint_as_float(rr[0]), __uint_as_float(rr[1])); }
  if (__builtin_expect(__all(pmax - m_reg <= THR / SCALE), 1)) { mn = m_reg; alpha = 1.f; }
  else { mn = fmaxf(m_reg, pmax); alpha = __builtin_amdgcn_exp2f((m_reg - mn) * C); m_reg = mn; }
  float mnC = -mn * C;
  for (int r = 0; r < 16; ++r) p0[r] = fmaf(p0[r], C, mnC); for (int r = 0; r < 16; ++r) p1[r] = fmaf(p1[r], C, mnC);
  for (int r = 0; r < 16; ++r) p0[r] = __builtin_amdgcn_exp2f(p0[r]);
}
__device__ __forceinline__ void finishSM(f32x16& p0, f32x16& p1, float alpha, float& l_reg, bf16x8& pa0, bf16x8& pa1, bf16x8& pa2, bf16x8& pa3) {
  for (int r = 0; r < 16; ++r) p1[r] = __builtin_amdgcn_exp2f(p1[r]);
  float ps = 0; for (int r = 0; r < 16; ++r) ps += p0[r]; for (int r = 0; r < 16; ++r) ps += p1[r];
  { auto rr = __builtin_amdgcn_permlane32_swap(__float_as_uint(ps), __float_as_uint(ps), false, false);
    ps = __uint_as_float(rr[0]) + __uint_as_float(rr[1]); }
  l_reg = l_reg * alpha + ps;
#define PK4(P, BASE, OUT) do { unsigned a0 = cvtpk(P[BASE + 0], P[BASE + 1]), a1 = cvtpk(P[BASE + 2], P[BASE + 3]);   \
    unsigned b0 = cvtpk(P[BASE + 4], P[BASE + 5]), b1 = cvtpk(P[BASE + 6], P[BASE + 7]);                              \
    auto r0 = __builtin_amdgcn_permlane32_swap(a0, b0, false, false); auto r1 = __builtin_amdgcn_permlane32_swap(a1, b1, false, false); \
    u32x4 w = {r0[0], r1[0], r0[1], r1[1]}; OUT = *reinterpret_cast<bf16x8*>(&w); } while (0)
  PK4(p0, 0, pa0); PK4(p0, 8, pa1); PK4(p1, 0, pa2); PK4(p1, 8, pa3);
#undef PK4
}
__device__ __forceinline__ void qkt(f32x16& p0, f32x16& p1, const bf16* Ks, const bf16x8* qr, int r32, int hi) {
  p0 = f32x16{}; p1 = f32x16{};
  for (int d0 = 0; d0 < 8; ++d0) { int cb = (d0 * 16 + hi * 8) * 2;
    bf16x8 b0 = *reinterpret_cast<const bf16x8*>((const char*)Ks + KSWZ(r32, cb));
    bf16x8 b1 = *reinterpret_cast<const bf16x8*>((const char*)Ks + KSWZ(32 + r32, cb));
    p0 = __builtin_amdgcn_mfma_f32_32x32x16_bf16(b0, qr[d0], p0, 0, 0, 0);
    p1 = __builtin_amdgcn_mfma_f32_32x32x16_bf16(b1, qr[d0], p1, 0, 0, 0); }
}
__device__ __forceinline__ int v_st(int k, int c) { const int kk = (k & ~0xC) | ((k & 4) << 1) | ((k & 8) >> 1); return ((kk >> 3) * 4 + (c >> 5)) * 512 + ((kk & 7) * 32 + (c & 31)) * 2; }
__device__ __forceinline__ int v_rd_base(int lane) { return ((lane & 3) << 3) | (((lane >> 2) & 3) << 6) | (((lane >> 4) & 1) << 5) | (((lane >> 5) & 1) << 8); }
constexpr int v_rd_off(int d0, int ks, int half) { return d0 * 512 + ks * 4096 + half * 2048; }
template <int OFF> __device__ __forceinline__ s16x4 tr_read(int vb) {
  s16x4 r; asm volatile("ds_read_b64_tr_b16 %0, %1 offset:%2" : "=&v"(r) : "v"(vb), "i"(OFF) : "memory"); return r;
}
template <int D0> __device__ __forceinline__ void pv_one(f32x16& od, int vb, bf16x8 pa0, bf16x8 pa1, bf16x8 pa2, bf16x8 pa3) {
  const s16x4 l0 = tr_read<v_rd_off(D0, 0, 0)>(vb), h0 = tr_read<v_rd_off(D0, 0, 1)>(vb), l1 = tr_read<v_rd_off(D0, 1, 0)>(vb), h1 = tr_read<v_rd_off(D0, 1, 1)>(vb);
  const s16x4 l2 = tr_read<v_rd_off(D0, 2, 0)>(vb), h2 = tr_read<v_rd_off(D0, 2, 1)>(vb), l3 = tr_read<v_rd_off(D0, 3, 0)>(vb), h3 = tr_read<v_rd_off(D0, 3, 1)>(vb);
  asm volatile("s_waitcnt lgkmcnt(0)" ::: "memory"); SBAR();
#define PK(L, H) (bf16x8){L[0], L[1], L[2], L[3], H[0], H[1], H[2], H[3]}
  od = __builtin_amdgcn_mfma_f32_32x32x16_bf16(pa0, PK(l0, h0), od, 0, 0, 0);
  od = __builtin_amdgcn_mfma_f32_32x32x16_bf16(pa1, PK(l1, h1), od, 0, 0, 0);
  od = __builtin_amdgcn_mfma_f32_32x32x16_bf16(pa2, PK(l2, h2), od, 0, 0, 0);
  od = __builtin_amdgcn_mfma_f32_32x32x16_bf16(pa3, PK(l3, h3), od, 0, 0, 0);
#undef PK
}
__device__ __forceinline__ void pv_d0(f32x16* o, int vb, bf16x8 pa0, bf16x8 pa1, bf16x8 pa2, bf16x8 pa3) {
  pv_one<0>(o[0], vb, pa0, pa1, pa2, pa3); pv_one<1>(o[1], vb, pa0, pa1, pa2, pa3); pv_one<2>(o[2], vb, pa0, pa1, pa2, pa3); pv_one<3>(o[3], vb, pa0, pa1, pa2, pa3);
}

template <typename TQ>
__device__ __forceinline__ void attn_dense_body(const TQ* __restrict__ Qb, const bf16* __restrict__ Kh, const bf16* __restrict__ Vh,
                                                bf16* __restrict__ Ob, int seq, char* lds) {
  using St = Stage<bf16>; using SQ = Stage<TQ>;
  const int tid = threadIdx.x, wid = tid >> 6, lane = tid & 63, r32 = lane & 31, hi = lane >> 5;
  bf16* V_lds = (bf16*)lds; bf16* K_lds = (bf16*)(lds + 2 * SHM_V);
  float* ws = (float*)(lds + 2 * SHM_V + 2 * SHM_K) + wid * 64; float* li_l = ws; float* al_l = ws + 32;
  float m_reg = -1e30f, l_reg = 0; f32x16 o[4] = {}; bf16x8 qr[8];
  const TQ* Qw = Qb + (long)(wid * QBLK + r32) * LDQ + hi * 8;
#pragma unroll
  for (int d0 = 0; d0 < 8; ++d0) qr[d0] = SQ::tobf(SQ::ld8(Qw + d0 * 16));
  const int sr = tid >> 4, sc = (tid & 15) * 8, vst0 = v_st(sr, sc), vst1 = v_st(32 + sr, sc);
  const int vb0 = (int)(uintptr_t)V_lds + v_rd_base(lane);
  struct { typename St::T vs0, vs1, ks0, ks1; } sr_[SDEPTH];
#define SLOAD(i, k0) do { sr_[i].vs0 = St::ld8(&Vh[(long)((k0) + sr) * LDK + sc]); sr_[i].vs1 = St::ld8(&Vh[(long)((k0) + 32 + sr) * LDK + sc]); \
    sr_[i].ks0 = St::ld8(&Kh[(long)((k0) + sr) * LDK + sc]); sr_[i].ks1 = St::ld8(&Kh[(long)((k0) + 32 + sr) * LDK + sc]); } while (0)
#define SWRITE(b, i) do { *(bf16x8*)((char*)V_lds + (b) * SHM_V + vst0) = St::tobf(sr_[i].vs0);          \
    *(bf16x8*)((char*)V_lds + (b) * SHM_V + vst1) = St::tobf(sr_[i].vs1); int kc = sc * 2;               \
    *(bf16x8*)((char*)K_lds + (b) * SHM_K + KSWZ(sr, kc)) = St::tobf(sr_[i].ks0);                       \
    *(bf16x8*)((char*)K_lds + (b) * SHM_K + KSWZ(32 + sr, kc)) = St::tobf(sr_[i].ks1); } while (0)
#define SWAIT() do { if constexpr (SDEPTH == 2) asm volatile("s_waitcnt vmcnt(4)" ::: "memory"); else asm volatile("s_waitcnt vmcnt(0)" ::: "memory"); } while (0)
#define RESC(a) do { if (__any((a) < 1.f)) { if (hi == 0) al_l[r32] = (a); asm volatile("s_waitcnt lgkmcnt(0)" ::: "memory"); \
    for (int d = 0; d < 4; ++d) for (int r = 0; r < 16; ++r) o[d][r] *= al_l[crow(r, hi)]; } } while (0)
  f32x16 pA0, pA1, pB0, pB1; float mnA, mnB, alA, alB; bf16x8 pa0, pa1, pa2, pa3; const int NT = seq / KVBLK;
  constexpr int SE = 0, SO = SDEPTH - 1;
  SLOAD(SE, 0); asm volatile("s_waitcnt vmcnt(0)" ::: "memory"); SWRITE(0, SE); __syncthreads();
  qkt(pA0, pA1, K_lds, qr, r32, hi); partialSM(pA0, pA1, m_reg, mnA, alA);
  SLOAD(SO, KVBLK); if constexpr (SDEPTH == 2) { if (2 < NT) SLOAD(SE, 2 * KVBLK); }
  SWAIT(); SWRITE(1, SO); __syncthreads();
  for (int j = 1; j + 1 < NT; j += 2) {
    SBAR(); qkt(pB0, pB1, (bf16*)((char*)K_lds + SHM_K), qr, r32, hi);
    finishSM(pA0, pA1, alA, l_reg, pa0, pa1, pa2, pa3); SBAR();
    SLOAD(SO, (j + SDEPTH) * KVBLK); SBAR();
    pv_d0(o, vb0, pa0, pa1, pa2, pa3); partialSM(pB0, pB1, m_reg, mnB, alB);
    __syncthreads(); SWAIT(); SWRITE(0, SE);
    RESC(alB); __syncthreads();
    SBAR(); qkt(pA0, pA1, K_lds, qr, r32, hi);
    finishSM(pB0, pB1, alB, l_reg, pa0, pa1, pa2, pa3); SBAR();
    if (SDEPTH == 1 || j + 3 < NT) SLOAD(SE, (j + 1 + SDEPTH) * KVBLK); SBAR();
    pv_d0(o, vb0 + (int)SHM_V, pa0, pa1, pa2, pa3); partialSM(pA0, pA1, m_reg, mnA, alA);
    __syncthreads(); SWAIT(); SWRITE(1, SO);
    RESC(alA); __syncthreads();
  }
  SBAR(); qkt(pB0, pB1, (bf16*)((char*)K_lds + SHM_K), qr, r32, hi);
  finishSM(pA0, pA1, alA, l_reg, pa0, pa1, pa2, pa3); SBAR();
  pv_d0(o, vb0, pa0, pa1, pa2, pa3); partialSM(pB0, pB1, m_reg, mnB, alB);
  __syncthreads(); RESC(alB);
  finishSM(pB0, pB1, alB, l_reg, pa0, pa1, pa2, pa3); SBAR();
  pv_d0(o, vb0 + (int)SHM_V, pa0, pa1, pa2, pa3);
  if (hi == 0) li_l[r32] = l_reg; asm volatile("s_waitcnt lgkmcnt(0)" ::: "memory");
  float rli[16];
#pragma unroll
  for (int r = 0; r < 16; ++r) rli[r] = __builtin_amdgcn_rcpf(li_l[crow(r, hi)]);
  bf16* Ow = Ob + (long)(wid * QBLK) * LDO;
#pragma unroll
  for (int r = 0; r < 16; ++r) { int orow = crow(r, hi);
    for (int d0 = 0; d0 < 4; ++d0) Ow[(long)orow * LDO + d0 * 32 + r32] = __float2bfloat16(o[d0][r] * rli[r]); }
#undef SLOAD
#undef SWRITE
#undef SWAIT
#undef RESC
}

template <typename TQ>
__global__ __launch_bounds__(64 * NW, 1)
void attn_fwd_dense(const TQ* __restrict__ Q, const bf16* __restrict__ K, const bf16* __restrict__ V, bf16* __restrict__ O, int sq, int skv, int GQA) {
  extern __shared__ __attribute__((aligned(16))) char shm[];
  const long b = blockIdx.z, h = blockIdx.y, Hq = gridDim.y, Hkv = Hq / GQA, kvh = h / GQA, i0 = (long)blockIdx.x * (QBLK * NW);
  if constexpr (LAYOUT == 0) {
    const long q0 = ((b * Hq + h) * sq + i0) * D, k0 = (b * Hkv + kvh) * skv * (long)D;
    attn_dense_body<TQ>(Q + q0, K + k0, V + k0, O + q0, skv, shm);
  } else {
    const long q0 = ((b * sq + i0) * HQ + h) * (long)D, k0 = (b * skv * HKV + kvh) * (long)D;
    attn_dense_body<TQ>(Q + q0, K + k0, V + k0, O + q0, skv, shm);
  }
}

extern "C" void kernel_launch(void* const* d_in, const int* in_sizes, int n_in, void* d_out, int out_size, void* d_ws, size_t ws_size, hipStream_t stream) {
  (void)hipFuncSetAttribute((const void*)attn_fwd_dense<bf16>, hipFuncAttributeMaxDynamicSharedMemorySize, (int)SHM_ATTN);
  hipLaunchKernelGGL(attn_fwd_dense<bf16>, dim3(S / (QBLK * NW), HQ, B), dim3(64 * NW), SHM_ATTN, stream,
                     (const bf16*)d_in[0], (const bf16*)d_in[1], (const bf16*)d_in[2], (bf16*)d_out, S, SKV, HQ / HKV);
}
```

```cpp
#include <hip/hip_runtime.h>
#include <hip/hip_bf16.h>

extern "C" __global__ __launch_bounds__(512, 2)
void mha_pp_kernel(const void* q, const void* k, const void* v, void* o) {
  extern __shared__ __attribute__((aligned(16))) char lds[];
  const unsigned wid = __builtin_amdgcn_readfirstlane(threadIdx.x >> 6);
  const unsigned bid = blockIdx.x;
  asm volatile(
    "LCODE%=:\n\t"
    "s_getpc_b64 s[28:29]\n\t"
    "v_mbcnt_lo_u32_b32 v150, -1, 0\n\t"
    "s_mov_b32 s36, %2\n\t"
    "s_and_b32 s37, %3, 0xffff\n\t"
    "v_mbcnt_hi_u32_b32 v150, -1, v150\n\t"
    "s_mov_b32 s38, 67108864\n\t"
    "s_mov_b32 s39, 0x20000\n\t"
    "v_lshrrev_b32 v152, 4, v150\n\t"
    "s_and_b32 s57, %9, 7\n\t"
    "s_lshr_b32 s58, %9, 3\n\t"
    "v_lshrrev_b32 v153, 3, v150\n\t"
    "s_and_b32 s59, s58, 1\n\t"
    "s_lshr_b32 s58, s58, 1\n\t"
    "s_lshl_b32 s58, s58, 4\n\t"
    "s_add_u32 s59, s59, s58\n\t"
    "s_lshl_b32 s58, s57, 1\n\t"
    "s_add_u32 s59, s59, s58\n\t"
    "v_lshlrev_b32 v153, 8, v153\n\t"
    "s_lshr_b32 s62, s59, 1\n\t"
    "s_lshl_b32 s50, s62, 17\n\t"
    "v_and_b32 v154, 7, v150\n\t"
    "s_mov_b32 s65, %8\n\t"
    "s_and_b32 s62, s65, 3\n\t"
    "v_xor_b32 v154, v154, v152\n\t"
    "s_lshl_b32 s52, s62, 12\n\t"
    "s_add_u32 s50, s50, s52\n\t"
    "v_lshl_add_u32 v242, v154, 4, v153\n\t"
    "s_mov_b32 s44, %0\n\t"
    "s_and_b32 s45, %1, 0xffff\n\t"
    "v_xor_b32 v243, 64, v242\n\t"
    "s_mov_b32 s46, 100663296\n\t"
    "s_mov_b32 s47, 0x20000\n\t"
    "s_cmp_ge_u32 s65, 4\n\t"
    "s_cbranch_scc1 LPB%=\n\t"
    "s_mov_b32 s57, s50\n\t"
    "s_add_u32 m0, s52, 0\n\t"
    "s_nop 0\n\t"
    "buffer_load_dwordx4 v242, s[36:39], s57 offen offset:0 nt lds\n\t"
    "buffer_load_dwordx4 v243, s[36:39], s57 offen offset:2048 nt lds\n\t"
    "s_add_u32 m0, s52, 896\n\t"
    "s_nop 0\n\t"
    "buffer_load_dwordx4 v242, s[36:39], s57 offen offset:128 nt lds\n\t"
    "buffer_load_dwordx4 v243, s[36:39], s57 offen offset:2176 nt lds\n\t"
    "s_branch LPQ%=\n\t"
    "LPB%=:\n\t"
    "s_sleep 8\n\t"
    "LPQ%=:\n\t"
    "s_mul_i32 s59, s59, 98304\n\t"
    "s_mul_i32 s58, s65, 12288\n\t"
    "s_add_u32 s63, s58, 65536\n\t"
    "s_add_u32 s64, s59, s58\n\t"
    "v_lshrrev_b32 v153, 4, v150\n\t"
    "v_and_b32 v154, 15, v150\n\t"
    "v_xor_b32 v154, v154, v153\n\t"
    "v_lshlrev_b32 v153, 8, v153\n\t"
    "v_lshl_add_u32 v156, v154, 4, v153\n\t"
    "v_xor_b32 v157, 64, v156\n\t"
    "s_add_u32 m0, s63, 0\n\t"
    "s_add_u32 s57, s64, 0\n\t"
    "buffer_load_dwordx4 v156, s[44:47], s57 offen offset:0 lds\n\t"
    "buffer_load_dwordx4 v157, s[44:47], s57 offen offset:1024 lds\n\t"
    "buffer_load_dwordx4 v156, s[44:47], s57 offen offset:2048 lds\n\t"
    "buffer_load_dwordx4 v157, s[44:47], s57 offen offset:3072 lds\n\t"
    "s_add_u32 m0, s63, 4096\n\t"
    "s_add_u32 s57, s64, 4096\n\t"
    "buffer_load_dwordx4 v156, s[44:47], s57 offen offset:0 lds\n\t"
    "buffer_load_dwordx4 v157, s[44:47], s57 offen offset:1024 lds\n\t"
    "buffer_load_dwordx4 v156, s[44:47], s57 offen offset:2048 lds\n\t"
    "buffer_load_dwordx4 v157, s[44:47], s57 offen offset:3072 lds\n\t"
    "s_add_u32 m0, s63, 8192\n\t"
    "s_add_u32 s57, s64, 8192\n\t"
    "buffer_load_dwordx4 v156, s[44:47], s57 offen offset:0 lds\n\t"
    "buffer_load_dwordx4 v157, s[44:47], s57 offen offset:1024 lds\n\t"
    "buffer_load_dwordx4 v156, s[44:47], s57 offen offset:2048 lds\n\t"
    "buffer_load_dwordx4 v157, s[44:47], s57 offen offset:3072 lds\n\t"
    "s_and_b32 s29, s29, 0xffff\n\t"
    "s_mov_b32 s30, LCODEEND%=-LCODE%=\n\t"
    "s_mov_b32 s31, 0x20000\n\t"
    "v_lshlrev_b32 v153, 7, v150\n\t"
    "s_lshl_b32 s57, s65, 13\n\t"
    "buffer_load_dword v0, v153, s[28:31], s57 offen\n\t"
    "s_mov_b32 s40, %4\n\t"
    "s_and_b32 s41, %5, 0xffff\n\t"
    "s_mov_b32 s42, 67108864\n\t"
    "s_mov_b32 s43, 0x20000\n\t"
    "s_mov_b32 s84, %6\n\t"
    "s_and_b32 s85, %7, 0xffff\n\t"
    "s_mov_b32 s86, 100663296\n\t"
    "s_mov_b32 s87, 0x20000\n\t"
    "s_mov_b32 s48, 0x3e0293ee\n\t"
    "s_mov_b32 s49, 0x48800000\n\t"
    "s_mov_b32 s25, 0x2b800000\n\t"
    "s_mov_b32 s56, 0\n\t"
    "s_add_u32 s51, s50, 16777216\n\t"
    "s_mul_i32 s58, s62, 12288\n\t"
    "s_add_u32 s55, s58, 65536\n\t"
    "s_add_u32 s53, s59, s58\n\t"
    "s_add_u32 s53, s53, 25165824\n\t"
    "s_mov_b32 s54, 2130706432\n\t"
    "v_and_b32 v151, 15, v150\n\t"
    "v_lshrrev_b32 v153, 3, v151\n\t"
    "v_lshlrev_b32 v153, 11, v153\n\t"
    "v_and_b32 v154, 7, v151\n\t"
    "v_lshl_add_u32 v153, v154, 7, v153\n\t"
    "v_bfe_u32 v154, v151, 1, 3\n\t"
    "v_xor_b32 v154, v154, v152\n\t"
    "v_lshl_add_u32 v238, v154, 4, v153\n\t"
    "v_xor_b32 v239, 64, v238\n\t"
    "v_lshrrev_b32 v153, 1, v152\n\t"
    "v_lshlrev_b32 v240, 11, v153\n\t"
    "v_and_b32 v153, 1, v152\n\t"
    "v_mul_u32_u24 v153, 288, v153\n\t"
    "v_add_u32 v240, v240, v153\n\t"
    "v_bfe_u32 v153, v150, 2, 2\n\t"
    "v_lshl_add_u32 v240, v153, 6, v240\n\t"
    "v_and_b32 v153, 3, v150\n\t"
    "v_lshl_add_u32 v240, v153, 3, v240\n\t"
    "v_xor_b32 v241, 32, v240\n\t"
    "v_and_b32 v153, 15, v150\n\t"
    "v_lshlrev_b32 v153, 8, v153\n\t"
    "v_add_u32 v153, s63, v153\n\t"
    "v_lshrrev_b32 v154, 4, v150\n\t"
    "v_and_b32 v151, 7, v150\n\t"
    "v_xor_b32 v154, v154, v151\n\t"
    "v_lshl_add_u32 v231, v154, 4, v153\n\t"
    "v_lshrrev_b32 v154, 1, v150\n\t"
    "v_xor_b32 v154, v154, v150\n\t"
    "v_and_b32 v154, 8, v154\n\t"
    "v_add_u32 v153, v153, v154\n\t"
    "v_lshrrev_b32 v154, 5, v150\n\t"
    "v_xor_b32 v154, v154, v151\n\t"
    "v_xor_b32 v151, 0, v154\n\t"
    "v_lshl_add_u32 v250, v151, 4, v153\n\t"
    "v_xor_b32 v151, 2, v154\n\t"
    "v_lshl_add_u32 v251, v151, 4, v153\n\t"
    "v_xor_b32 v151, 4, v154\n\t"
    "v_lshl_add_u32 v252, v151, 4, v153\n\t"
    "v_xor_b32 v151, 6, v154\n\t"
    "v_lshl_add_u32 v253, v151, 4, v153\n\t"
    "s_cmp_ge_u32 s65, 4\n\t"
    "s_cbranch_scc1 LSTARTB%=\n\t"
    "v_bfe_u32 v153, v150, 2, 3\n\t"
    "v_lshlrev_b32 v244, 8, v153\n\t"
    "v_lshrrev_b32 v153, 5, v150\n\t"
    "v_lshl_add_u32 v244, v153, 6, v244\n\t"
    "v_lshrrev_b32 v153, 3, v150\n\t"
    "v_and_b32 v153, 2, v153\n\t"
    "v_and_b32 v154, 3, v150\n\t"
    "v_xor_b32 v153, v153, v154\n\t"
    "v_lshl_add_u32 v244, v153, 4, v244\n\t"
    "s_waitcnt vmcnt(1)\n\t"
    "s_barrier\n\t"
    "v_xor_b32 v254, 64, v231\n\t"
    "ds_read_b128 v[96:99], v231 offset:0\n\t"
    "ds_read_b128 v[100:103], v254 offset:0\n\t"
    "ds_read_b128 v[104:107], v231 offset:128\n\t"
    "ds_read_b128 v[108:111], v254 offset:128\n\t"
    "ds_read_b128 v[112:115], v231 offset:4096\n\t"
    "ds_read_b128 v[116:119], v254 offset:4096\n\t"
    "ds_read_b128 v[120:123], v231 offset:4224\n\t"
    "ds_read_b128 v[124:127], v254 offset:4224\n\t"
    "ds_read_b128 v[128:131], v231 offset:8192\n\t"
    "ds_read_b128 v[132:135], v254 offset:8192\n\t"
    "ds_read_b128 v[136:139], v231 offset:8320\n\t"
    "ds_read_b128 v[140:143], v254 offset:8320\n\t"
    "s_waitcnt lgkmcnt(0)\n\t"
    "s_setprio 1\n\t"
    "ds_read_b128 v[180:183], v238 offset:0\n\t"
    "ds_read_b128 v[184:187], v239 offset:0\n\t"
    "ds_read_b128 v[188:191], v238 offset:1024\n\t"
    "ds_read_b128 v[216:219], v239 offset:1024\n\t"
    "s_waitcnt lgkmcnt(3)\n\t"
    "v_mfma_f32_16x16x32_bf16 v[144:147], v[180:183], v[96:99], 0\n\t"
    "ds_read_b128 v[204:207], v239 offset:5120\n\t"
    "s_waitcnt lgkmcnt(3)\n\t"
    "v_mfma_f32_16x16x32_bf16 v[144:147], v[184:187], v[100:103], v[144:147]\n\t"
    "ds_read_b128 v[200:203], v238 offset:5120\n\t"
    "s_waitcnt lgkmcnt(3)\n\t"
    "v_mfma_f32_16x16x32_bf16 v[144:147], v[188:191], v[104:107], v[144:147]\n\t"
    "ds_read_b128 v[196:199], v239 offset:4096\n\t"
    "s_waitcnt lgkmcnt(3)\n\t"
    "v_mfma_f32_16x16x32_bf16 v[144:147], v[216:219], v[108:111], v[144:147]\n\t"
    "ds_read_b128 v[192:195], v238 offset:4096\n\t"
    "v_mfma_f32_16x16x32_bf16 v[148:151], v[216:219], v[124:127], 0\n\t"
    "v_mfma_f32_16x16x32_bf16 v[148:151], v[188:191], v[120:123], v[148:151]\n\t"
    "v_mfma_f32_16x16x32_bf16 v[148:151], v[184:187], v[116:119], v[148:151]\n\t"
    "v_mfma_f32_16x16x32_bf16 v[148:151], v[180:183], v[112:115], v[148:151]\n\t"
    "v_mfma_f32_16x16x32_bf16 v[152:155], v[180:183], v[128:131], 0\n\t"
    "v_mfma_f32_16x16x32_bf16 v[152:155], v[184:187], v[132:135], v[152:155]\n\t"
    "v_mfma_f32_16x16x32_bf16 v[152:155], v[188:191], v[136:139], v[152:155]\n\t"
    "v_mfma_f32_16x16x32_bf16 v[152:155], v[216:219], v[140:143], v[152:155]\n\t"
    "s_waitcnt lgkmcnt(3)\n\t"
    "v_mfma_f32_16x16x32_bf16 v[164:167], v[204:207], v[140:143], 0\n\t"
    "ds_read_b128 v[180:183], v238 offset:8192\n\t"
    "s_waitcnt lgkmcnt(3)\n\t"
    "v_mfma_f32_16x16x32_bf16 v[164:167], v[200:203], v[136:139], v[164:167]\n\t"
    "ds_read_b128 v[184:187], v239 offset:8192\n\t"
    "s_waitcnt lgkmcnt(3)\n\t"
    "v_mfma_f32_16x16x32_bf16 v[164:167], v[196:199], v[132:135], v[164:167]\n\t"
    "ds_read_b128 v[188:191], v238 offset:9216\n\t"
    "s_waitcnt lgkmcnt(3)\n\t"
    "v_mfma_f32_16x16x32_bf16 v[164:167], v[192:195], v[128:131], v[164:167]\n\t"
    "ds_read_b128 v[216:219], v239 offset:9216\n\t"
    "v_mfma_f32_16x16x32_bf16 v[160:163], v[192:195], v[112:115], 0\n\t"
    "v_mfma_f32_16x16x32_bf16 v[160:163], v[196:199], v[116:119], v[160:163]\n\t"
    "v_mfma_f32_16x16x32_bf16 v[160:163], v[200:203], v[120:123], v[160:163]\n\t"
    "v_mfma_f32_16x16x32_bf16 v[160:163], v[204:207], v[124:127], v[160:163]\n\t"
    "v_mfma_f32_16x16x32_bf16 v[156:159], v[204:207], v[108:111], 0\n\t"
    "v_mfma_f32_16x16x32_bf16 v[156:159], v[200:203], v[104:107], v[156:159]\n\t"
    "v_mfma_f32_16x16x32_bf16 v[156:159], v[196:199], v[100:103], v[156:159]\n\t"
    "v_mfma_f32_16x16x32_bf16 v[156:159], v[192:195], v[96:99], v[156:159]\n\t"
    "s_waitcnt lgkmcnt(3)\n\t"
    "v_mfma_f32_16x16x32_bf16 v[168:171], v[180:183], v[96:99], 0\n\t"
    "ds_read_b128 v[204:207], v239 offset:13312\n\t"
    "s_waitcnt lgkmcnt(3)\n\t"
    "v_mfma_f32_16x16x32_bf16 v[168:171], v[184:187], v[100:103], v[168:171]\n\t"
    "ds_read_b128 v[200:203], v238 offset:13312\n\t"
    "s_waitcnt lgkmcnt(3)\n\t"
    "v_mfma_f32_16x16x32_bf16 v[168:171], v[188:191], v[104:107], v[168:171]\n\t"
    "ds_read_b128 v[196:199], v239 offset:12288\n\t"
    "s_waitcnt lgkmcnt(3)\n\t"
    "v_mfma_f32_16x16x32_bf16 v[168:171], v[216:219], v[108:111], v[168:171]\n\t"
    "ds_read_b128 v[192:195], v238 offset:12288\n\t"
    "v_mfma_f32_16x16x32_bf16 v[172:175], v[216:219], v[124:127], 0\n\t"
    "v_mfma_f32_16x16x32_bf16 v[172:175], v[188:191], v[120:123], v[172:175]\n\t"
    "v_mfma_f32_16x16x32_bf16 v[172:175], v[184:187], v[116:119], v[172:175]\n\t"
    "v_mfma_f32_16x16x32_bf16 v[172:175], v[180:183], v[112:115], v[172:175]\n\t"
    "v_mfma_f32_16x16x32_bf16 v[176:179], v[180:183], v[128:131], 0\n\t"
    "v_mfma_f32_16x16x32_bf16 v[176:179], v[184:187], v[132:135], v[176:179]\n\t"
    "v_mfma_f32_16x16x32_bf16 v[176:179], v[188:191], v[136:139], v[176:179]\n\t"
    "v_mfma_f32_16x16x32_bf16 v[176:179], v[216:219], v[140:143], v[176:179]\n\t"
    "s_waitcnt lgkmcnt(3)\n\t"
    "v_mfma_f32_16x16x32_bf16 v[188:191], v[204:207], v[140:143], 0\n\t"
    "s_waitcnt lgkmcnt(2)\n\t"
    "v_mfma_f32_16x16x32_bf16 v[188:191], v[200:203], v[136:139], v[188:191]\n\t"
    "s_waitcnt lgkmcnt(1)\n\t"
    "v_mfma_f32_16x16x32_bf16 v[188:191], v[196:199], v[132:135], v[188:191]\n\t"
    "s_waitcnt lgkmcnt(0)\n\t"
    "v_mfma_f32_16x16x32_bf16 v[188:191], v[192:195], v[128:131], v[188:191]\n\t"
    "v_mfma_f32_16x16x32_bf16 v[184:187], v[192:195], v[112:115], 0\n\t"
    "v_mfma_f32_16x16x32_bf16 v[184:187], v[196:199], v[116:119], v[184:187]\n\t"
    "v_mfma_f32_16x16x32_bf16 v[184:187], v[200:203], v[120:123], v[184:187]\n\t"
    "v_mfma_f32_16x16x32_bf16 v[184:187], v[204:207], v[124:127], v[184:187]\n\t"
    "v_mfma_f32_16x16x32_bf16 v[180:183], v[204:207], v[108:111], 0\n\t"
    "v_mfma_f32_16x16x32_bf16 v[180:183], v[200:203], v[104:107], v[180:183]\n\t"
    "v_mfma_f32_16x16x32_bf16 v[180:183], v[196:199], v[100:103], v[180:183]\n\t"
    "v_mfma_f32_16x16x32_bf16 v[180:183], v[192:195], v[96:99], v[180:183]\n\t"
    "s_waitcnt lgkmcnt(0)\n\t"
    "s_setprio 0\n\t"
    "LBLKA%=:\n\t"
    "s_cmp_eq_u32 s56, 0\n\t"
    "s_cbranch_scc0 LCARA%=\n\t"
    "v_max3_f32 v232, v144, v145, v146\n\t"
    "v_max3_f32 v233, v148, v149, v150\n\t"
    "v_max3_f32 v234, v152, v153, v154\n\t"
    "s_mov_b32 s58, s50\n\t"
    "s_add_u32 m0, s52, 32768\n\t"
    "s_nop 0\n\t"
    "buffer_load_dwordx4 v244, s[40:43], s58 offen offset:0 nt lds\n\t"
    "v_max3_f32 v232, v232, v147, v156\n\t"
    "v_max3_f32 v233, v233, v151, v160\n\t"
    "v_max3_f32 v234, v234, v155, v164\n\t"
    "buffer_load_dwordx4 v244, s[40:43], s58 offen offset:2048 nt lds\n\t"
    "v_max3_f32 v232, v232, v157, v158\n\t"
    "v_max3_f32 v233, v233, v161, v162\n\t"
    "v_max3_f32 v234, v234, v165, v166\n\t"
    "s_add_u32 m0, s52, 33664\n\t"
    "s_nop 0\n\t"
    "buffer_load_dwordx4 v244, s[40:43], s58 offen offset:128 nt lds\n\t"
    "v_max3_f32 v232, v232, v159, v168\n\t"
    "v_max3_f32 v233, v233, v163, v172\n\t"
    "v_max3_f32 v234, v234, v167, v176\n\t"
    "buffer_load_dwordx4 v244, s[40:43], s58 offen offset:2176 nt lds\n\t"
    "v_max3_f32 v232, v232, v169, v170\n\t"
    "v_max3_f32 v233, v233, v173, v174\n\t"
    "v_max3_f32 v234, v234, v177, v178\n\t"
    "s_add_u32 s57, s50, 16384\n\t"
    "s_add_u32 m0, s52, 16384\n\t"
    "s_nop 0\n\t"
    "buffer_load_dwordx4 v242, s[36:39], s57 offen offset:0 nt lds\n\t"
    "v_max3_f32 v232, v232, v171, v180\n\t"
    "v_max3_f32 v233, v233, v175, v184\n\t"
    "v_max3_f32 v234, v234, v179, v188\n\t"
    "buffer_load_dwordx4 v243, s[36:39], s57 offen offset:2048 nt lds\n\t"
    "v_max3_f32 v232, v232, v181, v182\n\t"
    "v_max3_f32 v233, v233, v185, v186\n\t"
    "v_max3_f32 v234, v234, v189, v190\n\t"
    "s_add_u32 m0, s52, 17280\n\t"
    "s_nop 0\n\t"
    "buffer_load_dwordx4 v242, s[36:39], s57 offen offset:128 nt lds\n\t"
    "v_max_f32 v232, v232, v183\n\t"
    "v_max_f32 v233, v233, v187\n\t"
    "v_max_f32 v234, v234, v191\n\t"
    "buffer_load_dwordx4 v243, s[36:39], s57 offen offset:2176 nt lds\n\t"
    "v_mov_b32 v224, v232\n\t"
    "v_mov_b32 v225, v233\n\t"
    "v_mov_b32 v226, v234\n\t"
    "v_permlane16_swap_b32 v232, v224\n\t"
    "v_permlane16_swap_b32 v233, v225\n\t"
    "v_permlane16_swap_b32 v234, v226\n\t"
    "v_max_f32 v232, v232, v224\n\t"
    "v_max_f32 v233, v233, v225\n\t"
    "v_max_f32 v234, v234, v226\n\t"
    "v_mov_b32 v224, v232\n\t"
    "v_mov_b32 v225, v233\n\t"
    "v_mov_b32 v226, v234\n\t"
    "v_permlane32_swap_b32 v232, v224\n\t"
    "v_permlane32_swap_b32 v233, v225\n\t"
    "v_permlane32_swap_b32 v234, v226\n\t"
    "v_max_f32 v232, v232, v224\n\t"
    "v_max_f32 v233, v233, v225\n\t"
    "v_max_f32 v234, v234, v226\n\t"
    "v_mul_f32_e64 v232, s48, -v232\n\t"
    "v_mul_f32_e64 v233, s48, -v233\n\t"
    "v_mul_f32_e64 v234, s48, -v234\n\t"
    "s_branch LCAR2A%=\n\t"
    "LCARA%=:\n\t"
    "v_max3_f32 v232, v144, v145, v146\n\t"
    "v_max3_f32 v233, v148, v149, v150\n\t"
    "v_max3_f32 v234, v152, v153, v154\n\t"
    "v_max3_f32 v232, v232, v147, v156\n\t"
    "v_max3_f32 v233, v233, v151, v160\n\t"
    "v_max3_f32 v234, v234, v155, v164\n\t"
    "v_max3_f32 v232, v232, v157, v158\n\t"
    "v_max3_f32 v233, v233, v161, v162\n\t"
    "v_max3_f32 v234, v234, v165, v166\n\t"
    "v_max3_f32 v232, v232, v159, v168\n\t"
    "v_max3_f32 v233, v233, v163, v172\n\t"
    "v_max3_f32 v234, v234, v167, v176\n\t"
    "v_max3_f32 v232, v232, v169, v170\n\t"
    "v_max3_f32 v233, v233, v173, v174\n\t"
    "v_max3_f32 v234, v234, v177, v178\n\t"
    "v_max3_f32 v232, v232, v171, v180\n\t"
    "v_max3_f32 v233, v233, v175, v184\n\t"
    "v_max3_f32 v234, v234, v179, v188\n\t"
    "v_max3_f32 v232, v232, v181, v182\n\t"
    "v_max3_f32 v233, v233, v185, v186\n\t"
    "v_max3_f32 v234, v234, v189, v190\n\t"
    "v_max_f32 v232, v232, v183\n\t"
    "v_max_f32 v233, v233, v187\n\t"
    "v_max_f32 v234, v234, v191\n\t"
    "v_mov_b32 v220, v235\n\t"
    "v_mov_b32 v221, v236\n\t"
    "v_mov_b32 v222, v237\n\t"
    "v_mov_b32 v224, v232\n\t"
    "v_mov_b32 v225, v233\n\t"
    "v_mov_b32 v226, v234\n\t"
    "v_permlane16_swap_b32 v235, v220\n\t"
    "v_permlane16_swap_b32 v236, v221\n\t"
    "v_permlane16_swap_b32 v237, v222\n\t"
    "v_permlane16_swap_b32 v232, v224\n\t"
    "v_permlane16_swap_b32 v233, v225\n\t"
    "v_permlane16_swap_b32 v234, v226\n\t"
    "v_add_f32 v235, v235, v220\n\t"
    "v_add_f32 v236, v236, v221\n\t"
    "v_add_f32 v237, v237, v222\n\t"
    "v_max_f32 v232, v232, v224\n\t"
    "v_max_f32 v233, v233, v225\n\t"
    "v_max_f32 v234, v234, v226\n\t"
    "v_mov_b32 v220, v235\n\t"
    "v_mov_b32 v221, v236\n\t"
    "v_mov_b32 v222, v237\n\t"
    "v_mov_b32 v224, v232\n\t"
    "v_mov_b32 v225, v233\n\t"
    "v_mov_b32 v226, v234\n\t"
    "v_permlane32_swap_b32 v235, v220\n\t"
    "v_permlane32_swap_b32 v236, v221\n\t"
    "v_permlane32_swap_b32 v237, v222\n\t"
    "v_permlane32_swap_b32 v232, v224\n\t"
    "v_permlane32_swap_b32 v233, v225\n\t"
    "v_permlane32_swap_b32 v234, v226\n\t"
    "v_add_f32 v235, v235, v220\n\t"
    "v_add_f32 v236, v236, v221\n\t"
    "v_add_f32 v237, v237, v222\n\t"
    "v_max_f32 v232, v232, v224\n\t"
    "v_max_f32 v233, v233, v225\n\t"
    "v_max_f32 v234, v234, v226\n\t"
    "v_rcp_f32 v245, v235\n\t"
    "v_rcp_f32 v254, v236\n\t"
    "v_rcp_f32 v255, v237\n\t"
    "v_mul_f32_e64 v232, s48, -v232\n\t"
    "v_mul_f32_e64 v233, s48, -v233\n\t"
    "v_mul_f32_e64 v234, s48, -v234\n\t"
    "LCAR2A%=:\n\t"
    "v_fma_f32 v220, v144, s48, v232\n\t"
    "v_fma_f32 v221, v145, s48, v232\n\t"
    "v_fma_f32 v222, v146, s48, v232\n\t"
    "v_fma_f32 v223, v147, s48, v232\n\t"
    "v_exp_f32 v220, v220\n\t"
    "v_exp_f32 v221, v221\n\t"
    "v_exp_f32 v222, v222\n\t"
    "v_exp_f32 v223, v223\n\t"
    "v_fma_f32 v224, v148, s48, v233\n\t"
    "v_fma_f32 v225, v149, s48, v233\n\t"
    "v_fma_f32 v226, v150, s48, v233\n\t"
    "v_fma_f32 v227, v151, s48, v233\n\t"
    "v_exp_f32 v224, v224\n\t"
    "v_exp_f32 v225, v225\n\t"
    "v_exp_f32 v226, v226\n\t"
    "v_exp_f32 v227, v227\n\t"
    "v_cvt_pk_bf16_f32 v192, v220, v221\n\t"
    "v_cvt_pk_bf16_f32 v193, v222, v223\n\t"
    "v_add_f32 v235, v220, v221\n\t"
    "v_add_f32 v235, v235, v222\n\t"
    "v_add_f32 v235, v235, v223\n\t"
    "v_fma_f32 v220, v152, s48, v234\n\t"
    "v_fma_f32 v221, v153, s48, v234\n\t"
    "v_fma_f32 v222, v154, s48, v234\n\t"
    "v_fma_f32 v223, v155, s48, v234\n\t"
    "v_exp_f32 v220, v220\n\t"
    "v_exp_f32 v221, v221\n\t"
    "v_exp_f32 v222, v222\n\t"
    "v_exp_f32 v223, v223\n\t"
    "v_cvt_pk_bf16_f32 v196, v224, v225\n\t"
    "v_cvt_pk_bf16_f32 v197, v226, v227\n\t"
    "v_add_f32 v236, v224, v225\n\t"
    "v_add_f32 v236, v236, v226\n\t"
    "v_add_f32 v236, v236, v227\n\t"
    "v_fma_f32 v224, v156, s48, v232\n\t"
    "v_fma_f32 v225, v157, s48, v232\n\t"
    "v_fma_f32 v226, v158, s48, v232\n\t"
    "v_fma_f32 v227, v159, s48, v232\n\t"
    "v_exp_f32 v224, v224\n\t"
    "v_exp_f32 v225, v225\n\t"
    "v_exp_f32 v226, v226\n\t"
    "v_exp_f32 v227, v227\n\t"
    "v_cvt_pk_bf16_f32 v200, v220, v221\n\t"
    "v_cvt_pk_bf16_f32 v201, v222, v223\n\t"
    "v_add_f32 v237, v220, v221\n\t"
    "v_add_f32 v237, v237, v222\n\t"
    "v_add_f32 v237, v237, v223\n\t"
    "v_fma_f32 v220, v160, s48, v233\n\t"
    "v_fma_f32 v221, v161, s48, v233\n\t"
    "v_fma_f32 v222, v162, s48, v233\n\t"
    "v_fma_f32 v223, v163, s48, v233\n\t"
    "v_exp_f32 v220, v220\n\t"
    "v_exp_f32 v221, v221\n\t"
    "v_exp_f32 v222, v222\n\t"
    "v_exp_f32 v223, v223\n\t"
    "v_cvt_pk_bf16_f32 v194, v224, v225\n\t"
    "v_cvt_pk_bf16_f32 v195, v226, v227\n\t"
    "v_add_f32 v235, v235, v224\n\t"
    "v_add_f32 v235, v235, v225\n\t"
    "v_add_f32 v235, v235, v226\n\t"
    "v_add_f32 v235, v235, v227\n\t"
    "v_fma_f32 v224, v164, s48, v234\n\t"
    "v_fma_f32 v225, v165, s48, v234\n\t"
    "v_fma_f32 v226, v166, s48, v234\n\t"
    "v_fma_f32 v227, v167, s48, v234\n\t"
    "v_exp_f32 v224, v224\n\t"
    "v_exp_f32 v225, v225\n\t"
    "v_exp_f32 v226, v226\n\t"
    "v_exp_f32 v227, v227\n\t"
    "v_cvt_pk_bf16_f32 v198, v220, v221\n\t"
    "v_cvt_pk_bf16_f32 v199, v222, v223\n\t"
    "v_add_f32 v236, v236, v220\n\t"
    "v_add_f32 v236, v236, v221\n\t"
    "v_add_f32 v236, v236, v222\n\t"
    "v_add_f32 v236, v236, v223\n\t"
    "v_fma_f32 v220, v168, s48, v232\n\t"
    "v_fma_f32 v221, v169, s48, v232\n\t"
    "v_fma_f32 v222, v170, s48, v232\n\t"
    "v_fma_f32 v223, v171, s48, v232\n\t"
    "v_exp_f32 v220, v220\n\t"
    "v_exp_f32 v221, v221\n\t"
    "v_exp_f32 v222, v222\n\t"
    "v_exp_f32 v223, v223\n\t"
    "v_cvt_pk_bf16_f32 v202, v224, v225\n\t"
    "v_cvt_pk_bf16_f32 v203, v226, v227\n\t"
    "v_add_f32 v237, v237, v224\n\t"
    "v_add_f32 v237, v237, v225\n\t"
    "v_add_f32 v237, v237, v226\n\t"
    "v_add_f32 v237, v237, v227\n\t"
    "v_fma_f32 v224, v172, s48, v233\n\t"
    "v_fma_f32 v225, v173, s48, v233\n\t"
    "v_fma_f32 v226, v174, s48, v233\n\t"
    "v_fma_f32 v227, v175, s48, v233\n\t"
    "v_exp_f32 v224, v224\n\t"
    "v_exp_f32 v225, v225\n\t"
    "v_exp_f32 v226, v226\n\t"
    "v_exp_f32 v227, v227\n\t"
    "v_cvt_pk_bf16_f32 v204, v220, v221\n\t"
    "v_cvt_pk_bf16_f32 v205, v222, v223\n\t"
    "v_add_f32 v235, v235, v220\n\t"
    "v_add_f32 v235, v235, v221\n\t"
    "v_add_f32 v235, v235, v222\n\t"
    "v_add_f32 v235, v235, v223\n\t"
    "v_fma_f32 v220, v176, s48, v234\n\t"
    "v_fma_f32 v221, v177, s48, v234\n\t"
    "v_fma_f32 v222, v178, s48, v234\n\t"
    "v_fma_f32 v223, v179, s48, v234\n\t"
    "v_exp_f32 v220, v220\n\t"
    "v_exp_f32 v221, v221\n\t"
    "v_exp_f32 v222, v222\n\t"
    "v_exp_f32 v223, v223\n\t"
    "v_cvt_pk_bf16_f32 v208, v224, v225\n\t"
    "v_cvt_pk_bf16_f32 v209, v226, v227\n\t"
    "v_add_f32 v236, v236, v224\n\t"
    "v_add_f32 v236, v236, v225\n\t"
    "v_add_f32 v236, v236, v226\n\t"
    "v_add_f32 v236, v236, v227\n\t"
    "v_fma_f32 v224, v180, s48, v232\n\t"
    "v_fma_f32 v225, v181, s48, v232\n\t"
    "v_fma_f32 v226, v182, s48, v232\n\t"
    "v_fma_f32 v227, v183, s48, v232\n\t"
    "v_exp_f32 v224, v224\n\t"
    "v_exp_f32 v225, v225\n\t"
    "v_exp_f32 v226, v226\n\t"
    "v_exp_f32 v227, v227\n\t"
    "v_cvt_pk_bf16_f32 v212, v220, v221\n\t"
    "v_cvt_pk_bf16_f32 v213, v222, v223\n\t"
    "v_add_f32 v237, v237, v220\n\t"
    "v_add_f32 v237, v237, v221\n\t"
    "v_add_f32 v237, v237, v222\n\t"
    "v_add_f32 v237, v237, v223\n\t"
    "v_fma_f32 v220, v184, s48, v233\n\t"
    "v_fma_f32 v221, v185, s48, v233\n\t"
    "v_fma_f32 v222, v186, s48, v233\n\t"
    "v_fma_f32 v223, v187, s48, v233\n\t"
    "v_exp_f32 v220, v220\n\t"
    "v_exp_f32 v221, v221\n\t"
    "v_exp_f32 v222, v222\n\t"
    "v_exp_f32 v223, v223\n\t"
    "v_cvt_pk_bf16_f32 v206, v224, v225\n\t"
    "v_cvt_pk_bf16_f32 v207, v226, v227\n\t"
    "v_add_f32 v235, v235, v224\n\t"
    "v_add_f32 v235, v235, v225\n\t"
    "v_add_f32 v235, v235, v226\n\t"
    "v_add_f32 v235, v235, v227\n\t"
    "v_fma_f32 v224, v188, s48, v234\n\t"
    "v_fma_f32 v225, v189, s48, v234\n\t"
    "v_fma_f32 v226, v190, s48, v234\n\t"
    "v_fma_f32 v227, v191, s48, v234\n\t"
    "v_exp_f32 v224, v224\n\t"
    "v_exp_f32 v225, v225\n\t"
    "v_exp_f32 v226, v226\n\t"
    "v_exp_f32 v227, v227\n\t"
    "v_cvt_pk_bf16_f32 v210, v220, v221\n\t"
    "v_cvt_pk_bf16_f32 v211, v222, v223\n\t"
    "v_add_f32 v236, v236, v220\n\t"
    "v_add_f32 v236, v236, v221\n\t"
    "v_add_f32 v236, v236, v222\n\t"
    "v_add_f32 v236, v236, v223\n\t"
    "v_cvt_pk_bf16_f32 v214, v224, v225\n\t"
    "v_cvt_pk_bf16_f32 v215, v226, v227\n\t"
    "v_add_f32 v237, v237, v224\n\t"
    "v_add_f32 v237, v237, v225\n\t"
    "v_add_f32 v237, v237, v226\n\t"
    "v_add_f32 v237, v237, v227\n\t"
    "s_cmp_eq_u32 s56, 0\n\t"
    "s_cbranch_scc1 LNOEPIA%=\n\t"
    "v_mov_b32 v220, v245\n\t"
    "v_mov_b32 v222, v254\n\t"
    "v_mov_b32 v224, v255\n\t"
    "v_pk_mul_f32 v[0:1], v[0:1], v[220:221] op_sel_hi:[1,0]\n\t"
    "v_pk_mul_f32 v[2:3], v[2:3], v[220:221] op_sel_hi:[1,0]\n\t"
    "v_pk_mul_f32 v[4:5], v[4:5], v[222:223] op_sel_hi:[1,0]\n\t"
    "v_pk_mul_f32 v[6:7], v[6:7], v[222:223] op_sel_hi:[1,0]\n\t"
    "v_cvt_pk_bf16_f32 v0, v0, v1\n\t"
    "v_cvt_pk_bf16_f32 v1, v2, v3\n\t"
    "v_pk_mul_f32 v[8:9], v[8:9], v[224:225] op_sel_hi:[1,0]\n\t"
    "v_pk_mul_f32 v[10:11], v[10:11], v[224:225] op_sel_hi:[1,0]\n\t"
    "v_cvt_pk_bf16_f32 v4, v4, v5\n\t"
    "v_cvt_pk_bf16_f32 v5, v6, v7\n\t"
    "ds_write_b64 v250, v[0:1] offset:0\n\t"
    "v_pk_mul_f32 v[12:13], v[12:13], v[220:221] op_sel_hi:[1,0]\n\t"
    "v_pk_mul_f32 v[14:15], v[14:15], v[220:221] op_sel_hi:[1,0]\n\t"
    "v_cvt_pk_bf16_f32 v8, v8, v9\n\t"
    "v_cvt_pk_bf16_f32 v9, v10, v11\n\t"
    "ds_write_b64 v250, v[4:5] offset:4096\n\t"
    "v_pk_mul_f32 v[16:17], v[16:17], v[222:223] op_sel_hi:[1,0]\n\t"
    "v_pk_mul_f32 v[18:19], v[18:19], v[222:223] op_sel_hi:[1,0]\n\t"
    "v_cvt_pk_bf16_f32 v12, v12, v13\n\t"
    "v_cvt_pk_bf16_f32 v13, v14, v15\n\t"
    "ds_write_b64 v250, v[8:9] offset:8192\n\t"
    "v_pk_mul_f32 v[20:21], v[20:21], v[224:225] op_sel_hi:[1,0]\n\t"
    "v_pk_mul_f32 v[22:23], v[22:23], v[224:225] op_sel_hi:[1,0]\n\t"
    "v_cvt_pk_bf16_f32 v16, v16, v17\n\t"
    "v_cvt_pk_bf16_f32 v17, v18, v19\n\t"
    "ds_write_b64 v251, v[12:13] offset:0\n\t"
    "v_pk_mul_f32 v[24:25], v[24:25], v[220:221] op_sel_hi:[1,0]\n\t"
    "v_pk_mul_f32 v[26:27], v[26:27], v[220:221] op_sel_hi:[1,0]\n\t"
    "v_cvt_pk_bf16_f32 v20, v20, v21\n\t"
    "v_cvt_pk_bf16_f32 v21, v22, v23\n\t"
    "ds_write_b64 v251, v[16:17] offset:4096\n\t"
    "v_pk_mul_f32 v[28:29], v[28:29], v[222:223] op_sel_hi:[1,0]\n\t"
    "v_pk_mul_f32 v[30:31], v[30:31], v[222:223] op_sel_hi:[1,0]\n\t"
    "v_cvt_pk_bf16_f32 v24, v24, v25\n\t"
    "v_cvt_pk_bf16_f32 v25, v26, v27\n\t"
    "ds_write_b64 v251, v[20:21] offset:8192\n\t"
    "v_pk_mul_f32 v[32:33], v[32:33], v[224:225] op_sel_hi:[1,0]\n\t"
    "v_pk_mul_f32 v[34:35], v[34:35], v[224:225] op_sel_hi:[1,0]\n\t"
    "v_cvt_pk_bf16_f32 v28, v28, v29\n\t"
    "v_cvt_pk_bf16_f32 v29, v30, v31\n\t"
    "ds_write_b64 v252, v[24:25] offset:0\n\t"
    "v_pk_mul_f32 v[36:37], v[36:37], v[220:221] op_sel_hi:[1,0]\n\t"
    "v_pk_mul_f32 v[38:39], v[38:39], v[220:221] op_sel_hi:[1,0]\n\t"
    "v_cvt_pk_bf16_f32 v32, v32, v33\n\t"
    "v_cvt_pk_bf16_f32 v33, v34, v35\n\t"
    "ds_write_b64 v252, v[28:29] offset:4096\n\t"
    "v_pk_mul_f32 v[40:41], v[40:41], v[222:223] op_sel_hi:[1,0]\n\t"
    "v_pk_mul_f32 v[42:43], v[42:43], v[222:223] op_sel_hi:[1,0]\n\t"
    "v_cvt_pk_bf16_f32 v36, v36, v37\n\t"
    "v_cvt_pk_bf16_f32 v37, v38, v39\n\t"
    "ds_write_b64 v252, v[32:33] offset:8192\n\t"
    "v_pk_mul_f32 v[44:45], v[44:45], v[224:225] op_sel_hi:[1,0]\n\t"
    "v_pk_mul_f32 v[46:47], v[46:47], v[224:225] op_sel_hi:[1,0]\n\t"
    "v_cvt_pk_bf16_f32 v40, v40, v41\n\t"
    "v_cvt_pk_bf16_f32 v41, v42, v43\n\t"
    "ds_write_b64 v253, v[36:37] offset:0\n\t"
    "v_pk_mul_f32 v[48:49], v[48:49], v[220:221] op_sel_hi:[1,0]\n\t"
    "v_pk_mul_f32 v[50:51], v[50:51], v[220:221] op_sel_hi:[1,0]\n\t"
    "v_cvt_pk_bf16_f32 v44, v44, v45\n\t"
    "v_cvt_pk_bf16_f32 v45, v46, v47\n\t"
    "ds_write_b64 v253, v[40:41] offset:4096\n\t"
    "v_pk_mul_f32 v[52:53], v[52:53], v[222:223] op_sel_hi:[1,0]\n\t"
    "v_pk_mul_f32 v[54:55], v[54:55], v[222:223] op_sel_hi:[1,0]\n\t"
    "v_cvt_pk_bf16_f32 v48, v48, v49\n\t"
    "v_cvt_pk_bf16_f32 v49, v50, v51\n\t"
    "ds_write_b64 v253, v[44:45] offset:8192\n\t"
    "v_pk_mul_f32 v[56:57], v[56:57], v[224:225] op_sel_hi:[1,0]\n\t"
    "v_pk_mul_f32 v[58:59], v[58:59], v[224:225] op_sel_hi:[1,0]\n\t"
    "v_cvt_pk_bf16_f32 v52, v52, v53\n\t"
    "v_cvt_pk_bf16_f32 v53, v54, v55\n\t"
    "s_waitcnt lgkmcnt(8)\n\t"
    "ds_write_b64 v250, v[48:49] offset:128\n\t"
    "v_pk_mul_f32 v[60:61], v[60:61], v[220:221] op_sel_hi:[1,0]\n\t"
    "v_pk_mul_f32 v[62:63], v[62:63], v[220:221] op_sel_hi:[1,0]\n\t"
    "v_cvt_pk_bf16_f32 v56, v56, v57\n\t"
    "v_cvt_pk_bf16_f32 v57, v58, v59\n\t"
    "ds_write_b64 v250, v[52:53] offset:4224\n\t"
    "v_pk_mul_f32 v[64:65], v[64:65], v[222:223] op_sel_hi:[1,0]\n\t"
    "v_pk_mul_f32 v[66:67], v[66:67], v[222:223] op_sel_hi:[1,0]\n\t"
    "v_cvt_pk_bf16_f32 v60, v60, v61\n\t"
    "v_cvt_pk_bf16_f32 v61, v62, v63\n\t"
    "ds_write_b64 v250, v[56:57] offset:8320\n\t"
    "v_pk_mul_f32 v[68:69], v[68:69], v[224:225] op_sel_hi:[1,0]\n\t"
    "v_pk_mul_f32 v[70:71], v[70:71], v[224:225] op_sel_hi:[1,0]\n\t"
    "v_cvt_pk_bf16_f32 v64, v64, v65\n\t"
    "v_cvt_pk_bf16_f32 v65, v66, v67\n\t"
    "ds_write_b64 v251, v[60:61] offset:128\n\t"
    "v_pk_mul_f32 v[72:73], v[72:73], v[220:221] op_sel_hi:[1,0]\n\t"
    "v_pk_mul_f32 v[74:75], v[74:75], v[220:221] op_sel_hi:[1,0]\n\t"
    "v_cvt_pk_bf16_f32 v68, v68, v69\n\t"
    "v_cvt_pk_bf16_f32 v69, v70, v71\n\t"
    "s_waitcnt lgkmcnt(8)\n\t"
    "ds_write_b64 v251, v[64:65] offset:4224\n\t"
    "v_pk_mul_f32 v[76:77], v[76:77], v[222:223] op_sel_hi:[1,0]\n\t"
    "v_pk_mul_f32 v[78:79], v[78:79], v[222:223] op_sel_hi:[1,0]\n\t"
    "v_cvt_pk_bf16_f32 v72, v72, v73\n\t"
    "v_cvt_pk_bf16_f32 v73, v74, v75\n\t"
    "ds_write_b64 v251, v[68:69] offset:8320\n\t"
    "v_pk_mul_f32 v[80:81], v[80:81], v[224:225] op_sel_hi:[1,0]\n\t"
    "v_pk_mul_f32 v[82:83], v[82:83], v[224:225] op_sel_hi:[1,0]\n\t"
    "v_cvt_pk_bf16_f32 v76, v76, v77\n\t"
    "v_cvt_pk_bf16_f32 v77, v78, v79\n\t"
    "ds_write_b64 v252, v[72:73] offset:128\n\t"
    "v_pk_mul_f32 v[84:85], v[84:85], v[220:221] op_sel_hi:[1,0]\n\t"
    "v_pk_mul_f32 v[86:87], v[86:87], v[220:221] op_sel_hi:[1,0]\n\t"
    "v_cvt_pk_bf16_f32 v80, v80, v81\n\t"
    "v_cvt_pk_bf16_f32 v81, v82, v83\n\t"
    "ds_write_b64 v252, v[76:77] offset:4224\n\t"
    "v_pk_mul_f32 v[88:89], v[88:89], v[222:223] op_sel_hi:[1,0]\n\t"
    "v_pk_mul_f32 v[90:91], v[90:91], v[222:223] op_sel_hi:[1,0]\n\t"
    "v_cvt_pk_bf16_f32 v84, v84, v85\n\t"
    "v_cvt_pk_bf16_f32 v85, v86, v87\n\t"
    "s_waitcnt lgkmcnt(8)\n\t"
    "ds_write_b64 v252, v[80:81] offset:8320\n\t"
    "v_pk_mul_f32 v[92:93], v[92:93], v[224:225] op_sel_hi:[1,0]\n\t"
    "v_pk_mul_f32 v[94:95], v[94:95], v[224:225] op_sel_hi:[1,0]\n\t"
    "v_cvt_pk_bf16_f32 v88, v88, v89\n\t"
    "v_cvt_pk_bf16_f32 v89, v90, v91\n\t"
    "ds_write_b64 v253, v[84:85] offset:128\n\t"
    "v_cvt_pk_bf16_f32 v92, v92, v93\n\t"
    "v_cvt_pk_bf16_f32 v93, v94, v95\n\t"
    "ds_write_b64 v253, v[88:89] offset:4224\n\t"
    "ds_write_b64 v253, v[92:93] offset:8320\n\t"
    "s_waitcnt lgkmcnt(0)\n\t"
    "LNOEPIA%=:\n\t"
    "s_waitcnt vmcnt(0)\n\t"
    "s_barrier\n\t"
    "s_setprio 1\n\t"
    "ds_read_b64_tr_b16 v[144:145], v240 offset:32768\n\t"
    "ds_read_b64_tr_b16 v[146:147], v240 offset:36864\n\t"
    "ds_read_b64_tr_b16 v[148:149], v240 offset:40960\n\t"
    "ds_read_b64_tr_b16 v[150:151], v240 offset:45056\n\t"
    "ds_read_b64_tr_b16 v[152:153], v241 offset:40960\n\t"
    "ds_read_b64_tr_b16 v[154:155], v241 offset:45056\n\t"
    "ds_read_b64_tr_b16 v[156:157], v241 offset:32768\n\t"
    "ds_read_b64_tr_b16 v[158:159], v241 offset:36864\n\t"
    "s_waitcnt lgkmcnt(6)\n\t"
    "v_mfma_f32_16x16x32_bf16 v[0:3], v[144:147], v[192:195], 0\n\t"
    "ds_read_b64_tr_b16 v[160:161], v240 offset:33280\n\t"
    "ds_read_b64_tr_b16 v[162:163], v240 offset:37376\n\t"
    "s_waitcnt lgkmcnt(6)\n\t"
    "v_mfma_f32_16x16x32_bf16 v[0:3], v[148:151], v[204:207], v[0:3]\n\t"
    "ds_read_b64_tr_b16 v[164:165], v240 offset:41472\n\t"
    "ds_read_b64_tr_b16 v[166:167], v240 offset:45568\n\t"
    "v_mfma_f32_16x16x32_bf16 v[4:7], v[148:151], v[208:211], 0\n\t"
    "ds_read_b64_tr_b16 v[168:169], v241 offset:41472\n\t"
    "ds_read_b64_tr_b16 v[170:171], v241 offset:45568\n\t"
    "v_mfma_f32_16x16x32_bf16 v[4:7], v[144:147], v[196:199], v[4:7]\n\t"
    "ds_read_b64_tr_b16 v[172:173], v241 offset:33280\n\t"
    "ds_read_b64_tr_b16 v[174:175], v241 offset:37376\n\t"
    "v_mfma_f32_16x16x32_bf16 v[8:11], v[144:147], v[200:203], 0\n\t"
    "s_add_u32 s58, s50, 16384\n\t"
    "v_mfma_f32_16x16x32_bf16 v[8:11], v[148:151], v[212:215], v[8:11]\n\t"
    "s_waitcnt lgkmcnt(10)\n\t"
    "v_mfma_f32_16x16x32_bf16 v[20:23], v[152:155], v[212:215], 0\n\t"
    "s_waitcnt lgkmcnt(8)\n\t"
    "v_mfma_f32_16x16x32_bf16 v[20:23], v[156:159], v[200:203], v[20:23]\n\t"
    "s_add_u32 m0, s52, 49152\n\t"
    "v_mfma_f32_16x16x32_bf16 v[16:19], v[156:159], v[196:199], 0\n\t"
    "ds_read_b64_tr_b16 v[176:177], v240 offset:33792\n\t"
    "ds_read_b64_tr_b16 v[178:179], v240 offset:37888\n\t"
    "v_mfma_f32_16x16x32_bf16 v[16:19], v[152:155], v[208:211], v[16:19]\n\t"
    "ds_read_b64_tr_b16 v[144:145], v240 offset:41984\n\t"
    "ds_read_b64_tr_b16 v[146:147], v240 offset:46080\n\t"
    "v_mfma_f32_16x16x32_bf16 v[12:15], v[152:155], v[204:207], 0\n\t"
    "buffer_load_dwordx4 v244, s[40:43], s58 offen offset:0 nt lds\n\t"
    "v_mfma_f32_16x16x32_bf16 v[12:15], v[156:159], v[192:195], v[12:15]\n\t"
    "s_waitcnt lgkmcnt(10)\n\t"
    "v_mfma_f32_16x16x32_bf16 v[24:27], v[160:163], v[192:195], 0\n\t"
    "s_waitcnt lgkmcnt(8)\n\t"
    "v_mfma_f32_16x16x32_bf16 v[24:27], v[164:167], v[204:207], v[24:27]\n\t"
    "buffer_load_dwordx4 v244, s[40:43], s58 offen offset:2048 nt lds\n\t"
    "v_mfma_f32_16x16x32_bf16 v[28:31], v[164:167], v[208:211], 0\n\t"
    "ds_read_b64_tr_b16 v[148:149], v241 offset:41984\n\t"
    "ds_read_b64_tr_b16 v[150:151], v241 offset:46080\n\t"
    "v_mfma_f32_16x16x32_bf16 v[28:31], v[160:163], v[196:199], v[28:31]\n\t"
    "ds_read_b64_tr_b16 v[152:153], v241 offset:33792\n\t"
    "ds_read_b64_tr_b16 v[154:155], v241 offset:37888\n\t"
    "v_mfma_f32_16x16x32_bf16 v[32:35], v[160:163], v[200:203], 0\n\t"
    "s_add_u32 m0, s52, 50048\n\t"
    "v_mfma_f32_16x16x32_bf16 v[32:35], v[164:167], v[212:215], v[32:35]\n\t"
    "s_waitcnt lgkmcnt(10)\n\t"
    "v_mfma_f32_16x16x32_bf16 v[44:47], v[168:171], v[212:215], 0\n\t"
    "s_waitcnt lgkmcnt(8)\n\t"
    "v_mfma_f32_16x16x32_bf16 v[44:47], v[172:175], v[200:203], v[44:47]\n\t"
    "buffer_load_dwordx4 v244, s[40:43], s58 offen offset:128 nt lds\n\t"
    "v_mfma_f32_16x16x32_bf16 v[40:43], v[172:175], v[196:199], 0\n\t"
    "ds_read_b64_tr_b16 v[156:157], v240 offset:34304\n\t"
    "ds_read_b64_tr_b16 v[158:159], v240 offset:38400\n\t"
    "v_mfma_f32_16x16x32_bf16 v[40:43], v[168:171], v[208:211], v[40:43]\n\t"
    "ds_read_b64_tr_b16 v[160:161], v240 offset:42496\n\t"
    "ds_read_b64_tr_b16 v[162:163], v240 offset:46592\n\t"
    "v_mfma_f32_16x16x32_bf16 v[36:39], v[168:171], v[204:207], 0\n\t"
    "buffer_load_dwordx4 v244, s[40:43], s58 offen offset:2176 nt lds\n\t"
    "v_mfma_f32_16x16x32_bf16 v[36:39], v[172:175], v[192:195], v[36:39]\n\t"
    "s_waitcnt lgkmcnt(10)\n\t"
    "v_mfma_f32_16x16x32_bf16 v[48:51], v[176:179], v[192:195], 0\n\t"
    "s_waitcnt lgkmcnt(8)\n\t"
    "v_mfma_f32_16x16x32_bf16 v[48:51], v[144:147], v[204:207], v[48:51]\n\t"
    "s_add_u32 s57, s50, 32768\n\t"
    "v_mfma_f32_16x16x32_bf16 v[52:55], v[144:147], v[208:211], 0\n\t"
    "ds_read_b64_tr_b16 v[164:165], v241 offset:42496\n\t"
    "ds_read_b64_tr_b16 v[166:167], v241 offset:46592\n\t"
    "v_mfma_f32_16x16x32_bf16 v[52:55], v[176:179], v[196:199], v[52:55]\n\t"
    "ds_read_b64_tr_b16 v[168:169], v241 offset:34304\n\t"
    "ds_read_b64_tr_b16 v[170:171], v241 offset:38400\n\t"
    "v_mfma_f32_16x16x32_bf16 v[56:59], v[176:179], v[200:203], 0\n\t"
    "s_add_u32 m0, s52, 0\n\t"
    "v_mfma_f32_16x16x32_bf16 v[56:59], v[144:147], v[212:215], v[56:59]\n\t"
    "s_waitcnt lgkmcnt(10)\n\t"
    "v_mfma_f32_16x16x32_bf16 v[68:71], v[148:151], v[212:215], 0\n\t"
    "s_waitcnt lgkmcnt(8)\n\t"
    "v_mfma_f32_16x16x32_bf16 v[68:71], v[152:155], v[200:203], v[68:71]\n\t"
    "buffer_load_dwordx4 v242, s[36:39], s57 offen offset:0 nt lds\n\t"
    "v_mfma_f32_16x16x32_bf16 v[64:67], v[152:155], v[196:199], 0\n\t"
    "ds_read_b128 v[180:183], v238 offset:16384\n\t"
    "v_mfma_f32_16x16x32_bf16 v[64:67], v[148:151], v[208:211], v[64:67]\n\t"
    "ds_read_b128 v[184:187], v239 offset:16384\n\t"
    "v_mfma_f32_16x16x32_bf16 v[60:63], v[148:151], v[204:207], 0\n\t"
    "ds_read_b128 v[188:191], v238 offset:17408\n\t"
    "v_mfma_f32_16x16x32_bf16 v[60:63], v[152:155], v[192:195], v[60:63]\n\t"
    "ds_read_b128 v[216:219], v239 offset:17408\n\t"
    "s_waitcnt lgkmcnt(10)\n\t"
    "v_mfma_f32_16x16x32_bf16 v[72:75], v[156:159], v[192:195], 0\n\t"
    "s_waitcnt lgkmcnt(8)\n\t"
    "v_mfma_f32_16x16x32_bf16 v[72:75], v[160:163], v[204:207], v[72:75]\n\t"
    "buffer_load_dwordx4 v243, s[36:39], s57 offen offset:2048 nt lds\n\t"
    "v_mfma_f32_16x16x32_bf16 v[76:79], v[160:163], v[208:211], 0\n\t"
    "v_mfma_f32_16x16x32_bf16 v[76:79], v[156:159], v[196:199], v[76:79]\n\t"
    "s_add_u32 m0, s52, 896\n\t"
    "v_mfma_f32_16x16x32_bf16 v[80:83], v[156:159], v[200:203], 0\n\t"
    "buffer_load_dwordx4 v242, s[36:39], s57 offen offset:128 nt lds\n\t"
    "v_mfma_f32_16x16x32_bf16 v[80:83], v[160:163], v[212:215], v[80:83]\n\t"
    "s_waitcnt lgkmcnt(6)\n\t"
    "v_mfma_f32_16x16x32_bf16 v[92:95], v[164:167], v[212:215], 0\n\t"
    "s_waitcnt lgkmcnt(4)\n\t"
    "v_mfma_f32_16x16x32_bf16 v[92:95], v[168:171], v[200:203], v[92:95]\n\t"
    "buffer_load_dwordx4 v243, s[36:39], s57 offen offset:2176 nt lds\n\t"
    "v_mfma_f32_16x16x32_bf16 v[88:91], v[168:171], v[196:199], 0\n\t"
    "v_mfma_f32_16x16x32_bf16 v[88:91], v[164:167], v[208:211], v[88:91]\n\t"
    "v_mfma_f32_16x16x32_bf16 v[84:87], v[164:167], v[204:207], 0\n\t"
    "v_mfma_f32_16x16x32_bf16 v[84:87], v[168:171], v[192:195], v[84:87]\n\t"
    "ds_read_b128 v[204:207], v239 offset:21504\n\t"
    "ds_read_b128 v[200:203], v238 offset:21504\n\t"
    "s_waitcnt lgkmcnt(5)\n\t"
    "v_mfma_f32_16x16x32_bf16 v[144:147], v[180:183], v[96:99], 0\n\t"
    "ds_read_b128 v[196:199], v239 offset:20480\n\t"
    "ds_read_b128 v[192:195], v238 offset:20480\n\t"
    "s_waitcnt lgkmcnt(6)\n\t"
    "v_mfma_f32_16x16x32_bf16 v[144:147], v[184:187], v[100:103], v[144:147]\n\t"
    "s_waitcnt lgkmcnt(5)\n\t"
    "v_mfma_f32_16x16x32_bf16 v[144:147], v[188:191], v[104:107], v[144:147]\n\t"
    "s_waitcnt lgkmcnt(4)\n\t"
    "v_mfma_f32_16x16x32_bf16 v[144:147], v[216:219], v[108:111], v[144:147]\n\t"
    "v_mfma_f32_16x16x32_bf16 v[148:151], v[216:219], v[124:127], 0\n\t"
    "v_mfma_f32_16x16x32_bf16 v[148:151], v[188:191], v[120:123], v[148:151]\n\t"
    "v_mfma_f32_16x16x32_bf16 v[148:151], v[184:187], v[116:119], v[148:151]\n\t"
    "v_mfma_f32_16x16x32_bf16 v[148:151], v[180:183], v[112:115], v[148:151]\n\t"
    "v_mfma_f32_16x16x32_bf16 v[152:155], v[180:183], v[128:131], 0\n\t"
    "ds_read_b128 v[180:183], v238 offset:24576\n\t"
    "v_mfma_f32_16x16x32_bf16 v[152:155], v[184:187], v[132:135], v[152:155]\n\t"
    "ds_read_b128 v[184:187], v239 offset:24576\n\t"
    "v_mfma_f32_16x16x32_bf16 v[152:155], v[188:191], v[136:139], v[152:155]\n\t"
    "ds_read_b128 v[188:191], v238 offset:25600\n\t"
    "v_mfma_f32_16x16x32_bf16 v[152:155], v[216:219], v[140:143], v[152:155]\n\t"
    "ds_read_b128 v[216:219], v239 offset:25600\n\t"
    "s_waitcnt lgkmcnt(7)\n\t"
    "v_mfma_f32_16x16x32_bf16 v[164:167], v[204:207], v[140:143], 0\n\t"
    "s_waitcnt lgkmcnt(6)\n\t"
    "v_mfma_f32_16x16x32_bf16 v[164:167], v[200:203], v[136:139], v[164:167]\n\t"
    "s_waitcnt lgkmcnt(5)\n\t"
    "v_mfma_f32_16x16x32_bf16 v[164:167], v[196:199], v[132:135], v[164:167]\n\t"
    "s_waitcnt lgkmcnt(4)\n\t"
    "v_mfma_f32_16x16x32_bf16 v[164:167], v[192:195], v[128:131], v[164:167]\n\t"
    "v_mfma_f32_16x16x32_bf16 v[160:163], v[192:195], v[112:115], 0\n\t"
    "v_mfma_f32_16x16x32_bf16 v[160:163], v[196:199], v[116:119], v[160:163]\n\t"
    "v_mfma_f32_16x16x32_bf16 v[160:163], v[200:203], v[120:123], v[160:163]\n\t"
    "v_mfma_f32_16x16x32_bf16 v[160:163], v[204:207], v[124:127], v[160:163]\n\t"
    "v_mfma_f32_16x16x32_bf16 v[156:159], v[204:207], v[108:111], 0\n\t"
    "ds_read_b128 v[204:207], v239 offset:29696\n\t"
    "v_mfma_f32_16x16x32_bf16 v[156:159], v[200:203], v[104:107], v[156:159]\n\t"
    "ds_read_b128 v[200:203], v238 offset:29696\n\t"
    "v_mfma_f32_16x16x32_bf16 v[156:159], v[196:199], v[100:103], v[156:159]\n\t"
    "ds_read_b128 v[196:199], v239 offset:28672\n\t"
    "v_mfma_f32_16x16x32_bf16 v[156:159], v[192:195], v[96:99], v[156:159]\n\t"
    "ds_read_b128 v[192:195], v238 offset:28672\n\t"
    "s_waitcnt lgkmcnt(7)\n\t"
    "v_mfma_f32_16x16x32_bf16 v[168:171], v[180:183], v[96:99], 0\n\t"
    "s_waitcnt lgkmcnt(6)\n\t"
    "v_mfma_f32_16x16x32_bf16 v[168:171], v[184:187], v[100:103], v[168:171]\n\t"
    "s_waitcnt lgkmcnt(5)\n\t"
    "v_mfma_f32_16x16x32_bf16 v[168:171], v[188:191], v[104:107], v[168:171]\n\t"
    "s_waitcnt lgkmcnt(4)\n\t"
    "v_mfma_f32_16x16x32_bf16 v[168:171], v[216:219], v[108:111], v[168:171]\n\t"
    "v_mfma_f32_16x16x32_bf16 v[172:175], v[216:219], v[124:127], 0\n\t"
    "v_mfma_f32_16x16x32_bf16 v[172:175], v[188:191], v[120:123], v[172:175]\n\t"
    "v_mfma_f32_16x16x32_bf16 v[172:175], v[184:187], v[116:119], v[172:175]\n\t"
    "v_mfma_f32_16x16x32_bf16 v[172:175], v[180:183], v[112:115], v[172:175]\n\t"
    "v_mfma_f32_16x16x32_bf16 v[176:179], v[180:183], v[128:131], 0\n\t"
    "v_mfma_f32_16x16x32_bf16 v[176:179], v[184:187], v[132:135], v[176:179]\n\t"
    "v_mfma_f32_16x16x32_bf16 v[176:179], v[188:191], v[136:139], v[176:179]\n\t"
    "v_mfma_f32_16x16x32_bf16 v[176:179], v[216:219], v[140:143], v[176:179]\n\t"
    "s_waitcnt lgkmcnt(3)\n\t"
    "v_mfma_f32_16x16x32_bf16 v[188:191], v[204:207], v[140:143], 0\n\t"
    "s_waitcnt lgkmcnt(2)\n\t"
    "v_mfma_f32_16x16x32_bf16 v[188:191], v[200:203], v[136:139], v[188:191]\n\t"
    "s_waitcnt lgkmcnt(1)\n\t"
    "v_mfma_f32_16x16x32_bf16 v[188:191], v[196:199], v[132:135], v[188:191]\n\t"
    "s_waitcnt lgkmcnt(0)\n\t"
    "v_mfma_f32_16x16x32_bf16 v[188:191], v[192:195], v[128:131], v[188:191]\n\t"
    "v_mfma_f32_16x16x32_bf16 v[184:187], v[192:195], v[112:115], 0\n\t"
    "v_mfma_f32_16x16x32_bf16 v[184:187], v[196:199], v[116:119], v[184:187]\n\t"
    "v_mfma_f32_16x16x32_bf16 v[184:187], v[200:203], v[120:123], v[184:187]\n\t"
    "v_mfma_f32_16x16x32_bf16 v[184:187], v[204:207], v[124:127], v[184:187]\n\t"
    "v_mfma_f32_16x16x32_bf16 v[180:183], v[204:207], v[108:111], 0\n\t"
    "v_mfma_f32_16x16x32_bf16 v[180:183], v[200:203], v[104:107], v[180:183]\n\t"
    "v_mfma_f32_16x16x32_bf16 v[180:183], v[196:199], v[100:103], v[180:183]\n\t"
    "v_mfma_f32_16x16x32_bf16 v[180:183], v[192:195], v[96:99], v[180:183]\n\t"
    "s_waitcnt lgkmcnt(0)\n\t"
    "s_barrier\n\t"
    "s_setprio 0\n\t"
    "v_fma_f32 v220, v144, s48, v232\n\t"
    "v_fma_f32 v221, v145, s48, v232\n\t"
    "v_fma_f32 v222, v146, s48, v232\n\t"
    "v_fma_f32 v223, v147, s48, v232\n\t"
    "v_exp_f32 v220, v220\n\t"
    "v_exp_f32 v221, v221\n\t"
    "v_exp_f32 v222, v222\n\t"
    "v_exp_f32 v223, v223\n\t"
    "v_fma_f32 v224, v148, s48, v233\n\t"
    "v_fma_f32 v225, v149, s48, v233\n\t"
    "v_fma_f32 v226, v150, s48, v233\n\t"
    "v_fma_f32 v227, v151, s48, v233\n\t"
    "v_exp_f32 v224, v224\n\t"
    "v_exp_f32 v225, v225\n\t"
    "v_exp_f32 v226, v226\n\t"
    "v_exp_f32 v227, v227\n\t"
    "v_cvt_pk_bf16_f32 v192, v220, v221\n\t"
    "v_cvt_pk_bf16_f32 v193, v222, v223\n\t"
    "v_add_f32 v228, v220, v221\n\t"
    "v_add_f32 v228, v228, v222\n\t"
    "v_add_f32 v228, v228, v223\n\t"
    "v_fma_f32 v220, v152, s48, v234\n\t"
    "v_fma_f32 v221, v153, s48, v234\n\t"
    "v_fma_f32 v222, v154, s48, v234\n\t"
    "v_fma_f32 v223, v155, s48, v234\n\t"
    "v_exp_f32 v220, v220\n\t"
    "v_exp_f32 v221, v221\n\t"
    "v_exp_f32 v222, v222\n\t"
    "v_exp_f32 v223, v223\n\t"
    "v_cvt_pk_bf16_f32 v196, v224, v225\n\t"
    "v_cvt_pk_bf16_f32 v197, v226, v227\n\t"
    "v_add_f32 v229, v224, v225\n\t"
    "v_add_f32 v229, v229, v226\n\t"
    "v_add_f32 v229, v229, v227\n\t"
    "v_fma_f32 v224, v156, s48, v232\n\t"
    "v_fma_f32 v225, v157, s48, v232\n\t"
    "v_fma_f32 v226, v158, s48, v232\n\t"
    "v_fma_f32 v227, v159, s48, v232\n\t"
    "v_exp_f32 v224, v224\n\t"
    "v_exp_f32 v225, v225\n\t"
    "v_exp_f32 v226, v226\n\t"
    "v_exp_f32 v227, v227\n\t"
    "v_cvt_pk_bf16_f32 v200, v220, v221\n\t"
    "v_cvt_pk_bf16_f32 v201, v222, v223\n\t"
    "v_add_f32 v230, v220, v221\n\t"
    "v_add_f32 v230, v230, v222\n\t"
    "v_add_f32 v230, v230, v223\n\t"
    "v_fma_f32 v220, v160, s48, v233\n\t"
    "v_fma_f32 v221, v161, s48, v233\n\t"
    "v_fma_f32 v222, v162, s48, v233\n\t"
    "v_fma_f32 v223, v163, s48, v233\n\t"
    "v_exp_f32 v220, v220\n\t"
    "v_exp_f32 v221, v221\n\t"
    "v_exp_f32 v222, v222\n\t"
    "v_exp_f32 v223, v223\n\t"
    "v_cvt_pk_bf16_f32 v194, v224, v225\n\t"
    "v_cvt_pk_bf16_f32 v195, v226, v227\n\t"
    "v_add_f32 v228, v228, v224\n\t"
    "v_add_f32 v228, v228, v225\n\t"
    "v_add_f32 v228, v228, v226\n\t"
    "v_add_f32 v228, v228, v227\n\t"
    "v_fma_f32 v224, v164, s48, v234\n\t"
    "v_fma_f32 v225, v165, s48, v234\n\t"
    "v_fma_f32 v226, v166, s48, v234\n\t"
    "v_fma_f32 v227, v167, s48, v234\n\t"
    "v_exp_f32 v224, v224\n\t"
    "v_exp_f32 v225, v225\n\t"
    "v_exp_f32 v226, v226\n\t"
    "v_exp_f32 v227, v227\n\t"
    "v_cvt_pk_bf16_f32 v198, v220, v221\n\t"
    "v_cvt_pk_bf16_f32 v199, v222, v223\n\t"
    "v_add_f32 v229, v229, v220\n\t"
    "v_add_f32 v229, v229, v221\n\t"
    "v_add_f32 v229, v229, v222\n\t"
    "v_add_f32 v229, v229, v223\n\t"
    "v_fma_f32 v220, v168, s48, v232\n\t"
    "v_fma_f32 v221, v169, s48, v232\n\t"
    "v_fma_f32 v222, v170, s48, v232\n\t"
    "v_fma_f32 v223, v171, s48, v232\n\t"
    "v_exp_f32 v220, v220\n\t"
    "v_exp_f32 v221, v221\n\t"
    "v_exp_f32 v222, v222\n\t"
    "v_exp_f32 v223, v223\n\t"
    "v_cvt_pk_bf16_f32 v202, v224, v225\n\t"
    "v_cvt_pk_bf16_f32 v203, v226, v227\n\t"
    "v_add_f32 v230, v230, v224\n\t"
    "v_add_f32 v230, v230, v225\n\t"
    "v_add_f32 v230, v230, v226\n\t"
    "v_add_f32 v230, v230, v227\n\t"
    "v_fma_f32 v224, v172, s48, v233\n\t"
    "v_fma_f32 v225, v173, s48, v233\n\t"
    "v_fma_f32 v226, v174, s48, v233\n\t"
    "v_fma_f32 v227, v175, s48, v233\n\t"
    "v_exp_f32 v224, v224\n\t"
    "v_exp_f32 v225, v225\n\t"
    "v_exp_f32 v226, v226\n\t"
    "v_exp_f32 v227, v227\n\t"
    "v_cvt_pk_bf16_f32 v204, v220, v221\n\t"
    "v_cvt_pk_bf16_f32 v205, v222, v223\n\t"
    "v_add_f32 v228, v228, v220\n\t"
    "v_add_f32 v228, v228, v221\n\t"
    "v_add_f32 v228, v228, v222\n\t"
    "v_add_f32 v228, v228, v223\n\t"
    "v_fma_f32 v220, v176, s48, v234\n\t"
    "v_fma_f32 v221, v177, s48, v234\n\t"
    "v_fma_f32 v222, v178, s48, v234\n\t"
    "v_fma_f32 v223, v179, s48, v234\n\t"
    "v_exp_f32 v220, v220\n\t"
    "v_exp_f32 v221, v221\n\t"
    "v_exp_f32 v222, v222\n\t"
    "v_exp_f32 v223, v223\n\t"
    "v_cvt_pk_bf16_f32 v208, v224, v225\n\t"
    "v_cvt_pk_bf16_f32 v209, v226, v227\n\t"
    "v_add_f32 v229, v229, v224\n\t"
    "v_add_f32 v229, v229, v225\n\t"
    "v_add_f32 v229, v229, v226\n\t"
    "v_add_f32 v229, v229, v227\n\t"
    "v_fma_f32 v224, v180, s48, v232\n\t"
    "v_fma_f32 v225, v181, s48, v232\n\t"
    "v_fma_f32 v226, v182, s48, v232\n\t"
    "v_fma_f32 v227, v183, s48, v232\n\t"
    "v_exp_f32 v224, v224\n\t"
    "v_exp_f32 v225, v225\n\t"
    "v_exp_f32 v226, v226\n\t"
    "v_exp_f32 v227, v227\n\t"
    "v_cvt_pk_bf16_f32 v212, v220, v221\n\t"
    "v_cvt_pk_bf16_f32 v213, v222, v223\n\t"
    "v_add_f32 v230, v230, v220\n\t"
    "v_add_f32 v230, v230, v221\n\t"
    "v_add_f32 v230, v230, v222\n\t"
    "v_add_f32 v230, v230, v223\n\t"
    "v_fma_f32 v220, v184, s48, v233\n\t"
    "v_fma_f32 v221, v185, s48, v233\n\t"
    "v_fma_f32 v222, v186, s48, v233\n\t"
    "v_fma_f32 v223, v187, s48, v233\n\t"
    "v_exp_f32 v220, v220\n\t"
    "v_exp_f32 v221, v221\n\t"
    "v_exp_f32 v222, v222\n\t"
    "v_exp_f32 v223, v223\n\t"
    "v_cvt_pk_bf16_f32 v206, v224, v225\n\t"
    "v_cvt_pk_bf16_f32 v207, v226, v227\n\t"
    "v_add_f32 v228, v228, v224\n\t"
    "v_add_f32 v228, v228, v225\n\t"
    "v_add_f32 v228, v228, v226\n\t"
    "v_add_f32 v228, v228, v227\n\t"
    "v_fma_f32 v224, v188, s48, v234\n\t"
    "v_fma_f32 v225, v189, s48, v234\n\t"
    "v_fma_f32 v226, v190, s48, v234\n\t"
    "v_fma_f32 v227, v191, s48, v234\n\t"
    "v_exp_f32 v224, v224\n\t"
    "v_exp_f32 v225, v225\n\t"
    "v_exp_f32 v226, v226\n\t"
    "v_exp_f32 v227, v227\n\t"
    "v_cvt_pk_bf16_f32 v210, v220, v221\n\t"
    "v_cvt_pk_bf16_f32 v211, v222, v223\n\t"
    "v_add_f32 v229, v229, v220\n\t"
    "v_add_f32 v229, v229, v221\n\t"
    "v_add_f32 v229, v229, v222\n\t"
    "v_add_f32 v229, v229, v223\n\t"
    "v_cvt_pk_bf16_f32 v214, v224, v225\n\t"
    "v_cvt_pk_bf16_f32 v215, v226, v227\n\t"
    "v_add_f32 v230, v230, v224\n\t"
    "v_add_f32 v230, v230, v225\n\t"
    "v_add_f32 v230, v230, v226\n\t"
    "v_add_f32 v230, v230, v227\n\t"
    "v_max3_f32 v220, v228, v229, v230\n\t"
    "v_cmp_lt_f32 vcc, s49, v220\n\t"
    "s_cbranch_vccnz LFBA1%=\n\t"
    "LRETA1%=:\n\t"
    "v_add_f32 v235, v235, v228\n\t"
    "v_add_f32 v236, v236, v229\n\t"
    "v_add_f32 v237, v237, v230\n\t"
    "s_waitcnt vmcnt(0)\n\t"
    "s_barrier\n\t"
    "s_setprio 1\n\t"
    "ds_read_b64_tr_b16 v[144:145], v240 offset:49152\n\t"
    "ds_read_b64_tr_b16 v[146:147], v240 offset:53248\n\t"
    "ds_read_b64_tr_b16 v[148:149], v240 offset:57344\n\t"
    "ds_read_b64_tr_b16 v[150:151], v240 offset:61440\n\t"
    "ds_read_b64_tr_b16 v[152:153], v241 offset:57344\n\t"
    "ds_read_b64_tr_b16 v[154:155], v241 offset:61440\n\t"
    "ds_read_b64_tr_b16 v[156:157], v241 offset:49152\n\t"
    "ds_read_b64_tr_b16 v[158:159], v241 offset:53248\n\t"
    "s_waitcnt lgkmcnt(6)\n\t"
    "v_mfma_f32_16x16x32_bf16 v[0:3], v[144:147], v[192:195], v[0:3]\n\t"
    "ds_read_b64_tr_b16 v[160:161], v240 offset:49664\n\t"
    "ds_read_b64_tr_b16 v[162:163], v240 offset:53760\n\t"
    "s_waitcnt lgkmcnt(6)\n\t"
    "v_mfma_f32_16x16x32_bf16 v[0:3], v[148:151], v[204:207], v[0:3]\n\t"
    "ds_read_b64_tr_b16 v[164:165], v240 offset:57856\n\t"
    "ds_read_b64_tr_b16 v[166:167], v240 offset:61952\n\t"
    "v_mfma_f32_16x16x32_bf16 v[4:7], v[148:151], v[208:211], v[4:7]\n\t"
    "ds_read_b64_tr_b16 v[168:169], v241 offset:57856\n\t"
    "ds_read_b64_tr_b16 v[170:171], v241 offset:61952\n\t"
    "v_mfma_f32_16x16x32_bf16 v[4:7], v[144:147], v[196:199], v[4:7]\n\t"
    "ds_read_b64_tr_b16 v[172:173], v241 offset:49664\n\t"
    "ds_read_b64_tr_b16 v[174:175], v241 offset:53760\n\t"
    "v_mfma_f32_16x16x32_bf16 v[8:11], v[144:147], v[200:203], v[8:11]\n\t"
    "s_add_u32 s58, s50, 32768\n\t"
    "v_mfma_f32_16x16x32_bf16 v[8:11], v[148:151], v[212:215], v[8:11]\n\t"
    "s_waitcnt lgkmcnt(10)\n\t"
    "v_mfma_f32_16x16x32_bf16 v[20:23], v[152:155], v[212:215], v[20:23]\n\t"
    "s_waitcnt lgkmcnt(8)\n\t"
    "v_mfma_f32_16x16x32_bf16 v[20:23], v[156:159], v[200:203], v[20:23]\n\t"
    "s_add_u32 m0, s52, 32768\n\t"
    "v_mfma_f32_16x16x32_bf16 v[16:19], v[156:159], v[196:199], v[16:19]\n\t"
    "ds_read_b64_tr_b16 v[176:177], v240 offset:50176\n\t"
    "ds_read_b64_tr_b16 v[178:179], v240 offset:54272\n\t"
    "v_mfma_f32_16x16x32_bf16 v[16:19], v[152:155], v[208:211], v[16:19]\n\t"
    "ds_read_b64_tr_b16 v[144:145], v240 offset:58368\n\t"
    "ds_read_b64_tr_b16 v[146:147], v240 offset:62464\n\t"
    "v_mfma_f32_16x16x32_bf16 v[12:15], v[152:155], v[204:207], v[12:15]\n\t"
    "buffer_load_dwordx4 v244, s[40:43], s58 offen offset:0 nt lds\n\t"
    "v_mfma_f32_16x16x32_bf16 v[12:15], v[156:159], v[192:195], v[12:15]\n\t"
    "s_waitcnt lgkmcnt(10)\n\t"
    "v_mfma_f32_16x16x32_bf16 v[24:27], v[160:163], v[192:195], v[24:27]\n\t"
    "s_waitcnt lgkmcnt(8)\n\t"
    "v_mfma_f32_16x16x32_bf16 v[24:27], v[164:167], v[204:207], v[24:27]\n\t"
    "buffer_load_dwordx4 v244, s[40:43], s58 offen offset:2048 nt lds\n\t"
    "v_mfma_f32_16x16x32_bf16 v[28:31], v[164:167], v[208:211], v[28:31]\n\t"
    "ds_read_b64_tr_b16 v[148:149], v241 offset:58368\n\t"
    "ds_read_b64_tr_b16 v[150:151], v241 offset:62464\n\t"
    "v_mfma_f32_16x16x32_bf16 v[28:31], v[160:163], v[196:199], v[28:31]\n\t"
    "ds_read_b64_tr_b16 v[152:153], v241 offset:50176\n\t"
    "ds_read_b64_tr_b16 v[154:155], v241 offset:54272\n\t"
    "v_mfma_f32_16x16x32_bf16 v[32:35], v[160:163], v[200:203], v[32:35]\n\t"
    "s_add_u32 m0, s52, 33664\n\t"
    "v_mfma_f32_16x16x32_bf16 v[32:35], v[164:167], v[212:215], v[32:35]\n\t"
    "s_waitcnt lgkmcnt(10)\n\t"
    "v_mfma_f32_16x16x32_bf16 v[44:47], v[168:171], v[212:215], v[44:47]\n\t"
    "s_waitcnt lgkmcnt(8)\n\t"
    "v_mfma_f32_16x16x32_bf16 v[44:47], v[172:175], v[200:203], v[44:47]\n\t"
    "buffer_load_dwordx4 v244, s[40:43], s58 offen offset:128 nt lds\n\t"
    "v_mfma_f32_16x16x32_bf16 v[40:43], v[172:175], v[196:199], v[40:43]\n\t"
    "ds_read_b64_tr_b16 v[156:157], v240 offset:50688\n\t"
    "ds_read_b64_tr_b16 v[158:159], v240 offset:54784\n\t"
    "v_mfma_f32_16x16x32_bf16 v[40:43], v[168:171], v[208:211], v[40:43]\n\t"
    "ds_read_b64_tr_b16 v[160:161], v240 offset:58880\n\t"
    "ds_read_b64_tr_b16 v[162:163], v240 offset:62976\n\t"
    "v_mfma_f32_16x16x32_bf16 v[36:39], v[168:171], v[204:207], v[36:39]\n\t"
    "buffer_load_dwordx4 v244, s[40:43], s58 offen offset:2176 nt lds\n\t"
    "v_mfma_f32_16x16x32_bf16 v[36:39], v[172:175], v[192:195], v[36:39]\n\t"
    "s_waitcnt lgkmcnt(10)\n\t"
    "v_mfma_f32_16x16x32_bf16 v[48:51], v[176:179], v[192:195], v[48:51]\n\t"
    "s_waitcnt lgkmcnt(8)\n\t"
    "v_mfma_f32_16x16x32_bf16 v[48:51], v[144:147], v[204:207], v[48:51]\n\t"
    "s_add_u32 s57, s50, 49152\n\t"
    "v_mfma_f32_16x16x32_bf16 v[52:55], v[144:147], v[208:211], v[52:55]\n\t"
    "ds_read_b64_tr_b16 v[164:165], v241 offset:58880\n\t"
    "ds_read_b64_tr_b16 v[166:167], v241 offset:62976\n\t"
    "v_mfma_f32_16x16x32_bf16 v[52:55], v[176:179], v[196:199], v[52:55]\n\t"
    "ds_read_b64_tr_b16 v[168:169], v241 offset:50688\n\t"
    "ds_read_b64_tr_b16 v[170:171], v241 offset:54784\n\t"
    "v_mfma_f32_16x16x32_bf16 v[56:59], v[176:179], v[200:203], v[56:59]\n\t"
    "s_add_u32 m0, s52, 16384\n\t"
    "v_mfma_f32_16x16x32_bf16 v[56:59], v[144:147], v[212:215], v[56:59]\n\t"
    "s_waitcnt lgkmcnt(10)\n\t"
    "v_mfma_f32_16x16x32_bf16 v[68:71], v[148:151], v[212:215], v[68:71]\n\t"
    "s_waitcnt lgkmcnt(8)\n\t"
    "v_mfma_f32_16x16x32_bf16 v[68:71], v[152:155], v[200:203], v[68:71]\n\t"
    "buffer_load_dwordx4 v242, s[36:39], s57 offen offset:0 nt lds\n\t"
    "v_mfma_f32_16x16x32_bf16 v[64:67], v[152:155], v[196:199], v[64:67]\n\t"
    "ds_read_b128 v[180:183], v238 offset:0\n\t"
    "v_mfma_f32_16x16x32_bf16 v[64:67], v[148:151], v[208:211], v[64:67]\n\t"
    "ds_read_b128 v[184:187], v239 offset:0\n\t"
    "v_mfma_f32_16x16x32_bf16 v[60:63], v[148:151], v[204:207], v[60:63]\n\t"
    "ds_read_b128 v[188:191], v238 offset:1024\n\t"
    "v_mfma_f32_16x16x32_bf16 v[60:63], v[152:155], v[192:195], v[60:63]\n\t"
    "ds_read_b128 v[216:219], v239 offset:1024\n\t"
    "s_waitcnt lgkmcnt(10)\n\t"
    "v_mfma_f32_16x16x32_bf16 v[72:75], v[156:159], v[192:195], v[72:75]\n\t"
    "s_waitcnt lgkmcnt(8)\n\t"
    "v_mfma_f32_16x16x32_bf16 v[72:75], v[160:163], v[204:207], v[72:75]\n\t"
    "buffer_load_dwordx4 v243, s[36:39], s57 offen offset:2048 nt lds\n\t"
    "v_mfma_f32_16x16x32_bf16 v[76:79], v[160:163], v[208:211], v[76:79]\n\t"
    "v_mfma_f32_16x16x32_bf16 v[76:79], v[156:159], v[196:199], v[76:79]\n\t"
    "s_add_u32 m0, s52, 17280\n\t"
    "v_mfma_f32_16x16x32_bf16 v[80:83], v[156:159], v[200:203], v[80:83]\n\t"
    "buffer_load_dwordx4 v242, s[36:39], s57 offen offset:128 nt lds\n\t"
    "v_mfma_f32_16x16x32_bf16 v[80:83], v[160:163], v[212:215], v[80:83]\n\t"
    "s_waitcnt lgkmcnt(6)\n\t"
    "v_mfma_f32_16x16x32_bf16 v[92:95], v[164:167], v[212:215], v[92:95]\n\t"
    "s_waitcnt lgkmcnt(4)\n\t"
    "v_mfma_f32_16x16x32_bf16 v[92:95], v[168:171], v[200:203], v[92:95]\n\t"
    "buffer_load_dwordx4 v243, s[36:39], s57 offen offset:2176 nt lds\n\t"
    "v_mfma_f32_16x16x32_bf16 v[88:91], v[168:171], v[196:199], v[88:91]\n\t"
    "v_mfma_f32_16x16x32_bf16 v[88:91], v[164:167], v[208:211], v[88:91]\n\t"
    "v_mfma_f32_16x16x32_bf16 v[84:87], v[164:167], v[204:207], v[84:87]\n\t"
    "v_mfma_f32_16x16x32_bf16 v[84:87], v[168:171], v[192:195], v[84:87]\n\t"
    "ds_read_b128 v[204:207], v239 offset:5120\n\t"
    "ds_read_b128 v[200:203], v238 offset:5120\n\t"
    "s_waitcnt lgkmcnt(5)\n\t"
    "v_mfma_f32_16x16x32_bf16 v[144:147], v[180:183], v[96:99], 0\n\t"
    "ds_read_b128 v[196:199], v239 offset:4096\n\t"
    "ds_read_b128 v[192:195], v238 offset:4096\n\t"
    "s_waitcnt lgkmcnt(6)\n\t"
    "v_mfma_f32_16x16x32_bf16 v[144:147], v[184:187], v[100:103], v[144:147]\n\t"
    "s_waitcnt lgkmcnt(5)\n\t"
    "v_mfma_f32_16x16x32_bf16 v[144:147], v[188:191], v[104:107], v[144:147]\n\t"
    "s_waitcnt lgkmcnt(4)\n\t"
    "v_mfma_f32_16x16x32_bf16 v[144:147], v[216:219], v[108:111], v[144:147]\n\t"
    "v_mfma_f32_16x16x32_bf16 v[148:151], v[216:219], v[124:127], 0\n\t"
    "v_mfma_f32_16x16x32_bf16 v[148:151], v[188:191], v[120:123], v[148:151]\n\t"
    "v_mfma_f32_16x16x32_bf16 v[148:151], v[184:187], v[116:119], v[148:151]\n\t"
    "v_mfma_f32_16x16x32_bf16 v[148:151], v[180:183], v[112:115], v[148:151]\n\t"
    "v_mfma_f32_16x16x32_bf16 v[152:155], v[180:183], v[128:131], 0\n\t"
    "ds_read_b128 v[180:183], v238 offset:8192\n\t"
    "v_mfma_f32_16x16x32_bf16 v[152:155], v[184:187], v[132:135], v[152:155]\n\t"
    "ds_read_b128 v[184:187], v239 offset:8192\n\t"
    "v_mfma_f32_16x16x32_bf16 v[152:155], v[188:191], v[136:139], v[152:155]\n\t"
    "ds_read_b128 v[188:191], v238 offset:9216\n\t"
    "v_mfma_f32_16x16x32_bf16 v[152:155], v[216:219], v[140:143], v[152:155]\n\t"
    "ds_read_b128 v[216:219], v239 offset:9216\n\t"
    "s_waitcnt lgkmcnt(7)\n\t"
    "v_mfma_f32_16x16x32_bf16 v[164:167], v[204:207], v[140:143], 0\n\t"
    "s_waitcnt lgkmcnt(6)\n\t"
    "v_mfma_f32_16x16x32_bf16 v[164:167], v[200:203], v[136:139], v[164:167]\n\t"
    "s_waitcnt lgkmcnt(5)\n\t"
    "v_mfma_f32_16x16x32_bf16 v[164:167], v[196:199], v[132:135], v[164:167]\n\t"
    "s_waitcnt lgkmcnt(4)\n\t"
    "v_mfma_f32_16x16x32_bf16 v[164:167], v[192:195], v[128:131], v[164:167]\n\t"
    "v_mfma_f32_16x16x32_bf16 v[160:163], v[192:195], v[112:115], 0\n\t"
    "v_mfma_f32_16x16x32_bf16 v[160:163], v[196:199], v[116:119], v[160:163]\n\t"
    "v_mfma_f32_16x16x32_bf16 v[160:163], v[200:203], v[120:123], v[160:163]\n\t"
    "v_mfma_f32_16x16x32_bf16 v[160:163], v[204:207], v[124:127], v[160:163]\n\t"
    "v_mfma_f32_16x16x32_bf16 v[156:159], v[204:207], v[108:111], 0\n\t"
    "ds_read_b128 v[204:207], v239 offset:13312\n\t"
    "v_mfma_f32_16x16x32_bf16 v[156:159], v[200:203], v[104:107], v[156:159]\n\t"
    "ds_read_b128 v[200:203], v238 offset:13312\n\t"
    "v_mfma_f32_16x16x32_bf16 v[156:159], v[196:199], v[100:103], v[156:159]\n\t"
    "ds_read_b128 v[196:199], v239 offset:12288\n\t"
    "v_mfma_f32_16x16x32_bf16 v[156:159], v[192:195], v[96:99], v[156:159]\n\t"
    "ds_read_b128 v[192:195], v238 offset:12288\n\t"
    "s_waitcnt lgkmcnt(7)\n\t"
    "v_mfma_f32_16x16x32_bf16 v[168:171], v[180:183], v[96:99], 0\n\t"
    "s_waitcnt lgkmcnt(6)\n\t"
    "v_mfma_f32_16x16x32_bf16 v[168:171], v[184:187], v[100:103], v[168:171]\n\t"
    "s_waitcnt lgkmcnt(5)\n\t"
    "v_mfma_f32_16x16x32_bf16 v[168:171], v[188:191], v[104:107], v[168:171]\n\t"
    "s_waitcnt lgkmcnt(4)\n\t"
    "v_mfma_f32_16x16x32_bf16 v[168:171], v[216:219], v[108:111], v[168:171]\n\t"
    "v_mfma_f32_16x16x32_bf16 v[172:175], v[216:219], v[124:127], 0\n\t"
    "v_mfma_f32_16x16x32_bf16 v[172:175], v[188:191], v[120:123], v[172:175]\n\t"
    "v_mfma_f32_16x16x32_bf16 v[172:175], v[184:187], v[116:119], v[172:175]\n\t"
    "v_mfma_f32_16x16x32_bf16 v[172:175], v[180:183], v[112:115], v[172:175]\n\t"
    "v_mfma_f32_16x16x32_bf16 v[176:179], v[180:183], v[128:131], 0\n\t"
    "v_mfma_f32_16x16x32_bf16 v[176:179], v[184:187], v[132:135], v[176:179]\n\t"
    "v_mfma_f32_16x16x32_bf16 v[176:179], v[188:191], v[136:139], v[176:179]\n\t"
    "v_mfma_f32_16x16x32_bf16 v[176:179], v[216:219], v[140:143], v[176:179]\n\t"
    "s_waitcnt lgkmcnt(3)\n\t"
    "v_mfma_f32_16x16x32_bf16 v[188:191], v[204:207], v[140:143], 0\n\t"
    "s_waitcnt lgkmcnt(2)\n\t"
    "v_mfma_f32_16x16x32_bf16 v[188:191], v[200:203], v[136:139], v[188:191]\n\t"
    "s_waitcnt lgkmcnt(1)\n\t"
    "v_mfma_f32_16x16x32_bf16 v[188:191], v[196:199], v[132:135], v[188:191]\n\t"
    "s_waitcnt lgkmcnt(0)\n\t"
    "v_mfma_f32_16x16x32_bf16 v[188:191], v[192:195], v[128:131], v[188:191]\n\t"
    "v_mfma_f32_16x16x32_bf16 v[184:187], v[192:195], v[112:115], 0\n\t"
    "v_mfma_f32_16x16x32_bf16 v[184:187], v[196:199], v[116:119], v[184:187]\n\t"
    "v_mfma_f32_16x16x32_bf16 v[184:187], v[200:203], v[120:123], v[184:187]\n\t"
    "v_mfma_f32_16x16x32_bf16 v[184:187], v[204:207], v[124:127], v[184:187]\n\t"
    "v_mfma_f32_16x16x32_bf16 v[180:183], v[204:207], v[108:111], 0\n\t"
    "v_mfma_f32_16x16x32_bf16 v[180:183], v[200:203], v[104:107], v[180:183]\n\t"
    "v_mfma_f32_16x16x32_bf16 v[180:183], v[196:199], v[100:103], v[180:183]\n\t"
    "v_mfma_f32_16x16x32_bf16 v[180:183], v[192:195], v[96:99], v[180:183]\n\t"
    "s_waitcnt lgkmcnt(0)\n\t"
    "s_barrier\n\t"
    "s_setprio 0\n\t"
    "v_fma_f32 v220, v144, s48, v232\n\t"
    "v_fma_f32 v221, v145, s48, v232\n\t"
    "v_fma_f32 v222, v146, s48, v232\n\t"
    "v_fma_f32 v223, v147, s48, v232\n\t"
    "v_exp_f32 v220, v220\n\t"
    "v_exp_f32 v221, v221\n\t"
    "v_exp_f32 v222, v222\n\t"
    "v_exp_f32 v223, v223\n\t"
    "v_fma_f32 v224, v148, s48, v233\n\t"
    "v_fma_f32 v225, v149, s48, v233\n\t"
    "v_fma_f32 v226, v150, s48, v233\n\t"
    "v_fma_f32 v227, v151, s48, v233\n\t"
    "v_exp_f32 v224, v224\n\t"
    "v_exp_f32 v225, v225\n\t"
    "v_exp_f32 v226, v226\n\t"
    "v_exp_f32 v227, v227\n\t"
    "v_cvt_pk_bf16_f32 v192, v220, v221\n\t"
    "v_cvt_pk_bf16_f32 v193, v222, v223\n\t"
    "v_add_f32 v228, v220, v221\n\t"
    "v_add_f32 v228, v228, v222\n\t"
    "v_add_f32 v228, v228, v223\n\t"
    "v_fma_f32 v220, v152, s48, v234\n\t"
    "v_fma_f32 v221, v153, s48, v234\n\t"
    "v_fma_f32 v222, v154, s48, v234\n\t"
    "v_fma_f32 v223, v155, s48, v234\n\t"
    "v_exp_f32 v220, v220\n\t"
    "v_exp_f32 v221, v221\n\t"
    "v_exp_f32 v222, v222\n\t"
    "v_exp_f32 v223, v223\n\t"
    "v_cvt_pk_bf16_f32 v196, v224, v225\n\t"
    "v_cvt_pk_bf16_f32 v197, v226, v227\n\t"
    "v_add_f32 v229, v224, v225\n\t"
    "v_add_f32 v229, v229, v226\n\t"
    "v_add_f32 v229, v229, v227\n\t"
    "v_fma_f32 v224, v156, s48, v232\n\t"
    "v_fma_f32 v225, v157, s48, v232\n\t"
    "v_fma_f32 v226, v158, s48, v232\n\t"
    "v_fma_f32 v227, v159, s48, v232\n\t"
    "v_exp_f32 v224, v224\n\t"
    "v_exp_f32 v225, v225\n\t"
    "v_exp_f32 v226, v226\n\t"
    "v_exp_f32 v227, v227\n\t"
    "v_cvt_pk_bf16_f32 v200, v220, v221\n\t"
    "v_cvt_pk_bf16_f32 v201, v222, v223\n\t"
    "v_add_f32 v230, v220, v221\n\t"
    "v_add_f32 v230, v230, v222\n\t"
    "v_add_f32 v230, v230, v223\n\t"
    "v_fma_f32 v220, v160, s48, v233\n\t"
    "v_fma_f32 v221, v161, s48, v233\n\t"
    "v_fma_f32 v222, v162, s48, v233\n\t"
    "v_fma_f32 v223, v163, s48, v233\n\t"
    "v_exp_f32 v220, v220\n\t"
    "v_exp_f32 v221, v221\n\t"
    "v_exp_f32 v222, v222\n\t"
    "v_exp_f32 v223, v223\n\t"
    "v_cvt_pk_bf16_f32 v194, v224, v225\n\t"
    "v_cvt_pk_bf16_f32 v195, v226, v227\n\t"
    "v_add_f32 v228, v228, v224\n\t"
    "v_add_f32 v228, v228, v225\n\t"
    "v_add_f32 v228, v228, v226\n\t"
    "v_add_f32 v228, v228, v227\n\t"
    "v_fma_f32 v224, v164, s48, v234\n\t"
    "v_fma_f32 v225, v165, s48, v234\n\t"
    "v_fma_f32 v226, v166, s48, v234\n\t"
    "v_fma_f32 v227, v167, s48, v234\n\t"
    "v_exp_f32 v224, v224\n\t"
    "v_exp_f32 v225, v225\n\t"
    "v_exp_f32 v226, v226\n\t"
    "v_exp_f32 v227, v227\n\t"
    "v_cvt_pk_bf16_f32 v198, v220, v221\n\t"
    "v_cvt_pk_bf16_f32 v199, v222, v223\n\t"
    "v_add_f32 v229, v229, v220\n\t"
    "v_add_f32 v229, v229, v221\n\t"
    "v_add_f32 v229, v229, v222\n\t"
    "v_add_f32 v229, v229, v223\n\t"
    "v_fma_f32 v220, v168, s48, v232\n\t"
    "v_fma_f32 v221, v169, s48, v232\n\t"
    "v_fma_f32 v222, v170, s48, v232\n\t"
    "v_fma_f32 v223, v171, s48, v232\n\t"
    "v_exp_f32 v220, v220\n\t"
    "v_exp_f32 v221, v221\n\t"
    "v_exp_f32 v222, v222\n\t"
    "v_exp_f32 v223, v223\n\t"
    "v_cvt_pk_bf16_f32 v202, v224, v225\n\t"
    "v_cvt_pk_bf16_f32 v203, v226, v227\n\t"
    "v_add_f32 v230, v230, v224\n\t"
    "v_add_f32 v230, v230, v225\n\t"
    "v_add_f32 v230, v230, v226\n\t"
    "v_add_f32 v230, v230, v227\n\t"
    "v_fma_f32 v224, v172, s48, v233\n\t"
    "v_fma_f32 v225, v173, s48, v233\n\t"
    "v_fma_f32 v226, v174, s48, v233\n\t"
    "v_fma_f32 v227, v175, s48, v233\n\t"
    "v_exp_f32 v224, v224\n\t"
    "v_exp_f32 v225, v225\n\t"
    "v_exp_f32 v226, v226\n\t"
    "v_exp_f32 v227, v227\n\t"
    "v_cvt_pk_bf16_f32 v204, v220, v221\n\t"
    "v_cvt_pk_bf16_f32 v205, v222, v223\n\t"
    "v_add_f32 v228, v228, v220\n\t"
    "v_add_f32 v228, v228, v221\n\t"
    "v_add_f32 v228, v228, v222\n\t"
    "v_add_f32 v228, v228, v223\n\t"
    "v_fma_f32 v220, v176, s48, v234\n\t"
    "v_fma_f32 v221, v177, s48, v234\n\t"
    "v_fma_f32 v222, v178, s48, v234\n\t"
    "v_fma_f32 v223, v179, s48, v234\n\t"
    "v_exp_f32 v220, v220\n\t"
    "v_exp_f32 v221, v221\n\t"
    "v_exp_f32 v222, v222\n\t"
    "v_exp_f32 v223, v223\n\t"
    "v_cvt_pk_bf16_f32 v208, v224, v225\n\t"
    "v_cvt_pk_bf16_f32 v209, v226, v227\n\t"
    "v_add_f32 v229, v229, v224\n\t"
    "v_add_f32 v229, v229, v225\n\t"
    "v_add_f32 v229, v229, v226\n\t"
    "v_add_f32 v229, v229, v227\n\t"
    "v_fma_f32 v224, v180, s48, v232\n\t"
    "v_fma_f32 v225, v181, s48, v232\n\t"
    "v_fma_f32 v226, v182, s48, v232\n\t"
    "v_fma_f32 v227, v183, s48, v232\n\t"
    "v_exp_f32 v224, v224\n\t"
    "v_exp_f32 v225, v225\n\t"
    "v_exp_f32 v226, v226\n\t"
    "v_exp_f32 v227, v227\n\t"
    "v_cvt_pk_bf16_f32 v212, v220, v221\n\t"
    "v_cvt_pk_bf16_f32 v213, v222, v223\n\t"
    "v_add_f32 v230, v230, v220\n\t"
    "v_add_f32 v230, v230, v221\n\t"
    "v_add_f32 v230, v230, v222\n\t"
    "v_add_f32 v230, v230, v223\n\t"
    "v_fma_f32 v220, v184, s48, v233\n\t"
    "v_fma_f32 v221, v185, s48, v233\n\t"
    "v_fma_f32 v222, v186, s48, v233\n\t"
    "v_fma_f32 v223, v187, s48, v233\n\t"
    "v_exp_f32 v220, v220\n\t"
    "v_exp_f32 v221, v221\n\t"
    "v_exp_f32 v222, v222\n\t"
    "v_exp_f32 v223, v223\n\t"
    "v_cvt_pk_bf16_f32 v206, v224, v225\n\t"
    "v_cvt_pk_bf16_f32 v207, v226, v227\n\t"
    "v_add_f32 v228, v228, v224\n\t"
    "v_add_f32 v228, v228, v225\n\t"
    "v_add_f32 v228, v228, v226\n\t"
    "v_add_f32 v228, v228, v227\n\t"
    "v_fma_f32 v224, v188, s48, v234\n\t"
    "v_fma_f32 v225, v189, s48, v234\n\t"
    "v_fma_f32 v226, v190, s48, v234\n\t"
    "v_fma_f32 v227, v191, s48, v234\n\t"
    "v_exp_f32 v224, v224\n\t"
    "v_exp_f32 v225, v225\n\t"
    "v_exp_f32 v226, v226\n\t"
    "v_exp_f32 v227, v227\n\t"
    "v_cvt_pk_bf16_f32 v210, v220, v221\n\t"
    "v_cvt_pk_bf16_f32 v211, v222, v223\n\t"
    "v_add_f32 v229, v229, v220\n\t"
    "v_add_f32 v229, v229, v221\n\t"
    "v_add_f32 v229, v229, v222\n\t"
    "v_add_f32 v229, v229, v223\n\t"
    "v_cvt_pk_bf16_f32 v214, v224, v225\n\t"
    "v_cvt_pk_bf16_f32 v215, v226, v227\n\t"
    "v_add_f32 v230, v230, v224\n\t"
    "v_add_f32 v230, v230, v225\n\t"
    "v_add_f32 v230, v230, v226\n\t"
    "v_add_f32 v230, v230, v227\n\t"
    "v_max3_f32 v220, v228, v229, v230\n\t"
    "v_cmp_lt_f32 vcc, s49, v220\n\t"
    "s_cbranch_vccnz LFBA2%=\n\t"
    "LRETA2%=:\n\t"
    "v_add_f32 v235, v235, v228\n\t"
    "v_add_f32 v236, v236, v229\n\t"
    "v_add_f32 v237, v237, v230\n\t"
    "s_waitcnt vmcnt(0)\n\t"
    "s_barrier\n\t"
    "s_setprio 1\n\t"
    "ds_read_b64_tr_b16 v[144:145], v240 offset:32768\n\t"
    "ds_read_b64_tr_b16 v[146:147], v240 offset:36864\n\t"
    "ds_read_b64_tr_b16 v[148:149], v240 offset:40960\n\t"
    "ds_read_b64_tr_b16 v[150:151], v240 offset:45056\n\t"
    "ds_read_b64_tr_b16 v[152:153], v241 offset:40960\n\t"
    "ds_read_b64_tr_b16 v[154:155], v241 offset:45056\n\t"
    "ds_read_b64_tr_b16 v[156:157], v241 offset:32768\n\t"
    "ds_read_b64_tr_b16 v[158:159], v241 offset:36864\n\t"
    "s_waitcnt lgkmcnt(6)\n\t"
    "v_mfma_f32_16x16x32_bf16 v[0:3], v[144:147], v[192:195], v[0:3]\n\t"
    "ds_read_b64_tr_b16 v[160:161], v240 offset:33280\n\t"
    "ds_read_b64_tr_b16 v[162:163], v240 offset:37376\n\t"
    "s_waitcnt lgkmcnt(6)\n\t"
    "v_mfma_f32_16x16x32_bf16 v[0:3], v[148:151], v[204:207], v[0:3]\n\t"
    "ds_read_b64_tr_b16 v[164:165], v240 offset:41472\n\t"
    "ds_read_b64_tr_b16 v[166:167], v240 offset:45568\n\t"
    "v_mfma_f32_16x16x32_bf16 v[4:7], v[148:151], v[208:211], v[4:7]\n\t"
    "ds_read_b64_tr_b16 v[168:169], v241 offset:41472\n\t"
    "ds_read_b64_tr_b16 v[170:171], v241 offset:45568\n\t"
    "v_mfma_f32_16x16x32_bf16 v[4:7], v[144:147], v[196:199], v[4:7]\n\t"
    "ds_read_b64_tr_b16 v[172:173], v241 offset:33280\n\t"
    "ds_read_b64_tr_b16 v[174:175], v241 offset:37376\n\t"
    "v_mfma_f32_16x16x32_bf16 v[8:11], v[144:147], v[200:203], v[8:11]\n\t"
    "s_add_u32 s58, s50, 49152\n\t"
    "v_mfma_f32_16x16x32_bf16 v[8:11], v[148:151], v[212:215], v[8:11]\n\t"
    "s_waitcnt lgkmcnt(10)\n\t"
    "v_mfma_f32_16x16x32_bf16 v[20:23], v[152:155], v[212:215], v[20:23]\n\t"
    "s_waitcnt lgkmcnt(8)\n\t"
    "v_mfma_f32_16x16x32_bf16 v[20:23], v[156:159], v[200:203], v[20:23]\n\t"
    "s_add_u32 m0, s52, 49152\n\t"
    "v_mfma_f32_16x16x32_bf16 v[16:19], v[156:159], v[196:199], v[16:19]\n\t"
    "ds_read_b64_tr_b16 v[176:177], v240 offset:33792\n\t"
    "ds_read_b64_tr_b16 v[178:179], v240 offset:37888\n\t"
    "v_mfma_f32_16x16x32_bf16 v[16:19], v[152:155], v[208:211], v[16:19]\n\t"
    "ds_read_b64_tr_b16 v[144:145], v240 offset:41984\n\t"
    "ds_read_b64_tr_b16 v[146:147], v240 offset:46080\n\t"
    "v_mfma_f32_16x16x32_bf16 v[12:15], v[152:155], v[204:207], v[12:15]\n\t"
    "buffer_load_dwordx4 v244, s[40:43], s58 offen offset:0 nt lds\n\t"
    "v_mfma_f32_16x16x32_bf16 v[12:15], v[156:159], v[192:195], v[12:15]\n\t"
    "s_waitcnt lgkmcnt(10)\n\t"
    "v_mfma_f32_16x16x32_bf16 v[24:27], v[160:163], v[192:195], v[24:27]\n\t"
    "s_waitcnt lgkmcnt(8)\n\t"
    "v_mfma_f32_16x16x32_bf16 v[24:27], v[164:167], v[204:207], v[24:27]\n\t"
    "buffer_load_dwordx4 v244, s[40:43], s58 offen offset:2048 nt lds\n\t"
    "v_mfma_f32_16x16x32_bf16 v[28:31], v[164:167], v[208:211], v[28:31]\n\t"
    "ds_read_b64_tr_b16 v[148:149], v241 offset:41984\n\t"
    "ds_read_b64_tr_b16 v[150:151], v241 offset:46080\n\t"
    "v_mfma_f32_16x16x32_bf16 v[28:31], v[160:163], v[196:199], v[28:31]\n\t"
    "ds_read_b64_tr_b16 v[152:153], v241 offset:33792\n\t"
    "ds_read_b64_tr_b16 v[154:155], v241 offset:37888\n\t"
    "v_mfma_f32_16x16x32_bf16 v[32:35], v[160:163], v[200:203], v[32:35]\n\t"
    "s_add_u32 m0, s52, 50048\n\t"
    "v_mfma_f32_16x16x32_bf16 v[32:35], v[164:167], v[212:215], v[32:35]\n\t"
    "s_waitcnt lgkmcnt(10)\n\t"
    "v_mfma_f32_16x16x32_bf16 v[44:47], v[168:171], v[212:215], v[44:47]\n\t"
    "s_waitcnt lgkmcnt(8)\n\t"
    "v_mfma_f32_16x16x32_bf16 v[44:47], v[172:175], v[200:203], v[44:47]\n\t"
    "buffer_load_dwordx4 v244, s[40:43], s58 offen offset:128 nt lds\n\t"
    "v_mfma_f32_16x16x32_bf16 v[40:43], v[172:175], v[196:199], v[40:43]\n\t"
    "ds_read_b64_tr_b16 v[156:157], v240 offset:34304\n\t"
    "ds_read_b64_tr_b16 v[158:159], v240 offset:38400\n\t"
    "v_mfma_f32_16x16x32_bf16 v[40:43], v[168:171], v[208:211], v[40:43]\n\t"
    "ds_read_b64_tr_b16 v[160:161], v240 offset:42496\n\t"
    "ds_read_b64_tr_b16 v[162:163], v240 offset:46592\n\t"
    "v_mfma_f32_16x16x32_bf16 v[36:39], v[168:171], v[204:207], v[36:39]\n\t"
    "buffer_load_dwordx4 v244, s[40:43], s58 offen offset:2176 nt lds\n\t"
    "v_mfma_f32_16x16x32_bf16 v[36:39], v[172:175], v[192:195], v[36:39]\n\t"
    "s_waitcnt lgkmcnt(10)\n\t"
    "v_mfma_f32_16x16x32_bf16 v[48:51], v[176:179], v[192:195], v[48:51]\n\t"
    "s_waitcnt lgkmcnt(8)\n\t"
    "v_mfma_f32_16x16x32_bf16 v[48:51], v[144:147], v[204:207], v[48:51]\n\t"
    "s_add_u32 s57, s50, 65536\n\t"
    "v_mfma_f32_16x16x32_bf16 v[52:55], v[144:147], v[208:211], v[52:55]\n\t"
    "ds_read_b64_tr_b16 v[164:165], v241 offset:42496\n\t"
    "ds_read_b64_tr_b16 v[166:167], v241 offset:46592\n\t"
    "v_mfma_f32_16x16x32_bf16 v[52:55], v[176:179], v[196:199], v[52:55]\n\t"
    "ds_read_b64_tr_b16 v[168:169], v241 offset:34304\n\t"
    "ds_read_b64_tr_b16 v[170:171], v241 offset:38400\n\t"
    "v_mfma_f32_16x16x32_bf16 v[56:59], v[176:179], v[200:203], v[56:59]\n\t"
    "s_add_u32 m0, s52, 0\n\t"
    "v_mfma_f32_16x16x32_bf16 v[56:59], v[144:147], v[212:215], v[56:59]\n\t"
    "s_waitcnt lgkmcnt(10)\n\t"
    "v_mfma_f32_16x16x32_bf16 v[68:71], v[148:151], v[212:215], v[68:71]\n\t"
    "s_waitcnt lgkmcnt(8)\n\t"
    "v_mfma_f32_16x16x32_bf16 v[68:71], v[152:155], v[200:203], v[68:71]\n\t"
    "buffer_load_dwordx4 v242, s[36:39], s57 offen offset:0 nt lds\n\t"
    "v_mfma_f32_16x16x32_bf16 v[64:67], v[152:155], v[196:199], v[64:67]\n\t"
    "ds_read_b128 v[180:183], v238 offset:16384\n\t"
    "v_mfma_f32_16x16x32_bf16 v[64:67], v[148:151], v[208:211], v[64:67]\n\t"
    "ds_read_b128 v[184:187], v239 offset:16384\n\t"
    "v_mfma_f32_16x16x32_bf16 v[60:63], v[148:151], v[204:207], v[60:63]\n\t"
    "ds_read_b128 v[188:191], v238 offset:17408\n\t"
    "v_mfma_f32_16x16x32_bf16 v[60:63], v[152:155], v[192:195], v[60:63]\n\t"
    "ds_read_b128 v[216:219], v239 offset:17408\n\t"
    "s_waitcnt lgkmcnt(10)\n\t"
    "v_mfma_f32_16x16x32_bf16 v[72:75], v[156:159], v[192:195], v[72:75]\n\t"
    "s_waitcnt lgkmcnt(8)\n\t"
    "v_mfma_f32_16x16x32_bf16 v[72:75], v[160:163], v[204:207], v[72:75]\n\t"
    "buffer_load_dwordx4 v243, s[36:39], s57 offen offset:2048 nt lds\n\t"
    "v_mfma_f32_16x16x32_bf16 v[76:79], v[160:163], v[208:211], v[76:79]\n\t"
    "v_mfma_f32_16x16x32_bf16 v[76:79], v[156:159], v[196:199], v[76:79]\n\t"
    "s_add_u32 m0, s52, 896\n\t"
    "v_mfma_f32_16x16x32_bf16 v[80:83], v[156:159], v[200:203], v[80:83]\n\t"
    "buffer_load_dwordx4 v242, s[36:39], s57 offen offset:128 nt lds\n\t"
    "v_mfma_f32_16x16x32_bf16 v[80:83], v[160:163], v[212:215], v[80:83]\n\t"
    "s_waitcnt lgkmcnt(6)\n\t"
    "v_mfma_f32_16x16x32_bf16 v[92:95], v[164:167], v[212:215], v[92:95]\n\t"
    "s_waitcnt lgkmcnt(4)\n\t"
    "v_mfma_f32_16x16x32_bf16 v[92:95], v[168:171], v[200:203], v[92:95]\n\t"
    "buffer_load_dwordx4 v243, s[36:39], s57 offen offset:2176 nt lds\n\t"
    "v_mfma_f32_16x16x32_bf16 v[88:91], v[168:171], v[196:199], v[88:91]\n\t"
    "v_mfma_f32_16x16x32_bf16 v[88:91], v[164:167], v[208:211], v[88:91]\n\t"
    "v_mfma_f32_16x16x32_bf16 v[84:87], v[164:167], v[204:207], v[84:87]\n\t"
    "v_mfma_f32_16x16x32_bf16 v[84:87], v[168:171], v[192:195], v[84:87]\n\t"
    "ds_read_b128 v[204:207], v239 offset:21504\n\t"
    "ds_read_b128 v[200:203], v238 offset:21504\n\t"
    "s_waitcnt lgkmcnt(5)\n\t"
    "v_mfma_f32_16x16x32_bf16 v[144:147], v[180:183], v[96:99], 0\n\t"
    "ds_read_b128 v[196:199], v239 offset:20480\n\t"
    "ds_read_b128 v[192:195], v238 offset:20480\n\t"
    "s_waitcnt lgkmcnt(6)\n\t"
    "v_mfma_f32_16x16x32_bf16 v[144:147], v[184:187], v[100:103], v[144:147]\n\t"
    "s_waitcnt lgkmcnt(5)\n\t"
    "v_mfma_f32_16x16x32_bf16 v[144:147], v[188:191], v[104:107], v[144:147]\n\t"
    "s_waitcnt lgkmcnt(4)\n\t"
    "v_mfma_f32_16x16x32_bf16 v[144:147], v[216:219], v[108:111], v[144:147]\n\t"
    "v_mfma_f32_16x16x32_bf16 v[148:151], v[216:219], v[124:127], 0\n\t"
    "v_mfma_f32_16x16x32_bf16 v[148:151], v[188:191], v[120:123], v[148:151]\n\t"
    "v_mfma_f32_16x16x32_bf16 v[148:151], v[184:187], v[116:119], v[148:151]\n\t"
    "v_mfma_f32_16x16x32_bf16 v[148:151], v[180:183], v[112:115], v[148:151]\n\t"
    "v_mfma_f32_16x16x32_bf16 v[152:155], v[180:183], v[128:131], 0\n\t"
    "ds_read_b128 v[180:183], v238 offset:24576\n\t"
    "v_mfma_f32_16x16x32_bf16 v[152:155], v[184:187], v[132:135], v[152:155]\n\t"
    "ds_read_b128 v[184:187], v239 offset:24576\n\t"
    "v_mfma_f32_16x16x32_bf16 v[152:155], v[188:191], v[136:139], v[152:155]\n\t"
    "ds_read_b128 v[188:191], v238 offset:25600\n\t"
    "v_mfma_f32_16x16x32_bf16 v[152:155], v[216:219], v[140:143], v[152:155]\n\t"
    "ds_read_b128 v[216:219], v239 offset:25600\n\t"
    "s_waitcnt lgkmcnt(7)\n\t"
    "v_mfma_f32_16x16x32_bf16 v[164:167], v[204:207], v[140:143], 0\n\t"
    "s_waitcnt lgkmcnt(6)\n\t"
    "v_mfma_f32_16x16x32_bf16 v[164:167], v[200:203], v[136:139], v[164:167]\n\t"
    "s_waitcnt lgkmcnt(5)\n\t"
    "v_mfma_f32_16x16x32_bf16 v[164:167], v[196:199], v[132:135], v[164:167]\n\t"
    "s_waitcnt lgkmcnt(4)\n\t"
    "v_mfma_f32_16x16x32_bf16 v[164:167], v[192:195], v[128:131], v[164:167]\n\t"
    "v_mfma_f32_16x16x32_bf16 v[160:163], v[192:195], v[112:115], 0\n\t"
    "v_mfma_f32_16x16x32_bf16 v[160:163], v[196:199], v[116:119], v[160:163]\n\t"
    "v_mfma_f32_16x16x32_bf16 v[160:163], v[200:203], v[120:123], v[160:163]\n\t"
    "v_mfma_f32_16x16x32_bf16 v[160:163], v[204:207], v[124:127], v[160:163]\n\t"
    "v_mfma_f32_16x16x32_bf16 v[156:159], v[204:207], v[108:111], 0\n\t"
    "ds_read_b128 v[204:207], v239 offset:29696\n\t"
    "v_mfma_f32_16x16x32_bf16 v[156:159], v[200:203], v[104:107], v[156:159]\n\t"
    "ds_read_b128 v[200:203], v238 offset:29696\n\t"
    "v_mfma_f32_16x16x32_bf16 v[156:159], v[196:199], v[100:103], v[156:159]\n\t"
    "ds_read_b128 v[196:199], v239 offset:28672\n\t"
    "v_mfma_f32_16x16x32_bf16 v[156:159], v[192:195], v[96:99], v[156:159]\n\t"
    "ds_read_b128 v[192:195], v238 offset:28672\n\t"
    "s_waitcnt lgkmcnt(7)\n\t"
    "v_mfma_f32_16x16x32_bf16 v[168:171], v[180:183], v[96:99], 0\n\t"
    "s_waitcnt lgkmcnt(6)\n\t"
    "v_mfma_f32_16x16x32_bf16 v[168:171], v[184:187], v[100:103], v[168:171]\n\t"
    "s_waitcnt lgkmcnt(5)\n\t"
    "v_mfma_f32_16x16x32_bf16 v[168:171], v[188:191], v[104:107], v[168:171]\n\t"
    "s_waitcnt lgkmcnt(4)\n\t"
    "v_mfma_f32_16x16x32_bf16 v[168:171], v[216:219], v[108:111], v[168:171]\n\t"
    "v_mfma_f32_16x16x32_bf16 v[172:175], v[216:219], v[124:127], 0\n\t"
    "v_mfma_f32_16x16x32_bf16 v[172:175], v[188:191], v[120:123], v[172:175]\n\t"
    "v_mfma_f32_16x16x32_bf16 v[172:175], v[184:187], v[116:119], v[172:175]\n\t"
    "v_mfma_f32_16x16x32_bf16 v[172:175], v[180:183], v[112:115], v[172:175]\n\t"
    "v_mfma_f32_16x16x32_bf16 v[176:179], v[180:183], v[128:131], 0\n\t"
    "v_mfma_f32_16x16x32_bf16 v[176:179], v[184:187], v[132:135], v[176:179]\n\t"
    "v_mfma_f32_16x16x32_bf16 v[176:179], v[188:191], v[136:139], v[176:179]\n\t"
    "v_mfma_f32_16x16x32_bf16 v[176:179], v[216:219], v[140:143], v[176:179]\n\t"
    "s_waitcnt lgkmcnt(3)\n\t"
    "v_mfma_f32_16x16x32_bf16 v[188:191], v[204:207], v[140:143], 0\n\t"
    "s_waitcnt lgkmcnt(2)\n\t"
    "v_mfma_f32_16x16x32_bf16 v[188:191], v[200:203], v[136:139], v[188:191]\n\t"
    "s_waitcnt lgkmcnt(1)\n\t"
    "v_mfma_f32_16x16x32_bf16 v[188:191], v[196:199], v[132:135], v[188:191]\n\t"
    "s_waitcnt lgkmcnt(0)\n\t"
    "v_mfma_f32_16x16x32_bf16 v[188:191], v[192:195], v[128:131], v[188:191]\n\t"
    "v_mfma_f32_16x16x32_bf16 v[184:187], v[192:195], v[112:115], 0\n\t"
    "v_mfma_f32_16x16x32_bf16 v[184:187], v[196:199], v[116:119], v[184:187]\n\t"
    "v_mfma_f32_16x16x32_bf16 v[184:187], v[200:203], v[120:123], v[184:187]\n\t"
    "v_mfma_f32_16x16x32_bf16 v[184:187], v[204:207], v[124:127], v[184:187]\n\t"
    "v_mfma_f32_16x16x32_bf16 v[180:183], v[204:207], v[108:111], 0\n\t"
    "v_mfma_f32_16x16x32_bf16 v[180:183], v[200:203], v[104:107], v[180:183]\n\t"
    "v_mfma_f32_16x16x32_bf16 v[180:183], v[196:199], v[100:103], v[180:183]\n\t"
    "v_mfma_f32_16x16x32_bf16 v[180:183], v[192:195], v[96:99], v[180:183]\n\t"
    "s_waitcnt lgkmcnt(0)\n\t"
    "s_barrier\n\t"
    "s_setprio 0\n\t"
    "v_fma_f32 v220, v144, s48, v232\n\t"
    "v_fma_f32 v221, v145, s48, v232\n\t"
    "v_fma_f32 v222, v146, s48, v232\n\t"
    "v_fma_f32 v223, v147, s48, v232\n\t"
    "v_exp_f32 v220, v220\n\t"
    "v_exp_f32 v221, v221\n\t"
    "v_exp_f32 v222, v222\n\t"
    "v_exp_f32 v223, v223\n\t"
    "v_fma_f32 v224, v148, s48, v233\n\t"
    "v_fma_f32 v225, v149, s48, v233\n\t"
    "v_fma_f32 v226, v150, s48, v233\n\t"
    "v_fma_f32 v227, v151, s48, v233\n\t"
    "v_exp_f32 v224, v224\n\t"
    "v_exp_f32 v225, v225\n\t"
    "v_exp_f32 v226, v226\n\t"
    "v_exp_f32 v227, v227\n\t"
    "v_cvt_pk_bf16_f32 v192, v220, v221\n\t"
    "v_cvt_pk_bf16_f32 v193, v222, v223\n\t"
    "v_add_f32 v228, v220, v221\n\t"
    "v_add_f32 v228, v228, v222\n\t"
    "v_add_f32 v228, v228, v223\n\t"
    "v_fma_f32 v220, v152, s48, v234\n\t"
    "v_fma_f32 v221, v153, s48, v234\n\t"
    "v_fma_f32 v222, v154, s48, v234\n\t"
    "v_fma_f32 v223, v155, s48, v234\n\t"
    "v_exp_f32 v220, v220\n\t"
    "v_exp_f32 v221, v221\n\t"
    "v_exp_f32 v222, v222\n\t"
    "v_exp_f32 v223, v223\n\t"
    "v_cvt_pk_bf16_f32 v196, v224, v225\n\t"
    "v_cvt_pk_bf16_f32 v197, v226, v227\n\t"
    "v_add_f32 v229, v224, v225\n\t"
    "v_add_f32 v229, v229, v226\n\t"
    "v_add_f32 v229, v229, v227\n\t"
    "v_fma_f32 v224, v156, s48, v232\n\t"
    "v_fma_f32 v225, v157, s48, v232\n\t"
    "v_fma_f32 v226, v158, s48, v232\n\t"
    "v_fma_f32 v227, v159, s48, v232\n\t"
    "v_exp_f32 v224, v224\n\t"
    "v_exp_f32 v225, v225\n\t"
    "v_exp_f32 v226, v226\n\t"
    "v_exp_f32 v227, v227\n\t"
    "v_cvt_pk_bf16_f32 v200, v220, v221\n\t"
    "v_cvt_pk_bf16_f32 v201, v222, v223\n\t"
    "v_add_f32 v230, v220, v221\n\t"
    "v_add_f32 v230, v230, v222\n\t"
    "v_add_f32 v230, v230, v223\n\t"
    "v_fma_f32 v220, v160, s48, v233\n\t"
    "v_fma_f32 v221, v161, s48, v233\n\t"
    "v_fma_f32 v222, v162, s48, v233\n\t"
    "v_fma_f32 v223, v163, s48, v233\n\t"
    "v_exp_f32 v220, v220\n\t"
    "v_exp_f32 v221, v221\n\t"
    "v_exp_f32 v222, v222\n\t"
    "v_exp_f32 v223, v223\n\t"
    "v_cvt_pk_bf16_f32 v194, v224, v225\n\t"
    "v_cvt_pk_bf16_f32 v195, v226, v227\n\t"
    "v_add_f32 v228, v228, v224\n\t"
    "v_add_f32 v228, v228, v225\n\t"
    "v_add_f32 v228, v228, v226\n\t"
    "v_add_f32 v228, v228, v227\n\t"
    "v_fma_f32 v224, v164, s48, v234\n\t"
    "v_fma_f32 v225, v165, s48, v234\n\t"
    "v_fma_f32 v226, v166, s48, v234\n\t"
    "v_fma_f32 v227, v167, s48, v234\n\t"
    "v_exp_f32 v224, v224\n\t"
    "v_exp_f32 v225, v225\n\t"
    "v_exp_f32 v226, v226\n\t"
    "v_exp_f32 v227, v227\n\t"
    "v_cvt_pk_bf16_f32 v198, v220, v221\n\t"
    "v_cvt_pk_bf16_f32 v199, v222, v223\n\t"
    "v_add_f32 v229, v229, v220\n\t"
    "v_add_f32 v229, v229, v221\n\t"
    "v_add_f32 v229, v229, v222\n\t"
    "v_add_f32 v229, v229, v223\n\t"
    "v_fma_f32 v220, v168, s48, v232\n\t"
    "v_fma_f32 v221, v169, s48, v232\n\t"
    "v_fma_f32 v222, v170, s48, v232\n\t"
    "v_fma_f32 v223, v171, s48, v232\n\t"
    "v_exp_f32 v220, v220\n\t"
    "v_exp_f32 v221, v221\n\t"
    "v_exp_f32 v222, v222\n\t"
    "v_exp_f32 v223, v223\n\t"
    "v_cvt_pk_bf16_f32 v202, v224, v225\n\t"
    "v_cvt_pk_bf16_f32 v203, v226, v227\n\t"
    "v_add_f32 v230, v230, v224\n\t"
    "v_add_f32 v230, v230, v225\n\t"
    "v_add_f32 v230, v230, v226\n\t"
    "v_add_f32 v230, v230, v227\n\t"
    "v_fma_f32 v224, v172, s48, v233\n\t"
    "v_fma_f32 v225, v173, s48, v233\n\t"
    "v_fma_f32 v226, v174, s48, v233\n\t"
    "v_fma_f32 v227, v175, s48, v233\n\t"
    "v_exp_f32 v224, v224\n\t"
    "v_exp_f32 v225, v225\n\t"
    "v_exp_f32 v226, v226\n\t"
    "v_exp_f32 v227, v227\n\t"
    "v_cvt_pk_bf16_f32 v204, v220, v221\n\t"
    "v_cvt_pk_bf16_f32 v205, v222, v223\n\t"
    "v_add_f32 v228, v228, v220\n\t"
    "v_add_f32 v228, v228, v221\n\t"
    "v_add_f32 v228, v228, v222\n\t"
    "v_add_f32 v228, v228, v223\n\t"
    "v_fma_f32 v220, v176, s48, v234\n\t"
    "v_fma_f32 v221, v177, s48, v234\n\t"
    "v_fma_f32 v222, v178, s48, v234\n\t"
    "v_fma_f32 v223, v179, s48, v234\n\t"
    "v_exp_f32 v220, v220\n\t"
    "v_exp_f32 v221, v221\n\t"
    "v_exp_f32 v222, v222\n\t"
    "v_exp_f32 v223, v223\n\t"
    "v_cvt_pk_bf16_f32 v208, v224, v225\n\t"
    "v_cvt_pk_bf16_f32 v209, v226, v227\n\t"
    "v_add_f32 v229, v229, v224\n\t"
    "v_add_f32 v229, v229, v225\n\t"
    "v_add_f32 v229, v229, v226\n\t"
    "v_add_f32 v229, v229, v227\n\t"
    "v_fma_f32 v224, v180, s48, v232\n\t"
    "v_fma_f32 v225, v181, s48, v232\n\t"
    "v_fma_f32 v226, v182, s48, v232\n\t"
    "v_fma_f32 v227, v183, s48, v232\n\t"
    "v_exp_f32 v224, v224\n\t"
    "v_exp_f32 v225, v225\n\t"
    "v_exp_f32 v226, v226\n\t"
    "v_exp_f32 v227, v227\n\t"
    "v_cvt_pk_bf16_f32 v212, v220, v221\n\t"
    "v_cvt_pk_bf16_f32 v213, v222, v223\n\t"
    "v_add_f32 v230, v230, v220\n\t"
    "v_add_f32 v230, v230, v221\n\t"
    "v_add_f32 v230, v230, v222\n\t"
    "v_add_f32 v230, v230, v223\n\t"
    "v_fma_f32 v220, v184, s48, v233\n\t"
    "v_fma_f32 v221, v185, s48, v233\n\t"
    "v_fma_f32 v222, v186, s48, v233\n\t"
    "v_fma_f32 v223, v187, s48, v233\n\t"
    "v_exp_f32 v220, v220\n\t"
    "v_exp_f32 v221, v221\n\t"
    "v_exp_f32 v222, v222\n\t"
    "v_exp_f32 v223, v223\n\t"
    "v_cvt_pk_bf16_f32 v206, v224, v225\n\t"
    "v_cvt_pk_bf16_f32 v207, v226, v227\n\t"
    "v_add_f32 v228, v228, v224\n\t"
    "v_add_f32 v228, v228, v225\n\t"
    "v_add_f32 v228, v228, v226\n\t"
    "v_add_f32 v228, v228, v227\n\t"
    "v_fma_f32 v224, v188, s48, v234\n\t"
    "v_fma_f32 v225, v189, s48, v234\n\t"
    "v_fma_f32 v226, v190, s48, v234\n\t"
    "v_fma_f32 v227, v191, s48, v234\n\t"
    "v_exp_f32 v224, v224\n\t"
    "v_exp_f32 v225, v225\n\t"
    "v_exp_f32 v226, v226\n\t"
    "v_exp_f32 v227, v227\n\t"
    "v_cvt_pk_bf16_f32 v210, v220, v221\n\t"
    "v_cvt_pk_bf16_f32 v211, v222, v223\n\t"
    "v_add_f32 v229, v229, v220\n\t"
    "v_add_f32 v229, v229, v221\n\t"
    "v_add_f32 v229, v229, v222\n\t"
    "v_add_f32 v229, v229, v223\n\t"
    "v_cvt_pk_bf16_f32 v214, v224, v225\n\t"
    "v_cvt_pk_bf16_f32 v215, v226, v227\n\t"
    "v_add_f32 v230, v230, v224\n\t"
    "v_add_f32 v230, v230, v225\n\t"
    "v_add_f32 v230, v230, v226\n\t"
    "v_add_f32 v230, v230, v227\n\t"
    "v_max3_f32 v220, v228, v229, v230\n\t"
    "v_cmp_lt_f32 vcc, s49, v220\n\t"
    "s_cbranch_vccnz LFBA3%=\n\t"
    "LRETA3%=:\n\t"
    "v_add_f32 v235, v235, v228\n\t"
    "v_add_f32 v236, v236, v229\n\t"
    "v_add_f32 v237, v237, v230\n\t"
    "s_waitcnt vmcnt(0)\n\t"
    "s_barrier\n\t"
    "s_setprio 1\n\t"
    "ds_read_b64_tr_b16 v[144:145], v240 offset:49152\n\t"
    "ds_read_b64_tr_b16 v[146:147], v240 offset:53248\n\t"
    "ds_read_b64_tr_b16 v[148:149], v240 offset:57344\n\t"
    "ds_read_b64_tr_b16 v[150:151], v240 offset:61440\n\t"
    "ds_read_b64_tr_b16 v[152:153], v241 offset:57344\n\t"
    "ds_read_b64_tr_b16 v[154:155], v241 offset:61440\n\t"
    "ds_read_b64_tr_b16 v[156:157], v241 offset:49152\n\t"
    "ds_read_b64_tr_b16 v[158:159], v241 offset:53248\n\t"
    "s_waitcnt lgkmcnt(6)\n\t"
    "v_mfma_f32_16x16x32_bf16 v[0:3], v[144:147], v[192:195], v[0:3]\n\t"
    "ds_read_b64_tr_b16 v[160:161], v240 offset:49664\n\t"
    "ds_read_b64_tr_b16 v[162:163], v240 offset:53760\n\t"
    "s_waitcnt lgkmcnt(6)\n\t"
    "v_mfma_f32_16x16x32_bf16 v[0:3], v[148:151], v[204:207], v[0:3]\n\t"
    "ds_read_b64_tr_b16 v[164:165], v240 offset:57856\n\t"
    "ds_read_b64_tr_b16 v[166:167], v240 offset:61952\n\t"
    "v_mfma_f32_16x16x32_bf16 v[4:7], v[148:151], v[208:211], v[4:7]\n\t"
    "ds_read_b64_tr_b16 v[168:169], v241 offset:57856\n\t"
    "ds_read_b64_tr_b16 v[170:171], v241 offset:61952\n\t"
    "v_mfma_f32_16x16x32_bf16 v[4:7], v[144:147], v[196:199], v[4:7]\n\t"
    "ds_read_b64_tr_b16 v[172:173], v241 offset:49664\n\t"
    "ds_read_b64_tr_b16 v[174:175], v241 offset:53760\n\t"
    "v_mfma_f32_16x16x32_bf16 v[8:11], v[144:147], v[200:203], v[8:11]\n\t"
    "s_add_u32 s58, s50, 65536\n\t"
    "v_mfma_f32_16x16x32_bf16 v[8:11], v[148:151], v[212:215], v[8:11]\n\t"
    "s_waitcnt lgkmcnt(10)\n\t"
    "v_mfma_f32_16x16x32_bf16 v[20:23], v[152:155], v[212:215], v[20:23]\n\t"
    "s_waitcnt lgkmcnt(8)\n\t"
    "v_mfma_f32_16x16x32_bf16 v[20:23], v[156:159], v[200:203], v[20:23]\n\t"
    "s_add_u32 m0, s52, 32768\n\t"
    "v_mfma_f32_16x16x32_bf16 v[16:19], v[156:159], v[196:199], v[16:19]\n\t"
    "ds_read_b64_tr_b16 v[176:177], v240 offset:50176\n\t"
    "ds_read_b64_tr_b16 v[178:179], v240 offset:54272\n\t"
    "v_mfma_f32_16x16x32_bf16 v[16:19], v[152:155], v[208:211], v[16:19]\n\t"
    "ds_read_b64_tr_b16 v[144:145], v240 offset:58368\n\t"
    "ds_read_b64_tr_b16 v[146:147], v240 offset:62464\n\t"
    "v_mfma_f32_16x16x32_bf16 v[12:15], v[152:155], v[204:207], v[12:15]\n\t"
    "buffer_load_dwordx4 v244, s[40:43], s58 offen offset:0 nt lds\n\t"
    "v_mfma_f32_16x16x32_bf16 v[12:15], v[156:159], v[192:195], v[12:15]\n\t"
    "s_waitcnt lgkmcnt(10)\n\t"
    "v_mfma_f32_16x16x32_bf16 v[24:27], v[160:163], v[192:195], v[24:27]\n\t"
    "s_waitcnt lgkmcnt(8)\n\t"
    "v_mfma_f32_16x16x32_bf16 v[24:27], v[164:167], v[204:207], v[24:27]\n\t"
    "buffer_load_dwordx4 v244, s[40:43], s58 offen offset:2048 nt lds\n\t"
    "v_mfma_f32_16x16x32_bf16 v[28:31], v[164:167], v[208:211], v[28:31]\n\t"
    "ds_read_b64_tr_b16 v[148:149], v241 offset:58368\n\t"
    "ds_read_b64_tr_b16 v[150:151], v241 offset:62464\n\t"
    "v_mfma_f32_16x16x32_bf16 v[28:31], v[160:163], v[196:199], v[28:31]\n\t"
    "ds_read_b64_tr_b16 v[152:153], v241 offset:50176\n\t"
    "ds_read_b64_tr_b16 v[154:155], v241 offset:54272\n\t"
    "v_mfma_f32_16x16x32_bf16 v[32:35], v[160:163], v[200:203], v[32:35]\n\t"
    "s_add_u32 m0, s52, 33664\n\t"
    "v_mfma_f32_16x16x32_bf16 v[32:35], v[164:167], v[212:215], v[32:35]\n\t"
    "s_waitcnt lgkmcnt(10)\n\t"
    "v_mfma_f32_16x16x32_bf16 v[44:47], v[168:171], v[212:215], v[44:47]\n\t"
    "s_waitcnt lgkmcnt(8)\n\t"
    "v_mfma_f32_16x16x32_bf16 v[44:47], v[172:175], v[200:203], v[44:47]\n\t"
    "buffer_load_dwordx4 v244, s[40:43], s58 offen offset:128 nt lds\n\t"
    "v_mfma_f32_16x16x32_bf16 v[40:43], v[172:175], v[196:199], v[40:43]\n\t"
    "ds_read_b64_tr_b16 v[156:157], v240 offset:50688\n\t"
    "ds_read_b64_tr_b16 v[158:159], v240 offset:54784\n\t"
    "v_mfma_f32_16x16x32_bf16 v[40:43], v[168:171], v[208:211], v[40:43]\n\t"
    "ds_read_b64_tr_b16 v[160:161], v240 offset:58880\n\t"
    "ds_read_b64_tr_b16 v[162:163], v240 offset:62976\n\t"
    "v_mfma_f32_16x16x32_bf16 v[36:39], v[168:171], v[204:207], v[36:39]\n\t"
    "buffer_load_dwordx4 v244, s[40:43], s58 offen offset:2176 nt lds\n\t"
    "v_mfma_f32_16x16x32_bf16 v[36:39], v[172:175], v[192:195], v[36:39]\n\t"
    "s_waitcnt lgkmcnt(10)\n\t"
    "v_mfma_f32_16x16x32_bf16 v[48:51], v[176:179], v[192:195], v[48:51]\n\t"
    "s_waitcnt lgkmcnt(8)\n\t"
    "v_mfma_f32_16x16x32_bf16 v[48:51], v[144:147], v[204:207], v[48:51]\n\t"
    "s_add_u32 s57, s50, 81920\n\t"
    "v_mfma_f32_16x16x32_bf16 v[52:55], v[144:147], v[208:211], v[52:55]\n\t"
    "ds_read_b64_tr_b16 v[164:165], v241 offset:58880\n\t"
    "ds_read_b64_tr_b16 v[166:167], v241 offset:62976\n\t"
    "v_mfma_f32_16x16x32_bf16 v[52:55], v[176:179], v[196:199], v[52:55]\n\t"
    "ds_read_b64_tr_b16 v[168:169], v241 offset:50688\n\t"
    "ds_read_b64_tr_b16 v[170:171], v241 offset:54784\n\t"
    "v_mfma_f32_16x16x32_bf16 v[56:59], v[176:179], v[200:203], v[56:59]\n\t"
    "s_add_u32 m0, s52, 16384\n\t"
    "v_mfma_f32_16x16x32_bf16 v[56:59], v[144:147], v[212:215], v[56:59]\n\t"
    "s_waitcnt lgkmcnt(10)\n\t"
    "v_mfma_f32_16x16x32_bf16 v[68:71], v[148:151], v[212:215], v[68:71]\n\t"
    "s_waitcnt lgkmcnt(8)\n\t"
    "v_mfma_f32_16x16x32_bf16 v[68:71], v[152:155], v[200:203], v[68:71]\n\t"
    "buffer_load_dwordx4 v242, s[36:39], s57 offen offset:0 nt lds\n\t"
    "v_mfma_f32_16x16x32_bf16 v[64:67], v[152:155], v[196:199], v[64:67]\n\t"
    "ds_read_b128 v[180:183], v238 offset:0\n\t"
    "v_mfma_f32_16x16x32_bf16 v[64:67], v[148:151], v[208:211], v[64:67]\n\t"
    "ds_read_b128 v[184:187], v239 offset:0\n\t"
    "v_mfma_f32_16x16x32_bf16 v[60:63], v[148:151], v[204:207], v[60:63]\n\t"
    "ds_read_b128 v[188:191], v238 offset:1024\n\t"
    "v_mfma_f32_16x16x32_bf16 v[60:63], v[152:155], v[192:195], v[60:63]\n\t"
    "ds_read_b128 v[216:219], v239 offset:1024\n\t"
    "s_waitcnt lgkmcnt(10)\n\t"
    "v_mfma_f32_16x16x32_bf16 v[72:75], v[156:159], v[192:195], v[72:75]\n\t"
    "s_waitcnt lgkmcnt(8)\n\t"
    "v_mfma_f32_16x16x32_bf16 v[72:75], v[160:163], v[204:207], v[72:75]\n\t"
    "buffer_load_dwordx4 v243, s[36:39], s57 offen offset:2048 nt lds\n\t"
    "v_mfma_f32_16x16x32_bf16 v[76:79], v[160:163], v[208:211], v[76:79]\n\t"
    "v_mfma_f32_16x16x32_bf16 v[76:79], v[156:159], v[196:199], v[76:79]\n\t"
    "s_add_u32 m0, s52, 17280\n\t"
    "v_mfma_f32_16x16x32_bf16 v[80:83], v[156:159], v[200:203], v[80:83]\n\t"
    "buffer_load_dwordx4 v242, s[36:39], s57 offen offset:128 nt lds\n\t"
    "v_mfma_f32_16x16x32_bf16 v[80:83], v[160:163], v[212:215], v[80:83]\n\t"
    "s_waitcnt lgkmcnt(6)\n\t"
    "v_mfma_f32_16x16x32_bf16 v[92:95], v[164:167], v[212:215], v[92:95]\n\t"
    "s_waitcnt lgkmcnt(4)\n\t"
    "v_mfma_f32_16x16x32_bf16 v[92:95], v[168:171], v[200:203], v[92:95]\n\t"
    "buffer_load_dwordx4 v243, s[36:39], s57 offen offset:2176 nt lds\n\t"
    "v_mfma_f32_16x16x32_bf16 v[88:91], v[168:171], v[196:199], v[88:91]\n\t"
    "v_mfma_f32_16x16x32_bf16 v[88:91], v[164:167], v[208:211], v[88:91]\n\t"
    "v_mfma_f32_16x16x32_bf16 v[84:87], v[164:167], v[204:207], v[84:87]\n\t"
    "v_mfma_f32_16x16x32_bf16 v[84:87], v[168:171], v[192:195], v[84:87]\n\t"
    "ds_read_b128 v[204:207], v239 offset:5120\n\t"
    "ds_read_b128 v[200:203], v238 offset:5120\n\t"
    "s_waitcnt lgkmcnt(5)\n\t"
    "v_mfma_f32_16x16x32_bf16 v[144:147], v[180:183], v[96:99], 0\n\t"
    "ds_read_b128 v[196:199], v239 offset:4096\n\t"
    "ds_read_b128 v[192:195], v238 offset:4096\n\t"
    "s_waitcnt lgkmcnt(6)\n\t"
    "v_mfma_f32_16x16x32_bf16 v[144:147], v[184:187], v[100:103], v[144:147]\n\t"
    "s_waitcnt lgkmcnt(5)\n\t"
    "v_mfma_f32_16x16x32_bf16 v[144:147], v[188:191], v[104:107], v[144:147]\n\t"
    "s_waitcnt lgkmcnt(4)\n\t"
    "v_mfma_f32_16x16x32_bf16 v[144:147], v[216:219], v[108:111], v[144:147]\n\t"
    "v_mfma_f32_16x16x32_bf16 v[148:151], v[216:219], v[124:127], 0\n\t"
    "v_mfma_f32_16x16x32_bf16 v[148:151], v[188:191], v[120:123], v[148:151]\n\t"
    "v_mfma_f32_16x16x32_bf16 v[148:151], v[184:187], v[116:119], v[148:151]\n\t"
    "v_mfma_f32_16x16x32_bf16 v[148:151], v[180:183], v[112:115], v[148:151]\n\t"
    "v_mfma_f32_16x16x32_bf16 v[152:155], v[180:183], v[128:131], 0\n\t"
    "ds_read_b128 v[180:183], v238 offset:8192\n\t"
    "v_mfma_f32_16x16x32_bf16 v[152:155], v[184:187], v[132:135], v[152:155]\n\t"
    "ds_read_b128 v[184:187], v239 offset:8192\n\t"
    "v_mfma_f32_16x16x32_bf16 v[152:155], v[188:191], v[136:139], v[152:155]\n\t"
    "ds_read_b128 v[188:191], v238 offset:9216\n\t"
    "v_mfma_f32_16x16x32_bf16 v[152:155], v[216:219], v[140:143], v[152:155]\n\t"
    "ds_read_b128 v[216:219], v239 offset:9216\n\t"
    "s_waitcnt lgkmcnt(7)\n\t"
    "v_mfma_f32_16x16x32_bf16 v[164:167], v[204:207], v[140:143], 0\n\t"
    "s_waitcnt lgkmcnt(6)\n\t"
    "v_mfma_f32_16x16x32_bf16 v[164:167], v[200:203], v[136:139], v[164:167]\n\t"
    "s_waitcnt lgkmcnt(5)\n\t"
    "v_mfma_f32_16x16x32_bf16 v[164:167], v[196:199], v[132:135], v[164:167]\n\t"
    "s_waitcnt lgkmcnt(4)\n\t"
    "v_mfma_f32_16x16x32_bf16 v[164:167], v[192:195], v[128:131], v[164:167]\n\t"
    "v_mfma_f32_16x16x32_bf16 v[160:163], v[192:195], v[112:115], 0\n\t"
    "v_mfma_f32_16x16x32_bf16 v[160:163], v[196:199], v[116:119], v[160:163]\n\t"
    "v_mfma_f32_16x16x32_bf16 v[160:163], v[200:203], v[120:123], v[160:163]\n\t"
    "v_mfma_f32_16x16x32_bf16 v[160:163], v[204:207], v[124:127], v[160:163]\n\t"
    "v_mfma_f32_16x16x32_bf16 v[156:159], v[204:207], v[108:111], 0\n\t"
    "ds_read_b128 v[204:207], v239 offset:13312\n\t"
    "v_mfma_f32_16x16x32_bf16 v[156:159], v[200:203], v[104:107], v[156:159]\n\t"
    "ds_read_b128 v[200:203], v238 offset:13312\n\t"
    "v_mfma_f32_16x16x32_bf16 v[156:159], v[196:199], v[100:103], v[156:159]\n\t"
    "ds_read_b128 v[196:199], v239 offset:12288\n\t"
    "v_mfma_f32_16x16x32_bf16 v[156:159], v[192:195], v[96:99], v[156:159]\n\t"
    "ds_read_b128 v[192:195], v238 offset:12288\n\t"
    "s_waitcnt lgkmcnt(7)\n\t"
    "v_mfma_f32_16x16x32_bf16 v[168:171], v[180:183], v[96:99], 0\n\t"
    "s_waitcnt lgkmcnt(6)\n\t"
    "v_mfma_f32_16x16x32_bf16 v[168:171], v[184:187], v[100:103], v[168:171]\n\t"
    "s_waitcnt lgkmcnt(5)\n\t"
    "v_mfma_f32_16x16x32_bf16 v[168:171], v[188:191], v[104:107], v[168:171]\n\t"
    "s_waitcnt lgkmcnt(4)\n\t"
    "v_mfma_f32_16x16x32_bf16 v[168:171], v[216:219], v[108:111], v[168:171]\n\t"
    "v_mfma_f32_16x16x32_bf16 v[172:175], v[216:219], v[124:127], 0\n\t"
    "v_mfma_f32_16x16x32_bf16 v[172:175], v[188:191], v[120:123], v[172:175]\n\t"
    "v_mfma_f32_16x16x32_bf16 v[172:175], v[184:187], v[116:119], v[172:175]\n\t"
    "v_mfma_f32_16x16x32_bf16 v[172:175], v[180:183], v[112:115], v[172:175]\n\t"
    "v_mfma_f32_16x16x32_bf16 v[176:179], v[180:183], v[128:131], 0\n\t"
    "v_mfma_f32_16x16x32_bf16 v[176:179], v[184:187], v[132:135], v[176:179]\n\t"
    "v_mfma_f32_16x16x32_bf16 v[176:179], v[188:191], v[136:139], v[176:179]\n\t"
    "v_mfma_f32_16x16x32_bf16 v[176:179], v[216:219], v[140:143], v[176:179]\n\t"
    "s_waitcnt lgkmcnt(3)\n\t"
    "v_mfma_f32_16x16x32_bf16 v[188:191], v[204:207], v[140:143], 0\n\t"
    "s_waitcnt lgkmcnt(2)\n\t"
    "v_mfma_f32_16x16x32_bf16 v[188:191], v[200:203], v[136:139], v[188:191]\n\t"
    "s_waitcnt lgkmcnt(1)\n\t"
    "v_mfma_f32_16x16x32_bf16 v[188:191], v[196:199], v[132:135], v[188:191]\n\t"
    "s_waitcnt lgkmcnt(0)\n\t"
    "v_mfma_f32_16x16x32_bf16 v[188:191], v[192:195], v[128:131], v[188:191]\n\t"
    "v_mfma_f32_16x16x32_bf16 v[184:187], v[192:195], v[112:115], 0\n\t"
    "v_mfma_f32_16x16x32_bf16 v[184:187], v[196:199], v[116:119], v[184:187]\n\t"
    "v_mfma_f32_16x16x32_bf16 v[184:187], v[200:203], v[120:123], v[184:187]\n\t"
    "v_mfma_f32_16x16x32_bf16 v[184:187], v[204:207], v[124:127], v[184:187]\n\t"
    "v_mfma_f32_16x16x32_bf16 v[180:183], v[204:207], v[108:111], 0\n\t"
    "v_mfma_f32_16x16x32_bf16 v[180:183], v[200:203], v[104:107], v[180:183]\n\t"
    "v_mfma_f32_16x16x32_bf16 v[180:183], v[196:199], v[100:103], v[180:183]\n\t"
    "v_mfma_f32_16x16x32_bf16 v[180:183], v[192:195], v[96:99], v[180:183]\n\t"
    "s_waitcnt lgkmcnt(0)\n\t"
    "s_barrier\n\t"
    "s_setprio 0\n\t"
    "v_fma_f32 v220, v144, s48, v232\n\t"
    "v_fma_f32 v221, v145, s48, v232\n\t"
    "v_fma_f32 v222, v146, s48, v232\n\t"
    "v_fma_f32 v223, v147, s48, v232\n\t"
    "v_exp_f32 v220, v220\n\t"
    "v_exp_f32 v221, v221\n\t"
    "v_exp_f32 v222, v222\n\t"
    "v_exp_f32 v223, v223\n\t"
    "v_fma_f32 v224, v148, s48, v233\n\t"
    "v_fma_f32 v225, v149, s48, v233\n\t"
    "v_fma_f32 v226, v150, s48, v233\n\t"
    "v_fma_f32 v227, v151, s48, v233\n\t"
    "v_exp_f32 v224, v224\n\t"
    "v_exp_f32 v225, v225\n\t"
    "v_exp_f32 v226, v226\n\t"
    "v_exp_f32 v227, v227\n\t"
    "v_cvt_pk_bf16_f32 v192, v220, v221\n\t"
    "v_cvt_pk_bf16_f32 v193, v222, v223\n\t"
    "v_add_f32 v228, v220, v221\n\t"
    "v_add_f32 v228, v228, v222\n\t"
    "v_add_f32 v228, v228, v223\n\t"
    "v_fma_f32 v220, v152, s48, v234\n\t"
    "v_fma_f32 v221, v153, s48, v234\n\t"
    "v_fma_f32 v222, v154, s48, v234\n\t"
    "v_fma_f32 v223, v155, s48, v234\n\t"
    "v_exp_f32 v220, v220\n\t"
    "v_exp_f32 v221, v221\n\t"
    "v_exp_f32 v222, v222\n\t"
    "v_exp_f32 v223, v223\n\t"
    "v_cvt_pk_bf16_f32 v196, v224, v225\n\t"
    "v_cvt_pk_bf16_f32 v197, v226, v227\n\t"
    "v_add_f32 v229, v224, v225\n\t"
    "v_add_f32 v229, v229, v226\n\t"
    "v_add_f32 v229, v229, v227\n\t"
    "v_fma_f32 v224, v156, s48, v232\n\t"
    "v_fma_f32 v225, v157, s48, v232\n\t"
    "v_fma_f32 v226, v158, s48, v232\n\t"
    "v_fma_f32 v227, v159, s48, v232\n\t"
    "v_exp_f32 v224, v224\n\t"
    "v_exp_f32 v225, v225\n\t"
    "v_exp_f32 v226, v226\n\t"
    "v_exp_f32 v227, v227\n\t"
    "v_cvt_pk_bf16_f32 v200, v220, v221\n\t"
    "v_cvt_pk_bf16_f32 v201, v222, v223\n\t"
    "v_add_f32 v230, v220, v221\n\t"
    "v_add_f32 v230, v230, v222\n\t"
    "v_add_f32 v230, v230, v223\n\t"
    "v_fma_f32 v220, v160, s48, v233\n\t"
    "v_fma_f32 v221, v161, s48, v233\n\t"
    "v_fma_f32 v222, v162, s48, v233\n\t"
    "v_fma_f32 v223, v163, s48, v233\n\t"
    "v_exp_f32 v220, v220\n\t"
    "v_exp_f32 v221, v221\n\t"
    "v_exp_f32 v222, v222\n\t"
    "v_exp_f32 v223, v223\n\t"
    "v_cvt_pk_bf16_f32 v194, v224, v225\n\t"
    "v_cvt_pk_bf16_f32 v195, v226, v227\n\t"
    "v_add_f32 v228, v228, v224\n\t"
    "v_add_f32 v228, v228, v225\n\t"
    "v_add_f32 v228, v228, v226\n\t"
    "v_add_f32 v228, v228, v227\n\t"
    "v_fma_f32 v224, v164, s48, v234\n\t"
    "v_fma_f32 v225, v165, s48, v234\n\t"
    "v_fma_f32 v226, v166, s48, v234\n\t"
    "v_fma_f32 v227, v167, s48, v234\n\t"
    "v_exp_f32 v224, v224\n\t"
    "v_exp_f32 v225, v225\n\t"
    "v_exp_f32 v226, v226\n\t"
    "v_exp_f32 v227, v227\n\t"
    "v_cvt_pk_bf16_f32 v198, v220, v221\n\t"
    "v_cvt_pk_bf16_f32 v199, v222, v223\n\t"
    "v_add_f32 v229, v229, v220\n\t"
    "v_add_f32 v229, v229, v221\n\t"
    "v_add_f32 v229, v229, v222\n\t"
    "v_add_f32 v229, v229, v223\n\t"
    "v_fma_f32 v220, v168, s48, v232\n\t"
    "v_fma_f32 v221, v169, s48, v232\n\t"
    "v_fma_f32 v222, v170, s48, v232\n\t"
    "v_fma_f32 v223, v171, s48, v232\n\t"
    "v_exp_f32 v220, v220\n\t"
    "v_exp_f32 v221, v221\n\t"
    "v_exp_f32 v222, v222\n\t"
    "v_exp_f32 v223, v223\n\t"
    "v_cvt_pk_bf16_f32 v202, v224, v225\n\t"
    "v_cvt_pk_bf16_f32 v203, v226, v227\n\t"
    "v_add_f32 v230, v230, v224\n\t"
    "v_add_f32 v230, v230, v225\n\t"
    "v_add_f32 v230, v230, v226\n\t"
    "v_add_f32 v230, v230, v227\n\t"
    "v_fma_f32 v224, v172, s48, v233\n\t"
    "v_fma_f32 v225, v173, s48, v233\n\t"
    "v_fma_f32 v226, v174, s48, v233\n\t"
    "v_fma_f32 v227, v175, s48, v233\n\t"
    "v_exp_f32 v224, v224\n\t"
    "v_exp_f32 v225, v225\n\t"
    "v_exp_f32 v226, v226\n\t"
    "v_exp_f32 v227, v227\n\t"
    "v_cvt_pk_bf16_f32 v204, v220, v221\n\t"
    "v_cvt_pk_bf16_f32 v205, v222, v223\n\t"
    "v_add_f32 v228, v228, v220\n\t"
    "v_add_f32 v228, v228, v221\n\t"
    "v_add_f32 v228, v228, v222\n\t"
    "v_add_f32 v228, v228, v223\n\t"
    "v_fma_f32 v220, v176, s48, v234\n\t"
    "v_fma_f32 v221, v177, s48, v234\n\t"
    "v_fma_f32 v222, v178, s48, v234\n\t"
    "v_fma_f32 v223, v179, s48, v234\n\t"
    "v_exp_f32 v220, v220\n\t"
    "v_exp_f32 v221, v221\n\t"
    "v_exp_f32 v222, v222\n\t"
    "v_exp_f32 v223, v223\n\t"
    "v_cvt_pk_bf16_f32 v208, v224, v225\n\t"
    "v_cvt_pk_bf16_f32 v209, v226, v227\n\t"
    "v_add_f32 v229, v229, v224\n\t"
    "v_add_f32 v229, v229, v225\n\t"
    "v_add_f32 v229, v229, v226\n\t"
    "v_add_f32 v229, v229, v227\n\t"
    "v_fma_f32 v224, v180, s48, v232\n\t"
    "v_fma_f32 v225, v181, s48, v232\n\t"
    "v_fma_f32 v226, v182, s48, v232\n\t"
    "v_fma_f32 v227, v183, s48, v232\n\t"
    "v_exp_f32 v224, v224\n\t"
    "v_exp_f32 v225, v225\n\t"
    "v_exp_f32 v226, v226\n\t"
    "v_exp_f32 v227, v227\n\t"
    "v_cvt_pk_bf16_f32 v212, v220, v221\n\t"
    "v_cvt_pk_bf16_f32 v213, v222, v223\n\t"
    "v_add_f32 v230, v230, v220\n\t"
    "v_add_f32 v230, v230, v221\n\t"
    "v_add_f32 v230, v230, v222\n\t"
    "v_add_f32 v230, v230, v223\n\t"
    "v_fma_f32 v220, v184, s48, v233\n\t"
    "v_fma_f32 v221, v185, s48, v233\n\t"
    "v_fma_f32 v222, v186, s48, v233\n\t"
    "v_fma_f32 v223, v187, s48, v233\n\t"
    "v_exp_f32 v220, v220\n\t"
    "v_exp_f32 v221, v221\n\t"
    "v_exp_f32 v222, v222\n\t"
    "v_exp_f32 v223, v223\n\t"
    "v_cvt_pk_bf16_f32 v206, v224, v225\n\t"
    "v_cvt_pk_bf16_f32 v207, v226, v227\n\t"
    "v_add_f32 v228, v228, v224\n\t"
    "v_add_f32 v228, v228, v225\n\t"
    "v_add_f32 v228, v228, v226\n\t"
    "v_add_f32 v228, v228, v227\n\t"
    "v_fma_f32 v224, v188, s48, v234\n\t"
    "v_fma_f32 v225, v189, s48, v234\n\t"
    "v_fma_f32 v226, v190, s48, v234\n\t"
    "v_fma_f32 v227, v191, s48, v234\n\t"
    "v_exp_f32 v224, v224\n\t"
    "v_exp_f32 v225, v225\n\t"
    "v_exp_f32 v226, v226\n\t"
    "v_exp_f32 v227, v227\n\t"
    "v_cvt_pk_bf16_f32 v210, v220, v221\n\t"
    "v_cvt_pk_bf16_f32 v211, v222, v223\n\t"
    "v_add_f32 v229, v229, v220\n\t"
    "v_add_f32 v229, v229, v221\n\t"
    "v_add_f32 v229, v229, v222\n\t"
    "v_add_f32 v229, v229, v223\n\t"
    "v_cvt_pk_bf16_f32 v214, v224, v225\n\t"
    "v_cvt_pk_bf16_f32 v215, v226, v227\n\t"
    "v_add_f32 v230, v230, v224\n\t"
    "v_add_f32 v230, v230, v225\n\t"
    "v_add_f32 v230, v230, v226\n\t"
    "v_add_f32 v230, v230, v227\n\t"
    "v_max3_f32 v220, v228, v229, v230\n\t"
    "v_cmp_lt_f32 vcc, s49, v220\n\t"
    "s_cbranch_vccnz LFBA4%=\n\t"
    "LRETA4%=:\n\t"
    "v_add_f32 v235, v235, v228\n\t"
    "v_add_f32 v236, v236, v229\n\t"
    "v_add_f32 v237, v237, v230\n\t"
    "s_waitcnt vmcnt(0)\n\t"
    "s_barrier\n\t"
    "s_setprio 1\n\t"
    "ds_read_b64_tr_b16 v[144:145], v240 offset:32768\n\t"
    "ds_read_b64_tr_b16 v[146:147], v240 offset:36864\n\t"
    "ds_read_b64_tr_b16 v[148:149], v240 offset:40960\n\t"
    "ds_read_b64_tr_b16 v[150:151], v240 offset:45056\n\t"
    "ds_read_b64_tr_b16 v[152:153], v241 offset:40960\n\t"
    "ds_read_b64_tr_b16 v[154:155], v241 offset:45056\n\t"
    "ds_read_b64_tr_b16 v[156:157], v241 offset:32768\n\t"
    "ds_read_b64_tr_b16 v[158:159], v241 offset:36864\n\t"
    "s_waitcnt lgkmcnt(6)\n\t"
    "v_mfma_f32_16x16x32_bf16 v[0:3], v[144:147], v[192:195], v[0:3]\n\t"
    "ds_read_b64_tr_b16 v[160:161], v240 offset:33280\n\t"
    "ds_read_b64_tr_b16 v[162:163], v240 offset:37376\n\t"
    "s_waitcnt lgkmcnt(6)\n\t"
    "v_mfma_f32_16x16x32_bf16 v[0:3], v[148:151], v[204:207], v[0:3]\n\t"
    "ds_read_b64_tr_b16 v[164:165], v240 offset:41472\n\t"
    "ds_read_b64_tr_b16 v[166:167], v240 offset:45568\n\t"
    "v_mfma_f32_16x16x32_bf16 v[4:7], v[148:151], v[208:211], v[4:7]\n\t"
    "ds_read_b64_tr_b16 v[168:169], v241 offset:41472\n\t"
    "ds_read_b64_tr_b16 v[170:171], v241 offset:45568\n\t"
    "v_mfma_f32_16x16x32_bf16 v[4:7], v[144:147], v[196:199], v[4:7]\n\t"
    "ds_read_b64_tr_b16 v[172:173], v241 offset:33280\n\t"
    "ds_read_b64_tr_b16 v[174:175], v241 offset:37376\n\t"
    "v_mfma_f32_16x16x32_bf16 v[8:11], v[144:147], v[200:203], v[8:11]\n\t"
    "s_add_u32 s58, s50, 81920\n\t"
    "v_mfma_f32_16x16x32_bf16 v[8:11], v[148:151], v[212:215], v[8:11]\n\t"
    "s_waitcnt lgkmcnt(10)\n\t"
    "v_mfma_f32_16x16x32_bf16 v[20:23], v[152:155], v[212:215], v[20:23]\n\t"
    "s_waitcnt lgkmcnt(8)\n\t"
    "v_mfma_f32_16x16x32_bf16 v[20:23], v[156:159], v[200:203], v[20:23]\n\t"
    "s_add_u32 m0, s52, 49152\n\t"
    "v_mfma_f32_16x16x32_bf16 v[16:19], v[156:159], v[196:199], v[16:19]\n\t"
    "ds_read_b64_tr_b16 v[176:177], v240 offset:33792\n\t"
    "ds_read_b64_tr_b16 v[178:179], v240 offset:37888\n\t"
    "v_mfma_f32_16x16x32_bf16 v[16:19], v[152:155], v[208:211], v[16:19]\n\t"
    "ds_read_b64_tr_b16 v[144:145], v240 offset:41984\n\t"
    "ds_read_b64_tr_b16 v[146:147], v240 offset:46080\n\t"
    "v_mfma_f32_16x16x32_bf16 v[12:15], v[152:155], v[204:207], v[12:15]\n\t"
    "buffer_load_dwordx4 v244, s[40:43], s58 offen offset:0 nt lds\n\t"
    "v_mfma_f32_16x16x32_bf16 v[12:15], v[156:159], v[192:195], v[12:15]\n\t"
    "s_waitcnt lgkmcnt(10)\n\t"
    "v_mfma_f32_16x16x32_bf16 v[24:27], v[160:163], v[192:195], v[24:27]\n\t"
    "s_waitcnt lgkmcnt(8)\n\t"
    "v_mfma_f32_16x16x32_bf16 v[24:27], v[164:167], v[204:207], v[24:27]\n\t"
    "buffer_load_dwordx4 v244, s[40:43], s58 offen offset:2048 nt lds\n\t"
    "v_mfma_f32_16x16x32_bf16 v[28:31], v[164:167], v[208:211], v[28:31]\n\t"
    "ds_read_b64_tr_b16 v[148:149], v241 offset:41984\n\t"
    "ds_read_b64_tr_b16 v[150:151], v241 offset:46080\n\t"
    "v_mfma_f32_16x16x32_bf16 v[28:31], v[160:163], v[196:199], v[28:31]\n\t"
    "ds_read_b64_tr_b16 v[152:153], v241 offset:33792\n\t"
    "ds_read_b64_tr_b16 v[154:155], v241 offset:37888\n\t"
    "v_mfma_f32_16x16x32_bf16 v[32:35], v[160:163], v[200:203], v[32:35]\n\t"
    "s_add_u32 m0, s52, 50048\n\t"
    "v_mfma_f32_16x16x32_bf16 v[32:35], v[164:167], v[212:215], v[32:35]\n\t"
    "s_waitcnt lgkmcnt(10)\n\t"
    "v_mfma_f32_16x16x32_bf16 v[44:47], v[168:171], v[212:215], v[44:47]\n\t"
    "s_waitcnt lgkmcnt(8)\n\t"
    "v_mfma_f32_16x16x32_bf16 v[44:47], v[172:175], v[200:203], v[44:47]\n\t"
    "buffer_load_dwordx4 v244, s[40:43], s58 offen offset:128 nt lds\n\t"
    "v_mfma_f32_16x16x32_bf16 v[40:43], v[172:175], v[196:199], v[40:43]\n\t"
    "ds_read_b64_tr_b16 v[156:157], v240 offset:34304\n\t"
    "ds_read_b64_tr_b16 v[158:159], v240 offset:38400\n\t"
    "v_mfma_f32_16x16x32_bf16 v[40:43], v[168:171], v[208:211], v[40:43]\n\t"
    "ds_read_b64_tr_b16 v[160:161], v240 offset:42496\n\t"
    "ds_read_b64_tr_b16 v[162:163], v240 offset:46592\n\t"
    "v_mfma_f32_16x16x32_bf16 v[36:39], v[168:171], v[204:207], v[36:39]\n\t"
    "buffer_load_dwordx4 v244, s[40:43], s58 offen offset:2176 nt lds\n\t"
    "v_mfma_f32_16x16x32_bf16 v[36:39], v[172:175], v[192:195], v[36:39]\n\t"
    "s_waitcnt lgkmcnt(10)\n\t"
    "v_mfma_f32_16x16x32_bf16 v[48:51], v[176:179], v[192:195], v[48:51]\n\t"
    "s_waitcnt lgkmcnt(8)\n\t"
    "v_mfma_f32_16x16x32_bf16 v[48:51], v[144:147], v[204:207], v[48:51]\n\t"
    "s_add_u32 s57, s50, 98304\n\t"
    "v_mfma_f32_16x16x32_bf16 v[52:55], v[144:147], v[208:211], v[52:55]\n\t"
    "ds_read_b64_tr_b16 v[164:165], v241 offset:42496\n\t"
    "ds_read_b64_tr_b16 v[166:167], v241 offset:46592\n\t"
    "v_mfma_f32_16x16x32_bf16 v[52:55], v[176:179], v[196:199], v[52:55]\n\t"
    "ds_read_b64_tr_b16 v[168:169], v241 offset:34304\n\t"
    "ds_read_b64_tr_b16 v[170:171], v241 offset:38400\n\t"
    "v_mfma_f32_16x16x32_bf16 v[56:59], v[176:179], v[200:203], v[56:59]\n\t"
    "s_add_u32 m0, s52, 0\n\t"
    "v_mfma_f32_16x16x32_bf16 v[56:59], v[144:147], v[212:215], v[56:59]\n\t"
    "s_waitcnt lgkmcnt(10)\n\t"
    "v_mfma_f32_16x16x32_bf16 v[68:71], v[148:151], v[212:215], v[68:71]\n\t"
    "s_waitcnt lgkmcnt(8)\n\t"
    "v_mfma_f32_16x16x32_bf16 v[68:71], v[152:155], v[200:203], v[68:71]\n\t"
    "buffer_load_dwordx4 v242, s[36:39], s57 offen offset:0 nt lds\n\t"
    "v_mfma_f32_16x16x32_bf16 v[64:67], v[152:155], v[196:199], v[64:67]\n\t"
    "ds_read_b128 v[180:183], v238 offset:16384\n\t"
    "v_mfma_f32_16x16x32_bf16 v[64:67], v[148:151], v[208:211], v[64:67]\n\t"
    "ds_read_b128 v[184:187], v239 offset:16384\n\t"
    "v_mfma_f32_16x16x32_bf16 v[60:63], v[148:151], v[204:207], v[60:63]\n\t"
    "ds_read_b128 v[188:191], v238 offset:17408\n\t"
    "v_mfma_f32_16x16x32_bf16 v[60:63], v[152:155], v[192:195], v[60:63]\n\t"
    "ds_read_b128 v[216:219], v239 offset:17408\n\t"
    "s_waitcnt lgkmcnt(10)\n\t"
    "v_mfma_f32_16x16x32_bf16 v[72:75], v[156:159], v[192:195], v[72:75]\n\t"
    "s_waitcnt lgkmcnt(8)\n\t"
    "v_mfma_f32_16x16x32_bf16 v[72:75], v[160:163], v[204:207], v[72:75]\n\t"
    "buffer_load_dwordx4 v243, s[36:39], s57 offen offset:2048 nt lds\n\t"
    "v_mfma_f32_16x16x32_bf16 v[76:79], v[160:163], v[208:211], v[76:79]\n\t"
    "v_mfma_f32_16x16x32_bf16 v[76:79], v[156:159], v[196:199], v[76:79]\n\t"
    "s_add_u32 m0, s52, 896\n\t"
    "v_mfma_f32_16x16x32_bf16 v[80:83], v[156:159], v[200:203], v[80:83]\n\t"
    "buffer_load_dwordx4 v242, s[36:39], s57 offen offset:128 nt lds\n\t"
    "v_mfma_f32_16x16x32_bf16 v[80:83], v[160:163], v[212:215], v[80:83]\n\t"
    "s_waitcnt lgkmcnt(6)\n\t"
    "v_mfma_f32_16x16x32_bf16 v[92:95], v[164:167], v[212:215], v[92:95]\n\t"
    "s_waitcnt lgkmcnt(4)\n\t"
    "v_mfma_f32_16x16x32_bf16 v[92:95], v[168:171], v[200:203], v[92:95]\n\t"
    "buffer_load_dwordx4 v243, s[36:39], s57 offen offset:2176 nt lds\n\t"
    "v_mfma_f32_16x16x32_bf16 v[88:91], v[168:171], v[196:199], v[88:91]\n\t"
    "v_mfma_f32_16x16x32_bf16 v[88:91], v[164:167], v[208:211], v[88:91]\n\t"
    "v_mfma_f32_16x16x32_bf16 v[84:87], v[164:167], v[204:207], v[84:87]\n\t"
    "v_mfma_f32_16x16x32_bf16 v[84:87], v[168:171], v[192:195], v[84:87]\n\t"
    "ds_read_b128 v[204:207], v239 offset:21504\n\t"
    "ds_read_b128 v[200:203], v238 offset:21504\n\t"
    "s_waitcnt lgkmcnt(5)\n\t"
    "v_mfma_f32_16x16x32_bf16 v[144:147], v[180:183], v[96:99], 0\n\t"
    "ds_read_b128 v[196:199], v239 offset:20480\n\t"
    "ds_read_b128 v[192:195], v238 offset:20480\n\t"
    "s_waitcnt lgkmcnt(6)\n\t"
    "v_mfma_f32_16x16x32_bf16 v[144:147], v[184:187], v[100:103], v[144:147]\n\t"
    "s_waitcnt lgkmcnt(5)\n\t"
    "v_mfma_f32_16x16x32_bf16 v[144:147], v[188:191], v[104:107], v[144:147]\n\t"
    "s_waitcnt lgkmcnt(4)\n\t"
    "v_mfma_f32_16x16x32_bf16 v[144:147], v[216:219], v[108:111], v[144:147]\n\t"
    "v_mfma_f32_16x16x32_bf16 v[148:151], v[216:219], v[124:127], 0\n\t"
    "v_mfma_f32_16x16x32_bf16 v[148:151], v[188:191], v[120:123], v[148:151]\n\t"
    "v_mfma_f32_16x16x32_bf16 v[148:151], v[184:187], v[116:119], v[148:151]\n\t"
    "v_mfma_f32_16x16x32_bf16 v[148:151], v[180:183], v[112:115], v[148:151]\n\t"
    "v_mfma_f32_16x16x32_bf16 v[152:155], v[180:183], v[128:131], 0\n\t"
    "ds_read_b128 v[180:183], v238 offset:24576\n\t"
    "v_mfma_f32_16x16x32_bf16 v[152:155], v[184:187], v[132:135], v[152:155]\n\t"
    "ds_read_b128 v[184:187], v239 offset:24576\n\t"
    "v_mfma_f32_16x16x32_bf16 v[152:155], v[188:191], v[136:139], v[152:155]\n\t"
    "ds_read_b128 v[188:191], v238 offset:25600\n\t"
    "v_mfma_f32_16x16x32_bf16 v[152:155], v[216:219], v[140:143], v[152:155]\n\t"
    "ds_read_b128 v[216:219], v239 offset:25600\n\t"
    "s_waitcnt lgkmcnt(7)\n\t"
    "v_mfma_f32_16x16x32_bf16 v[164:167], v[204:207], v[140:143], 0\n\t"
    "s_waitcnt lgkmcnt(6)\n\t"
    "v_mfma_f32_16x16x32_bf16 v[164:167], v[200:203], v[136:139], v[164:167]\n\t"
    "s_waitcnt lgkmcnt(5)\n\t"
    "v_mfma_f32_16x16x32_bf16 v[164:167], v[196:199], v[132:135], v[164:167]\n\t"
    "s_waitcnt lgkmcnt(4)\n\t"
    "v_mfma_f32_16x16x32_bf16 v[164:167], v[192:195], v[128:131], v[164:167]\n\t"
    "v_mfma_f32_16x16x32_bf16 v[160:163], v[192:195], v[112:115], 0\n\t"
    "v_mfma_f32_16x16x32_bf16 v[160:163], v[196:199], v[116:119], v[160:163]\n\t"
    "v_mfma_f32_16x16x32_bf16 v[160:163], v[200:203], v[120:123], v[160:163]\n\t"
    "v_mfma_f32_16x16x32_bf16 v[160:163], v[204:207], v[124:127], v[160:163]\n\t"
    "v_mfma_f32_16x16x32_bf16 v[156:159], v[204:207], v[108:111], 0\n\t"
    "ds_read_b128 v[204:207], v239 offset:29696\n\t"
    "v_mfma_f32_16x16x32_bf16 v[156:159], v[200:203], v[104:107], v[156:159]\n\t"
    "ds_read_b128 v[200:203], v238 offset:29696\n\t"
    "v_mfma_f32_16x16x32_bf16 v[156:159], v[196:199], v[100:103], v[156:159]\n\t"
    "ds_read_b128 v[196:199], v239 offset:28672\n\t"
    "v_mfma_f32_16x16x32_bf16 v[156:159], v[192:195], v[96:99], v[156:159]\n\t"
    "ds_read_b128 v[192:195], v238 offset:28672\n\t"
    "s_waitcnt lgkmcnt(7)\n\t"
    "v_mfma_f32_16x16x32_bf16 v[168:171], v[180:183], v[96:99], 0\n\t"
    "s_waitcnt lgkmcnt(6)\n\t"
    "v_mfma_f32_16x16x32_bf16 v[168:171], v[184:187], v[100:103], v[168:171]\n\t"
    "s_waitcnt lgkmcnt(5)\n\t"
    "v_mfma_f32_16x16x32_bf16 v[168:171], v[188:191], v[104:107], v[168:171]\n\t"
    "s_waitcnt lgkmcnt(4)\n\t"
    "v_mfma_f32_16x16x32_bf16 v[168:171], v[216:219], v[108:111], v[168:171]\n\t"
    "v_mfma_f32_16x16x32_bf16 v[172:175], v[216:219], v[124:127], 0\n\t"
    "v_mfma_f32_16x16x32_bf16 v[172:175], v[188:191], v[120:123], v[172:175]\n\t"
    "v_mfma_f32_16x16x32_bf16 v[172:175], v[184:187], v[116:119], v[172:175]\n\t"
    "v_mfma_f32_16x16x32_bf16 v[172:175], v[180:183], v[112:115], v[172:175]\n\t"
    "v_mfma_f32_16x16x32_bf16 v[176:179], v[180:183], v[128:131], 0\n\t"
    "v_mfma_f32_16x16x32_bf16 v[176:179], v[184:187], v[132:135], v[176:179]\n\t"
    "v_mfma_f32_16x16x32_bf16 v[176:179], v[188:191], v[136:139], v[176:179]\n\t"
    "v_mfma_f32_16x16x32_bf16 v[176:179], v[216:219], v[140:143], v[176:179]\n\t"
    "s_waitcnt lgkmcnt(3)\n\t"
    "v_mfma_f32_16x16x32_bf16 v[188:191], v[204:207], v[140:143], 0\n\t"
    "s_waitcnt lgkmcnt(2)\n\t"
    "v_mfma_f32_16x16x32_bf16 v[188:191], v[200:203], v[136:139], v[188:191]\n\t"
    "s_waitcnt lgkmcnt(1)\n\t"
    "v_mfma_f32_16x16x32_bf16 v[188:191], v[196:199], v[132:135], v[188:191]\n\t"
    "s_waitcnt lgkmcnt(0)\n\t"
    "v_mfma_f32_16x16x32_bf16 v[188:191], v[192:195], v[128:131], v[188:191]\n\t"
    "v_mfma_f32_16x16x32_bf16 v[184:187], v[192:195], v[112:115], 0\n\t"
    "v_mfma_f32_16x16x32_bf16 v[184:187], v[196:199], v[116:119], v[184:187]\n\t"
    "v_mfma_f32_16x16x32_bf16 v[184:187], v[200:203], v[120:123], v[184:187]\n\t"
    "v_mfma_f32_16x16x32_bf16 v[184:187], v[204:207], v[124:127], v[184:187]\n\t"
    "v_mfma_f32_16x16x32_bf16 v[180:183], v[204:207], v[108:111], 0\n\t"
    "v_mfma_f32_16x16x32_bf16 v[180:183], v[200:203], v[104:107], v[180:183]\n\t"
    "v_mfma_f32_16x16x32_bf16 v[180:183], v[196:199], v[100:103], v[180:183]\n\t"
    "v_mfma_f32_16x16x32_bf16 v[180:183], v[192:195], v[96:99], v[180:183]\n\t"
    "s_waitcnt lgkmcnt(0)\n\t"
    "s_barrier\n\t"
    "s_setprio 0\n\t"
    "v_fma_f32 v220, v144, s48, v232\n\t"
    "v_fma_f32 v221, v145, s48, v232\n\t"
    "v_fma_f32 v222, v146, s48, v232\n\t"
    "v_fma_f32 v223, v147, s48, v232\n\t"
    "v_exp_f32 v220, v220\n\t"
    "v_exp_f32 v221, v221\n\t"
    "v_exp_f32 v222, v222\n\t"
    "v_exp_f32 v223, v223\n\t"
    "v_fma_f32 v224, v148, s48, v233\n\t"
    "v_fma_f32 v225, v149, s48, v233\n\t"
    "v_fma_f32 v226, v150, s48, v233\n\t"
    "v_fma_f32 v227, v151, s48, v233\n\t"
    "v_exp_f32 v224, v224\n\t"
    "v_exp_f32 v225, v225\n\t"
    "v_exp_f32 v226, v226\n\t"
    "v_exp_f32 v227, v227\n\t"
    "v_cvt_pk_bf16_f32 v192, v220, v221\n\t"
    "v_cvt_pk_bf16_f32 v193, v222, v223\n\t"
    "v_add_f32 v228, v220, v221\n\t"
    "v_add_f32 v228, v228, v222\n\t"
    "v_add_f32 v228, v228, v223\n\t"
    "v_fma_f32 v220, v152, s48, v234\n\t"
    "v_fma_f32 v221, v153, s48, v234\n\t"
    "v_fma_f32 v222, v154, s48, v234\n\t"
    "v_fma_f32 v223, v155, s48, v234\n\t"
    "v_exp_f32 v220, v220\n\t"
    "v_exp_f32 v221, v221\n\t"
    "v_exp_f32 v222, v222\n\t"
    "v_exp_f32 v223, v223\n\t"
    "v_cvt_pk_bf16_f32 v196, v224, v225\n\t"
    "v_cvt_pk_bf16_f32 v197, v226, v227\n\t"
    "v_add_f32 v229, v224, v225\n\t"
    "v_add_f32 v229, v229, v226\n\t"
    "v_add_f32 v229, v229, v227\n\t"
    "v_fma_f32 v224, v156, s48, v232\n\t"
    "v_fma_f32 v225, v157, s48, v232\n\t"
    "v_fma_f32 v226, v158, s48, v232\n\t"
    "v_fma_f32 v227, v159, s48, v232\n\t"
    "v_exp_f32 v224, v224\n\t"
    "v_exp_f32 v225, v225\n\t"
    "v_exp_f32 v226, v226\n\t"
    "v_exp_f32 v227, v227\n\t"
    "v_cvt_pk_bf16_f32 v200, v220, v221\n\t"
    "v_cvt_pk_bf16_f32 v201, v222, v223\n\t"
    "v_add_f32 v230, v220, v221\n\t"
    "v_add_f32 v230, v230, v222\n\t"
    "v_add_f32 v230, v230, v223\n\t"
    "v_fma_f32 v220, v160, s48, v233\n\t"
    "v_fma_f32 v221, v161, s48, v233\n\t"
    "v_fma_f32 v222, v162, s48, v233\n\t"
    "v_fma_f32 v223, v163, s48, v233\n\t"
    "v_exp_f32 v220, v220\n\t"
    "v_exp_f32 v221, v221\n\t"
    "v_exp_f32 v222, v222\n\t"
    "v_exp_f32 v223, v223\n\t"
    "v_cvt_pk_bf16_f32 v194, v224, v225\n\t"
    "v_cvt_pk_bf16_f32 v195, v226, v227\n\t"
    "v_add_f32 v228, v228, v224\n\t"
    "v_add_f32 v228, v228, v225\n\t"
    "v_add_f32 v228, v228, v226\n\t"
    "v_add_f32 v228, v228, v227\n\t"
    "v_fma_f32 v224, v164, s48, v234\n\t"
    "v_fma_f32 v225, v165, s48, v234\n\t"
    "v_fma_f32 v226, v166, s48, v234\n\t"
    "v_fma_f32 v227, v167, s48, v234\n\t"
    "v_exp_f32 v224, v224\n\t"
    "v_exp_f32 v225, v225\n\t"
    "v_exp_f32 v226, v226\n\t"
    "v_exp_f32 v227, v227\n\t"
    "v_cvt_pk_bf16_f32 v198, v220, v221\n\t"
    "v_cvt_pk_bf16_f32 v199, v222, v223\n\t"
    "v_add_f32 v229, v229, v220\n\t"
    "v_add_f32 v229, v229, v221\n\t"
    "v_add_f32 v229, v229, v222\n\t"
    "v_add_f32 v229, v229, v223\n\t"
    "v_fma_f32 v220, v168, s48, v232\n\t"
    "v_fma_f32 v221, v169, s48, v232\n\t"
    "v_fma_f32 v222, v170, s48, v232\n\t"
    "v_fma_f32 v223, v171, s48, v232\n\t"
    "v_exp_f32 v220, v220\n\t"
    "v_exp_f32 v221, v221\n\t"
    "v_exp_f32 v222, v222\n\t"
    "v_exp_f32 v223, v223\n\t"
    "v_cvt_pk_bf16_f32 v202, v224, v225\n\t"
    "v_cvt_pk_bf16_f32 v203, v226, v227\n\t"
    "v_add_f32 v230, v230, v224\n\t"
    "v_add_f32 v230, v230, v225\n\t"
    "v_add_f32 v230, v230, v226\n\t"
    "v_add_f32 v230, v230, v227\n\t"
    "v_fma_f32 v224, v172, s48, v233\n\t"
    "v_fma_f32 v225, v173, s48, v233\n\t"
    "v_fma_f32 v226, v174, s48, v233\n\t"
    "v_fma_f32 v227, v175, s48, v233\n\t"
    "v_exp_f32 v224, v224\n\t"
    "v_exp_f32 v225, v225\n\t"
    "v_exp_f32 v226, v226\n\t"
    "v_exp_f32 v227, v227\n\t"
    "v_cvt_pk_bf16_f32 v204, v220, v221\n\t"
    "v_cvt_pk_bf16_f32 v205, v222, v223\n\t"
    "v_add_f32 v228, v228, v220\n\t"
    "v_add_f32 v228, v228, v221\n\t"
    "v_add_f32 v228, v228, v222\n\t"
    "v_add_f32 v228, v228, v223\n\t"
    "v_fma_f32 v220, v176, s48, v234\n\t"
    "v_fma_f32 v221, v177, s48, v234\n\t"
    "v_fma_f32 v222, v178, s48, v234\n\t"
    "v_fma_f32 v223, v179, s48, v234\n\t"
    "v_exp_f32 v220, v220\n\t"
    "v_exp_f32 v221, v221\n\t"
    "v_exp_f32 v222, v222\n\t"
    "v_exp_f32 v223, v223\n\t"
    "v_cvt_pk_bf16_f32 v208, v224, v225\n\t"
    "v_cvt_pk_bf16_f32 v209, v226, v227\n\t"
    "v_add_f32 v229, v229, v224\n\t"
    "v_add_f32 v229, v229, v225\n\t"
    "v_add_f32 v229, v229, v226\n\t"
    "v_add_f32 v229, v229, v227\n\t"
    "v_fma_f32 v224, v180, s48, v232\n\t"
    "v_fma_f32 v225, v181, s48, v232\n\t"
    "v_fma_f32 v226, v182, s48, v232\n\t"
    "v_fma_f32 v227, v183, s48, v232\n\t"
    "v_exp_f32 v224, v224\n\t"
    "v_exp_f32 v225, v225\n\t"
    "v_exp_f32 v226, v226\n\t"
    "v_exp_f32 v227, v227\n\t"
    "v_cvt_pk_bf16_f32 v212, v220, v221\n\t"
    "v_cvt_pk_bf16_f32 v213, v222, v223\n\t"
    "v_add_f32 v230, v230, v220\n\t"
    "v_add_f32 v230, v230, v221\n\t"
    "v_add_f32 v230, v230, v222\n\t"
    "v_add_f32 v230, v230, v223\n\t"
    "v_fma_f32 v220, v184, s48, v233\n\t"
    "v_fma_f32 v221, v185, s48, v233\n\t"
    "v_fma_f32 v222, v186, s48, v233\n\t"
    "v_fma_f32 v223, v187, s48, v233\n\t"
    "v_exp_f32 v220, v220\n\t"
    "v_exp_f32 v221, v221\n\t"
    "v_exp_f32 v222, v222\n\t"
    "v_exp_f32 v223, v223\n\t"
    "v_cvt_pk_bf16_f32 v206, v224, v225\n\t"
    "v_cvt_pk_bf16_f32 v207, v226, v227\n\t"
    "v_add_f32 v228, v228, v224\n\t"
    "v_add_f32 v228, v228, v225\n\t"
    "v_add_f32 v228, v228, v226\n\t"
    "v_add_f32 v228, v228, v227\n\t"
    "v_fma_f32 v224, v188, s48, v234\n\t"
    "v_fma_f32 v225, v189, s48, v234\n\t"
    "v_fma_f32 v226, v190, s48, v234\n\t"
    "v_fma_f32 v227, v191, s48, v234\n\t"
    "v_exp_f32 v224, v224\n\t"
    "v_exp_f32 v225, v225\n\t"
    "v_exp_f32 v226, v226\n\t"
    "v_exp_f32 v227, v227\n\t"
    "v_cvt_pk_bf16_f32 v210, v220, v221\n\t"
    "v_cvt_pk_bf16_f32 v211, v222, v223\n\t"
    "v_add_f32 v229, v229, v220\n\t"
    "v_add_f32 v229, v229, v221\n\t"
    "v_add_f32 v229, v229, v222\n\t"
    "v_add_f32 v229, v229, v223\n\t"
    "v_cvt_pk_bf16_f32 v214, v224, v225\n\t"
    "v_cvt_pk_bf16_f32 v215, v226, v227\n\t"
    "v_add_f32 v230, v230, v224\n\t"
    "v_add_f32 v230, v230, v225\n\t"
    "v_add_f32 v230, v230, v226\n\t"
    "v_add_f32 v230, v230, v227\n\t"
    "v_max3_f32 v220, v228, v229, v230\n\t"
    "v_cmp_lt_f32 vcc, s49, v220\n\t"
    "s_cbranch_vccnz LFBA5%=\n\t"
    "LRETA5%=:\n\t"
    "v_add_f32 v235, v235, v228\n\t"
    "v_add_f32 v236, v236, v229\n\t"
    "v_add_f32 v237, v237, v230\n\t"
    "s_waitcnt vmcnt(0)\n\t"
    "s_barrier\n\t"
    "s_setprio 1\n\t"
    "ds_read_b64_tr_b16 v[144:145], v240 offset:49152\n\t"
    "ds_read_b64_tr_b16 v[146:147], v240 offset:53248\n\t"
    "ds_read_b64_tr_b16 v[148:149], v240 offset:57344\n\t"
    "ds_read_b64_tr_b16 v[150:151], v240 offset:61440\n\t"
    "ds_read_b64_tr_b16 v[152:153], v241 offset:57344\n\t"
    "ds_read_b64_tr_b16 v[154:155], v241 offset:61440\n\t"
    "ds_read_b64_tr_b16 v[156:157], v241 offset:49152\n\t"
    "ds_read_b64_tr_b16 v[158:159], v241 offset:53248\n\t"
    "s_waitcnt lgkmcnt(6)\n\t"
    "v_mfma_f32_16x16x32_bf16 v[0:3], v[144:147], v[192:195], v[0:3]\n\t"
    "ds_read_b64_tr_b16 v[160:161], v240 offset:49664\n\t"
    "ds_read_b64_tr_b16 v[162:163], v240 offset:53760\n\t"
    "s_waitcnt lgkmcnt(6)\n\t"
    "v_mfma_f32_16x16x32_bf16 v[0:3], v[148:151], v[204:207], v[0:3]\n\t"
    "ds_read_b64_tr_b16 v[164:165], v240 offset:57856\n\t"
    "ds_read_b64_tr_b16 v[166:167], v240 offset:61952\n\t"
    "v_mfma_f32_16x16x32_bf16 v[4:7], v[148:151], v[208:211], v[4:7]\n\t"
    "ds_read_b64_tr_b16 v[168:169], v241 offset:57856\n\t"
    "ds_read_b64_tr_b16 v[170:171], v241 offset:61952\n\t"
    "v_mfma_f32_16x16x32_bf16 v[4:7], v[144:147], v[196:199], v[4:7]\n\t"
    "ds_read_b64_tr_b16 v[172:173], v241 offset:49664\n\t"
    "ds_read_b64_tr_b16 v[174:175], v241 offset:53760\n\t"
    "v_mfma_f32_16x16x32_bf16 v[8:11], v[144:147], v[200:203], v[8:11]\n\t"
    "s_add_u32 s58, s50, 98304\n\t"
    "v_mfma_f32_16x16x32_bf16 v[8:11], v[148:151], v[212:215], v[8:11]\n\t"
    "s_waitcnt lgkmcnt(10)\n\t"
    "v_mfma_f32_16x16x32_bf16 v[20:23], v[152:155], v[212:215], v[20:23]\n\t"
    "s_waitcnt lgkmcnt(8)\n\t"
    "v_mfma_f32_16x16x32_bf16 v[20:23], v[156:159], v[200:203], v[20:23]\n\t"
    "s_add_u32 m0, s52, 32768\n\t"
    "v_mfma_f32_16x16x32_bf16 v[16:19], v[156:159], v[196:199], v[16:19]\n\t"
    "ds_read_b64_tr_b16 v[176:177], v240 offset:50176\n\t"
    "ds_read_b64_tr_b16 v[178:179], v240 offset:54272\n\t"
    "v_mfma_f32_16x16x32_bf16 v[16:19], v[152:155], v[208:211], v[16:19]\n\t"
    "ds_read_b64_tr_b16 v[144:145], v240 offset:58368\n\t"
    "ds_read_b64_tr_b16 v[146:147], v240 offset:62464\n\t"
    "v_mfma_f32_16x16x32_bf16 v[12:15], v[152:155], v[204:207], v[12:15]\n\t"
    "buffer_load_dwordx4 v244, s[40:43], s58 offen offset:0 nt lds\n\t"
    "v_mfma_f32_16x16x32_bf16 v[12:15], v[156:159], v[192:195], v[12:15]\n\t"
    "s_waitcnt lgkmcnt(10)\n\t"
    "v_mfma_f32_16x16x32_bf16 v[24:27], v[160:163], v[192:195], v[24:27]\n\t"
    "s_waitcnt lgkmcnt(8)\n\t"
    "v_mfma_f32_16x16x32_bf16 v[24:27], v[164:167], v[204:207], v[24:27]\n\t"
    "buffer_load_dwordx4 v244, s[40:43], s58 offen offset:2048 nt lds\n\t"
    "v_mfma_f32_16x16x32_bf16 v[28:31], v[164:167], v[208:211], v[28:31]\n\t"
    "ds_read_b64_tr_b16 v[148:149], v241 offset:58368\n\t"
    "ds_read_b64_tr_b16 v[150:151], v241 offset:62464\n\t"
    "v_mfma_f32_16x16x32_bf16 v[28:31], v[160:163], v[196:199], v[28:31]\n\t"
    "ds_read_b64_tr_b16 v[152:153], v241 offset:50176\n\t"
    "ds_read_b64_tr_b16 v[154:155], v241 offset:54272\n\t"
    "v_mfma_f32_16x16x32_bf16 v[32:35], v[160:163], v[200:203], v[32:35]\n\t"
    "s_add_u32 m0, s52, 33664\n\t"
    "v_mfma_f32_16x16x32_bf16 v[32:35], v[164:167], v[212:215], v[32:35]\n\t"
    "s_waitcnt lgkmcnt(10)\n\t"
    "v_mfma_f32_16x16x32_bf16 v[44:47], v[168:171], v[212:215], v[44:47]\n\t"
    "s_waitcnt lgkmcnt(8)\n\t"
    "v_mfma_f32_16x16x32_bf16 v[44:47], v[172:175], v[200:203], v[44:47]\n\t"
    "buffer_load_dwordx4 v244, s[40:43], s58 offen offset:128 nt lds\n\t"
    "v_mfma_f32_16x16x32_bf16 v[40:43], v[172:175], v[196:199], v[40:43]\n\t"
    "ds_read_b64_tr_b16 v[156:157], v240 offset:50688\n\t"
    "ds_read_b64_tr_b16 v[158:159], v240 offset:54784\n\t"
    "v_mfma_f32_16x16x32_bf16 v[40:43], v[168:171], v[208:211], v[40:43]\n\t"
    "ds_read_b64_tr_b16 v[160:161], v240 offset:58880\n\t"
    "ds_read_b64_tr_b16 v[162:163], v240 offset:62976\n\t"
    "v_mfma_f32_16x16x32_bf16 v[36:39], v[168:171], v[204:207], v[36:39]\n\t"
    "buffer_load_dwordx4 v244, s[40:43], s58 offen offset:2176 nt lds\n\t"
    "v_mfma_f32_16x16x32_bf16 v[36:39], v[172:175], v[192:195], v[36:39]\n\t"
    "s_waitcnt lgkmcnt(10)\n\t"
    "v_mfma_f32_16x16x32_bf16 v[48:51], v[176:179], v[192:195], v[48:51]\n\t"
    "s_waitcnt lgkmcnt(8)\n\t"
    "v_mfma_f32_16x16x32_bf16 v[48:51], v[144:147], v[204:207], v[48:51]\n\t"
    "s_add_u32 s57, s50, 114688\n\t"
    "v_mfma_f32_16x16x32_bf16 v[52:55], v[144:147], v[208:211], v[52:55]\n\t"
    "ds_read_b64_tr_b16 v[164:165], v241 offset:58880\n\t"
    "ds_read_b64_tr_b16 v[166:167], v241 offset:62976\n\t"
    "v_mfma_f32_16x16x32_bf16 v[52:55], v[176:179], v[196:199], v[52:55]\n\t"
    "ds_read_b64_tr_b16 v[168:169], v241 offset:50688\n\t"
    "ds_read_b64_tr_b16 v[170:171], v241 offset:54784\n\t"
    "v_mfma_f32_16x16x32_bf16 v[56:59], v[176:179], v[200:203], v[56:59]\n\t"
    "s_add_u32 m0, s52, 16384\n\t"
    "v_mfma_f32_16x16x32_bf16 v[56:59], v[144:147], v[212:215], v[56:59]\n\t"
    "s_waitcnt lgkmcnt(10)\n\t"
    "v_mfma_f32_16x16x32_bf16 v[68:71], v[148:151], v[212:215], v[68:71]\n\t"
    "s_waitcnt lgkmcnt(8)\n\t"
    "v_mfma_f32_16x16x32_bf16 v[68:71], v[152:155], v[200:203], v[68:71]\n\t"
    "buffer_load_dwordx4 v242, s[36:39], s57 offen offset:0 nt lds\n\t"
    "v_mfma_f32_16x16x32_bf16 v[64:67], v[152:155], v[196:199], v[64:67]\n\t"
    "ds_read_b128 v[180:183], v238 offset:0\n\t"
    "v_mfma_f32_16x16x32_bf16 v[64:67], v[148:151], v[208:211], v[64:67]\n\t"
    "ds_read_b128 v[184:187], v239 offset:0\n\t"
    "v_mfma_f32_16x16x32_bf16 v[60:63], v[148:151], v[204:207], v[60:63]\n\t"
    "ds_read_b128 v[188:191], v238 offset:1024\n\t"
    "v_mfma_f32_16x16x32_bf16 v[60:63], v[152:155], v[192:195], v[60:63]\n\t"
    "ds_read_b128 v[216:219], v239 offset:1024\n\t"
    "s_waitcnt lgkmcnt(10)\n\t"
    "v_mfma_f32_16x16x32_bf16 v[72:75], v[156:159], v[192:195], v[72:75]\n\t"
    "s_waitcnt lgkmcnt(8)\n\t"
    "v_mfma_f32_16x16x32_bf16 v[72:75], v[160:163], v[204:207], v[72:75]\n\t"
    "buffer_load_dwordx4 v243, s[36:39], s57 offen offset:2048 nt lds\n\t"
    "v_mfma_f32_16x16x32_bf16 v[76:79], v[160:163], v[208:211], v[76:79]\n\t"
    "v_mfma_f32_16x16x32_bf16 v[76:79], v[156:159], v[196:199], v[76:79]\n\t"
    "s_add_u32 m0, s52, 17280\n\t"
    "v_mfma_f32_16x16x32_bf16 v[80:83], v[156:159], v[200:203], v[80:83]\n\t"
    "buffer_load_dwordx4 v242, s[36:39], s57 offen offset:128 nt lds\n\t"
    "v_mfma_f32_16x16x32_bf16 v[80:83], v[160:163], v[212:215], v[80:83]\n\t"
    "s_waitcnt lgkmcnt(6)\n\t"
    "v_mfma_f32_16x16x32_bf16 v[92:95], v[164:167], v[212:215], v[92:95]\n\t"
    "s_waitcnt lgkmcnt(4)\n\t"
    "v_mfma_f32_16x16x32_bf16 v[92:95], v[168:171], v[200:203], v[92:95]\n\t"
    "buffer_load_dwordx4 v243, s[36:39], s57 offen offset:2176 nt lds\n\t"
    "v_mfma_f32_16x16x32_bf16 v[88:91], v[168:171], v[196:199], v[88:91]\n\t"
    "v_mfma_f32_16x16x32_bf16 v[88:91], v[164:167], v[208:211], v[88:91]\n\t"
    "v_mfma_f32_16x16x32_bf16 v[84:87], v[164:167], v[204:207], v[84:87]\n\t"
    "v_mfma_f32_16x16x32_bf16 v[84:87], v[168:171], v[192:195], v[84:87]\n\t"
    "ds_read_b128 v[204:207], v239 offset:5120\n\t"
    "ds_read_b128 v[200:203], v238 offset:5120\n\t"
    "s_waitcnt lgkmcnt(5)\n\t"
    "v_mfma_f32_16x16x32_bf16 v[144:147], v[180:183], v[96:99], 0\n\t"
    "ds_read_b128 v[196:199], v239 offset:4096\n\t"
    "ds_read_b128 v[192:195], v238 offset:4096\n\t"
    "s_waitcnt lgkmcnt(6)\n\t"
    "v_mfma_f32_16x16x32_bf16 v[144:147], v[184:187], v[100:103], v[144:147]\n\t"
    "s_waitcnt lgkmcnt(5)\n\t"
    "v_mfma_f32_16x16x32_bf16 v[144:147], v[188:191], v[104:107], v[144:147]\n\t"
    "s_waitcnt lgkmcnt(4)\n\t"
    "v_mfma_f32_16x16x32_bf16 v[144:147], v[216:219], v[108:111], v[144:147]\n\t"
    "v_mfma_f32_16x16x32_bf16 v[148:151], v[216:219], v[124:127], 0\n\t"
    "v_mfma_f32_16x16x32_bf16 v[148:151], v[188:191], v[120:123], v[148:151]\n\t"
    "v_mfma_f32_16x16x32_bf16 v[148:151], v[184:187], v[116:119], v[148:151]\n\t"
    "v_mfma_f32_16x16x32_bf16 v[148:151], v[180:183], v[112:115], v[148:151]\n\t"
    "v_mfma_f32_16x16x32_bf16 v[152:155], v[180:183], v[128:131], 0\n\t"
    "ds_read_b128 v[180:183], v238 offset:8192\n\t"
    "v_mfma_f32_16x16x32_bf16 v[152:155], v[184:187], v[132:135], v[152:155]\n\t"
    "ds_read_b128 v[184:187], v239 offset:8192\n\t"
    "v_mfma_f32_16x16x32_bf16 v[152:155], v[188:191], v[136:139], v[152:155]\n\t"
    "ds_read_b128 v[188:191], v238 offset:9216\n\t"
    "v_mfma_f32_16x16x32_bf16 v[152:155], v[216:219], v[140:143], v[152:155]\n\t"
    "ds_read_b128 v[216:219], v239 offset:9216\n\t"
    "s_waitcnt lgkmcnt(7)\n\t"
    "v_mfma_f32_16x16x32_bf16 v[164:167], v[204:207], v[140:143], 0\n\t"
    "s_waitcnt lgkmcnt(6)\n\t"
    "v_mfma_f32_16x16x32_bf16 v[164:167], v[200:203], v[136:139], v[164:167]\n\t"
    "s_waitcnt lgkmcnt(5)\n\t"
    "v_mfma_f32_16x16x32_bf16 v[164:167], v[196:199], v[132:135], v[164:167]\n\t"
    "s_waitcnt lgkmcnt(4)\n\t"
    "v_mfma_f32_16x16x32_bf16 v[164:167], v[192:195], v[128:131], v[164:167]\n\t"
    "v_mfma_f32_16x16x32_bf16 v[160:163], v[192:195], v[112:115], 0\n\t"
    "v_mfma_f32_16x16x32_bf16 v[160:163], v[196:199], v[116:119], v[160:163]\n\t"
    "v_mfma_f32_16x16x32_bf16 v[160:163], v[200:203], v[120:123], v[160:163]\n\t"
    "v_mfma_f32_16x16x32_bf16 v[160:163], v[204:207], v[124:127], v[160:163]\n\t"
    "v_mfma_f32_16x16x32_bf16 v[156:159], v[204:207], v[108:111], 0\n\t"
    "ds_read_b128 v[204:207], v239 offset:13312\n\t"
    "v_mfma_f32_16x16x32_bf16 v[156:159], v[200:203], v[104:107], v[156:159]\n\t"
    "ds_read_b128 v[200:203], v238 offset:13312\n\t"
    "v_mfma_f32_16x16x32_bf16 v[156:159], v[196:199], v[100:103], v[156:159]\n\t"
    "ds_read_b128 v[196:199], v239 offset:12288\n\t"
    "v_mfma_f32_16x16x32_bf16 v[156:159], v[192:195], v[96:99], v[156:159]\n\t"
    "ds_read_b128 v[192:195], v238 offset:12288\n\t"
    "s_waitcnt lgkmcnt(7)\n\t"
    "v_mfma_f32_16x16x32_bf16 v[168:171], v[180:183], v[96:99], 0\n\t"
    "s_waitcnt lgkmcnt(6)\n\t"
    "v_mfma_f32_16x16x32_bf16 v[168:171], v[184:187], v[100:103], v[168:171]\n\t"
    "s_waitcnt lgkmcnt(5)\n\t"
    "v_mfma_f32_16x16x32_bf16 v[168:171], v[188:191], v[104:107], v[168:171]\n\t"
    "s_waitcnt lgkmcnt(4)\n\t"
    "v_mfma_f32_16x16x32_bf16 v[168:171], v[216:219], v[108:111], v[168:171]\n\t"
    "v_mfma_f32_16x16x32_bf16 v[172:175], v[216:219], v[124:127], 0\n\t"
    "v_mfma_f32_16x16x32_bf16 v[172:175], v[188:191], v[120:123], v[172:175]\n\t"
    "v_mfma_f32_16x16x32_bf16 v[172:175], v[184:187], v[116:119], v[172:175]\n\t"
    "v_mfma_f32_16x16x32_bf16 v[172:175], v[180:183], v[112:115], v[172:175]\n\t"
    "v_mfma_f32_16x16x32_bf16 v[176:179], v[180:183], v[128:131], 0\n\t"
    "v_mfma_f32_16x16x32_bf16 v[176:179], v[184:187], v[132:135], v[176:179]\n\t"
    "v_mfma_f32_16x16x32_bf16 v[176:179], v[188:191], v[136:139], v[176:179]\n\t"
    "v_mfma_f32_16x16x32_bf16 v[176:179], v[216:219], v[140:143], v[176:179]\n\t"
    "s_waitcnt lgkmcnt(3)\n\t"
    "v_mfma_f32_16x16x32_bf16 v[188:191], v[204:207], v[140:143], 0\n\t"
    "s_waitcnt lgkmcnt(2)\n\t"
    "v_mfma_f32_16x16x32_bf16 v[188:191], v[200:203], v[136:139], v[188:191]\n\t"
    "s_waitcnt lgkmcnt(1)\n\t"
    "v_mfma_f32_16x16x32_bf16 v[188:191], v[196:199], v[132:135], v[188:191]\n\t"
    "s_waitcnt lgkmcnt(0)\n\t"
    "v_mfma_f32_16x16x32_bf16 v[188:191], v[192:195], v[128:131], v[188:191]\n\t"
    "v_mfma_f32_16x16x32_bf16 v[184:187], v[192:195], v[112:115], 0\n\t"
    "v_mfma_f32_16x16x32_bf16 v[184:187], v[196:199], v[116:119], v[184:187]\n\t"
    "v_mfma_f32_16x16x32_bf16 v[184:187], v[200:203], v[120:123], v[184:187]\n\t"
    "v_mfma_f32_16x16x32_bf16 v[184:187], v[204:207], v[124:127], v[184:187]\n\t"
    "v_mfma_f32_16x16x32_bf16 v[180:183], v[204:207], v[108:111], 0\n\t"
    "v_mfma_f32_16x16x32_bf16 v[180:183], v[200:203], v[104:107], v[180:183]\n\t"
    "v_mfma_f32_16x16x32_bf16 v[180:183], v[196:199], v[100:103], v[180:183]\n\t"
    "v_mfma_f32_16x16x32_bf16 v[180:183], v[192:195], v[96:99], v[180:183]\n\t"
    "s_waitcnt lgkmcnt(0)\n\t"
    "s_barrier\n\t"
    "s_setprio 0\n\t"
    "v_fma_f32 v220, v144, s48, v232\n\t"
    "v_fma_f32 v221, v145, s48, v232\n\t"
    "v_fma_f32 v222, v146, s48, v232\n\t"
    "v_fma_f32 v223, v147, s48, v232\n\t"
    "v_exp_f32 v220, v220\n\t"
    "v_exp_f32 v221, v221\n\t"
    "v_exp_f32 v222, v222\n\t"
    "v_exp_f32 v223, v223\n\t"
    "v_fma_f32 v224, v148, s48, v233\n\t"
    "v_fma_f32 v225, v149, s48, v233\n\t"
    "v_fma_f32 v226, v150, s48, v233\n\t"
    "v_fma_f32 v227, v151, s48, v233\n\t"
    "v_exp_f32 v224, v224\n\t"
    "v_exp_f32 v225, v225\n\t"
    "v_exp_f32 v226, v226\n\t"
    "v_exp_f32 v227, v227\n\t"
    "v_cvt_pk_bf16_f32 v192, v220, v221\n\t"
    "v_cvt_pk_bf16_f32 v193, v222, v223\n\t"
    "v_add_f32 v228, v220, v221\n\t"
    "v_add_f32 v228, v228, v222\n\t"
    "v_add_f32 v228, v228, v223\n\t"
    "v_fma_f32 v220, v152, s48, v234\n\t"
    "v_fma_f32 v221, v153, s48, v234\n\t"
    "v_fma_f32 v222, v154, s48, v234\n\t"
    "v_fma_f32 v223, v155, s48, v234\n\t"
    "v_exp_f32 v220, v220\n\t"
    "v_exp_f32 v221, v221\n\t"
    "v_exp_f32 v222, v222\n\t"
    "v_exp_f32 v223, v223\n\t"
    "v_cvt_pk_bf16_f32 v196, v224, v225\n\t"
    "v_cvt_pk_bf16_f32 v197, v226, v227\n\t"
    "v_add_f32 v229, v224, v225\n\t"
    "v_add_f32 v229, v229, v226\n\t"
    "v_add_f32 v229, v229, v227\n\t"
    "v_fma_f32 v224, v156, s48, v232\n\t"
    "v_fma_f32 v225, v157, s48, v232\n\t"
    "v_fma_f32 v226, v158, s48, v232\n\t"
    "v_fma_f32 v227, v159, s48, v232\n\t"
    "v_exp_f32 v224, v224\n\t"
    "v_exp_f32 v225, v225\n\t"
    "v_exp_f32 v226, v226\n\t"
    "v_exp_f32 v227, v227\n\t"
    "v_cvt_pk_bf16_f32 v200, v220, v221\n\t"
    "v_cvt_pk_bf16_f32 v201, v222, v223\n\t"
    "v_add_f32 v230, v220, v221\n\t"
    "v_add_f32 v230, v230, v222\n\t"
    "v_add_f32 v230, v230, v223\n\t"
    "v_fma_f32 v220, v160, s48, v233\n\t"
    "v_fma_f32 v221, v161, s48, v233\n\t"
    "v_fma_f32 v222, v162, s48, v233\n\t"
    "v_fma_f32 v223, v163, s48, v233\n\t"
    "v_exp_f32 v220, v220\n\t"
    "v_exp_f32 v221, v221\n\t"
    "v_exp_f32 v222, v222\n\t"
    "v_exp_f32 v223, v223\n\t"
    "v_cvt_pk_bf16_f32 v194, v224, v225\n\t"
    "v_cvt_pk_bf16_f32 v195, v226, v227\n\t"
    "v_add_f32 v228, v228, v224\n\t"
    "v_add_f32 v228, v228, v225\n\t"
    "v_add_f32 v228, v228, v226\n\t"
    "v_add_f32 v228, v228, v227\n\t"
    "v_fma_f32 v224, v164, s48, v234\n\t"
    "v_fma_f32 v225, v165, s48, v234\n\t"
    "v_fma_f32 v226, v166, s48, v234\n\t"
    "v_fma_f32 v227, v167, s48, v234\n\t"
    "v_exp_f32 v224, v224\n\t"
    "v_exp_f32 v225, v225\n\t"
    "v_exp_f32 v226, v226\n\t"
    "v_exp_f32 v227, v227\n\t"
    "v_cvt_pk_bf16_f32 v198, v220, v221\n\t"
    "v_cvt_pk_bf16_f32 v199, v222, v223\n\t"
    "v_add_f32 v229, v229, v220\n\t"
    "v_add_f32 v229, v229, v221\n\t"
    "v_add_f32 v229, v229, v222\n\t"
    "v_add_f32 v229, v229, v223\n\t"
    "v_fma_f32 v220, v168, s48, v232\n\t"
    "v_fma_f32 v221, v169, s48, v232\n\t"
    "v_fma_f32 v222, v170, s48, v232\n\t"
    "v_fma_f32 v223, v171, s48, v232\n\t"
    "v_exp_f32 v220, v220\n\t"
    "v_exp_f32 v221, v221\n\t"
    "v_exp_f32 v222, v222\n\t"
    "v_exp_f32 v223, v223\n\t"
    "v_cvt_pk_bf16_f32 v202, v224, v225\n\t"
    "v_cvt_pk_bf16_f32 v203, v226, v227\n\t"
    "v_add_f32 v230, v230, v224\n\t"
    "v_add_f32 v230, v230, v225\n\t"
    "v_add_f32 v230, v230, v226\n\t"
    "v_add_f32 v230, v230, v227\n\t"
    "v_fma_f32 v224, v172, s48, v233\n\t"
    "v_fma_f32 v225, v173, s48, v233\n\t"
    "v_fma_f32 v226, v174, s48, v233\n\t"
    "v_fma_f32 v227, v175, s48, v233\n\t"
    "v_exp_f32 v224, v224\n\t"
    "v_exp_f32 v225, v225\n\t"
    "v_exp_f32 v226, v226\n\t"
    "v_exp_f32 v227, v227\n\t"
    "v_cvt_pk_bf16_f32 v204, v220, v221\n\t"
    "v_cvt_pk_bf16_f32 v205, v222, v223\n\t"
    "v_add_f32 v228, v228, v220\n\t"
    "v_add_f32 v228, v228, v221\n\t"
    "v_add_f32 v228, v228, v222\n\t"
    "v_add_f32 v228, v228, v223\n\t"
    "v_fma_f32 v220, v176, s48, v234\n\t"
    "v_fma_f32 v221, v177, s48, v234\n\t"
    "v_fma_f32 v222, v178, s48, v234\n\t"
    "v_fma_f32 v223, v179, s48, v234\n\t"
    "v_exp_f32 v220, v220\n\t"
    "v_exp_f32 v221, v221\n\t"
    "v_exp_f32 v222, v222\n\t"
    "v_exp_f32 v223, v223\n\t"
    "v_cvt_pk_bf16_f32 v208, v224, v225\n\t"
    "v_cvt_pk_bf16_f32 v209, v226, v227\n\t"
    "v_add_f32 v229, v229, v224\n\t"
    "v_add_f32 v229, v229, v225\n\t"
    "v_add_f32 v229, v229, v226\n\t"
    "v_add_f32 v229, v229, v227\n\t"
    "v_fma_f32 v224, v180, s48, v232\n\t"
    "v_fma_f32 v225, v181, s48, v232\n\t"
    "v_fma_f32 v226, v182, s48, v232\n\t"
    "v_fma_f32 v227, v183, s48, v232\n\t"
    "v_exp_f32 v224, v224\n\t"
    "v_exp_f32 v225, v225\n\t"
    "v_exp_f32 v226, v226\n\t"
    "v_exp_f32 v227, v227\n\t"
    "v_cvt_pk_bf16_f32 v212, v220, v221\n\t"
    "v_cvt_pk_bf16_f32 v213, v222, v223\n\t"
    "v_add_f32 v230, v230, v220\n\t"
    "v_add_f32 v230, v230, v221\n\t"
    "v_add_f32 v230, v230, v222\n\t"
    "v_add_f32 v230, v230, v223\n\t"
    "v_fma_f32 v220, v184, s48, v233\n\t"
    "v_fma_f32 v221, v185, s48, v233\n\t"
    "v_fma_f32 v222, v186, s48, v233\n\t"
    "v_fma_f32 v223, v187, s48, v233\n\t"
    "v_exp_f32 v220, v220\n\t"
    "v_exp_f32 v221, v221\n\t"
    "v_exp_f32 v222, v222\n\t"
    "v_exp_f32 v223, v223\n\t"
    "v_cvt_pk_bf16_f32 v206, v224, v225\n\t"
    "v_cvt_pk_bf16_f32 v207, v226, v227\n\t"
    "v_add_f32 v228, v228, v224\n\t"
    "v_add_f32 v228, v228, v225\n\t"
    "v_add_f32 v228, v228, v226\n\t"
    "v_add_f32 v228, v228, v227\n\t"
    "v_fma_f32 v224, v188, s48, v234\n\t"
    "v_fma_f32 v225, v189, s48, v234\n\t"
    "v_fma_f32 v226, v190, s48, v234\n\t"
    "v_fma_f32 v227, v191, s48, v234\n\t"
    "v_exp_f32 v224, v224\n\t"
    "v_exp_f32 v225, v225\n\t"
    "v_exp_f32 v226, v226\n\t"
    "v_exp_f32 v227, v227\n\t"
    "v_cvt_pk_bf16_f32 v210, v220, v221\n\t"
    "v_cvt_pk_bf16_f32 v211, v222, v223\n\t"
    "v_add_f32 v229, v229, v220\n\t"
    "v_add_f32 v229, v229, v221\n\t"
    "v_add_f32 v229, v229, v222\n\t"
    "v_add_f32 v229, v229, v223\n\t"
    "v_cvt_pk_bf16_f32 v214, v224, v225\n\t"
    "v_cvt_pk_bf16_f32 v215, v226, v227\n\t"
    "v_add_f32 v230, v230, v224\n\t"
    "v_add_f32 v230, v230, v225\n\t"
    "v_add_f32 v230, v230, v226\n\t"
    "v_add_f32 v230, v230, v227\n\t"
    "v_max3_f32 v220, v228, v229, v230\n\t"
    "v_cmp_lt_f32 vcc, s49, v220\n\t"
    "s_cbranch_vccnz LFBA6%=\n\t"
    "LRETA6%=:\n\t"
    "v_add_f32 v235, v235, v228\n\t"
    "v_add_f32 v236, v236, v229\n\t"
    "v_add_f32 v237, v237, v230\n\t"
    "s_waitcnt vmcnt(0)\n\t"
    "s_barrier\n\t"
    "s_setprio 1\n\t"
    "ds_read_b64_tr_b16 v[144:145], v240 offset:32768\n\t"
    "ds_read_b64_tr_b16 v[146:147], v240 offset:36864\n\t"
    "ds_read_b64_tr_b16 v[148:149], v240 offset:40960\n\t"
    "ds_read_b64_tr_b16 v[150:151], v240 offset:45056\n\t"
    "ds_read_b64_tr_b16 v[152:153], v241 offset:40960\n\t"
    "ds_read_b64_tr_b16 v[154:155], v241 offset:45056\n\t"
    "ds_read_b64_tr_b16 v[156:157], v241 offset:32768\n\t"
    "ds_read_b64_tr_b16 v[158:159], v241 offset:36864\n\t"
    "s_waitcnt lgkmcnt(6)\n\t"
    "v_mfma_f32_16x16x32_bf16 v[0:3], v[144:147], v[192:195], v[0:3]\n\t"
    "ds_read_b64_tr_b16 v[160:161], v240 offset:33280\n\t"
    "ds_read_b64_tr_b16 v[162:163], v240 offset:37376\n\t"
    "s_waitcnt lgkmcnt(6)\n\t"
    "v_mfma_f32_16x16x32_bf16 v[0:3], v[148:151], v[204:207], v[0:3]\n\t"
    "ds_read_b64_tr_b16 v[164:165], v240 offset:41472\n\t"
    "ds_read_b64_tr_b16 v[166:167], v240 offset:45568\n\t"
    "v_mfma_f32_16x16x32_bf16 v[4:7], v[148:151], v[208:211], v[4:7]\n\t"
    "ds_read_b64_tr_b16 v[168:169], v241 offset:41472\n\t"
    "ds_read_b64_tr_b16 v[170:171], v241 offset:45568\n\t"
    "v_mfma_f32_16x16x32_bf16 v[4:7], v[144:147], v[196:199], v[4:7]\n\t"
    "ds_read_b64_tr_b16 v[172:173], v241 offset:33280\n\t"
    "ds_read_b64_tr_b16 v[174:175], v241 offset:37376\n\t"
    "v_mfma_f32_16x16x32_bf16 v[8:11], v[144:147], v[200:203], v[8:11]\n\t"
    "s_add_u32 s58, s50, 114688\n\t"
    "v_mfma_f32_16x16x32_bf16 v[8:11], v[148:151], v[212:215], v[8:11]\n\t"
    "s_waitcnt lgkmcnt(10)\n\t"
    "v_mfma_f32_16x16x32_bf16 v[20:23], v[152:155], v[212:215], v[20:23]\n\t"
    "s_waitcnt lgkmcnt(8)\n\t"
    "v_mfma_f32_16x16x32_bf16 v[20:23], v[156:159], v[200:203], v[20:23]\n\t"
    "s_add_u32 m0, s52, 49152\n\t"
    "v_mfma_f32_16x16x32_bf16 v[16:19], v[156:159], v[196:199], v[16:19]\n\t"
    "ds_read_b64_tr_b16 v[176:177], v240 offset:33792\n\t"
    "ds_read_b64_tr_b16 v[178:179], v240 offset:37888\n\t"
    "v_mfma_f32_16x16x32_bf16 v[16:19], v[152:155], v[208:211], v[16:19]\n\t"
    "ds_read_b64_tr_b16 v[144:145], v240 offset:41984\n\t"
    "ds_read_b64_tr_b16 v[146:147], v240 offset:46080\n\t"
    "v_mfma_f32_16x16x32_bf16 v[12:15], v[152:155], v[204:207], v[12:15]\n\t"
    "buffer_load_dwordx4 v244, s[40:43], s58 offen offset:0 nt lds\n\t"
    "v_mfma_f32_16x16x32_bf16 v[12:15], v[156:159], v[192:195], v[12:15]\n\t"
    "s_waitcnt lgkmcnt(10)\n\t"
    "v_mfma_f32_16x16x32_bf16 v[24:27], v[160:163], v[192:195], v[24:27]\n\t"
    "s_waitcnt lgkmcnt(8)\n\t"
    "v_mfma_f32_16x16x32_bf16 v[24:27], v[164:167], v[204:207], v[24:27]\n\t"
    "buffer_load_dwordx4 v244, s[40:43], s58 offen offset:2048 nt lds\n\t"
    "v_mfma_f32_16x16x32_bf16 v[28:31], v[164:167], v[208:211], v[28:31]\n\t"
    "ds_read_b64_tr_b16 v[148:149], v241 offset:41984\n\t"
    "ds_read_b64_tr_b16 v[150:151], v241 offset:46080\n\t"
    "v_mfma_f32_16x16x32_bf16 v[28:31], v[160:163], v[196:199], v[28:31]\n\t"
    "ds_read_b64_tr_b16 v[152:153], v241 offset:33792\n\t"
    "ds_read_b64_tr_b16 v[154:155], v241 offset:37888\n\t"
    "v_mfma_f32_16x16x32_bf16 v[32:35], v[160:163], v[200:203], v[32:35]\n\t"
    "s_add_u32 m0, s52, 50048\n\t"
    "v_mfma_f32_16x16x32_bf16 v[32:35], v[164:167], v[212:215], v[32:35]\n\t"
    "s_waitcnt lgkmcnt(10)\n\t"
    "v_mfma_f32_16x16x32_bf16 v[44:47], v[168:171], v[212:215], v[44:47]\n\t"
    "s_waitcnt lgkmcnt(8)\n\t"
    "v_mfma_f32_16x16x32_bf16 v[44:47], v[172:175], v[200:203], v[44:47]\n\t"
    "buffer_load_dwordx4 v244, s[40:43], s58 offen offset:128 nt lds\n\t"
    "v_mfma_f32_16x16x32_bf16 v[40:43], v[172:175], v[196:199], v[40:43]\n\t"
    "ds_read_b64_tr_b16 v[156:157], v240 offset:34304\n\t"
    "ds_read_b64_tr_b16 v[158:159], v240 offset:38400\n\t"
    "v_mfma_f32_16x16x32_bf16 v[40:43], v[168:171], v[208:211], v[40:43]\n\t"
    "ds_read_b64_tr_b16 v[160:161], v240 offset:42496\n\t"
    "ds_read_b64_tr_b16 v[162:163], v240 offset:46592\n\t"
    "v_mfma_f32_16x16x32_bf16 v[36:39], v[168:171], v[204:207], v[36:39]\n\t"
    "buffer_load_dwordx4 v244, s[40:43], s58 offen offset:2176 nt lds\n\t"
    "v_mfma_f32_16x16x32_bf16 v[36:39], v[172:175], v[192:195], v[36:39]\n\t"
    "s_waitcnt lgkmcnt(10)\n\t"
    "v_mfma_f32_16x16x32_bf16 v[48:51], v[176:179], v[192:195], v[48:51]\n\t"
    "s_waitcnt lgkmcnt(8)\n\t"
    "v_mfma_f32_16x16x32_bf16 v[48:51], v[144:147], v[204:207], v[48:51]\n\t"
    "s_mov_b32 s57, s51\n\t"
    "v_mfma_f32_16x16x32_bf16 v[52:55], v[144:147], v[208:211], v[52:55]\n\t"
    "ds_read_b64_tr_b16 v[164:165], v241 offset:42496\n\t"
    "ds_read_b64_tr_b16 v[166:167], v241 offset:46592\n\t"
    "v_mfma_f32_16x16x32_bf16 v[52:55], v[176:179], v[196:199], v[52:55]\n\t"
    "ds_read_b64_tr_b16 v[168:169], v241 offset:34304\n\t"
    "ds_read_b64_tr_b16 v[170:171], v241 offset:38400\n\t"
    "v_mfma_f32_16x16x32_bf16 v[56:59], v[176:179], v[200:203], v[56:59]\n\t"
    "s_add_u32 m0, s52, 0\n\t"
    "v_mfma_f32_16x16x32_bf16 v[56:59], v[144:147], v[212:215], v[56:59]\n\t"
    "s_waitcnt lgkmcnt(10)\n\t"
    "v_mfma_f32_16x16x32_bf16 v[68:71], v[148:151], v[212:215], v[68:71]\n\t"
    "s_waitcnt lgkmcnt(8)\n\t"
    "v_mfma_f32_16x16x32_bf16 v[68:71], v[152:155], v[200:203], v[68:71]\n\t"
    "buffer_load_dwordx4 v242, s[36:39], s57 offen offset:0 nt lds\n\t"
    "v_mfma_f32_16x16x32_bf16 v[64:67], v[152:155], v[196:199], v[64:67]\n\t"
    "ds_read_b128 v[180:183], v238 offset:16384\n\t"
    "v_mfma_f32_16x16x32_bf16 v[64:67], v[148:151], v[208:211], v[64:67]\n\t"
    "ds_read_b128 v[184:187], v239 offset:16384\n\t"
    "v_mfma_f32_16x16x32_bf16 v[60:63], v[148:151], v[204:207], v[60:63]\n\t"
    "ds_read_b128 v[188:191], v238 offset:17408\n\t"
    "v_mfma_f32_16x16x32_bf16 v[60:63], v[152:155], v[192:195], v[60:63]\n\t"
    "ds_read_b128 v[216:219], v239 offset:17408\n\t"
    "s_waitcnt lgkmcnt(10)\n\t"
    "v_mfma_f32_16x16x32_bf16 v[72:75], v[156:159], v[192:195], v[72:75]\n\t"
    "s_waitcnt lgkmcnt(8)\n\t"
    "v_mfma_f32_16x16x32_bf16 v[72:75], v[160:163], v[204:207], v[72:75]\n\t"
    "buffer_load_dwordx4 v243, s[36:39], s57 offen offset:2048 nt lds\n\t"
    "v_mfma_f32_16x16x32_bf16 v[76:79], v[160:163], v[208:211], v[76:79]\n\t"
    "v_mfma_f32_16x16x32_bf16 v[76:79], v[156:159], v[196:199], v[76:79]\n\t"
    "s_add_u32 m0, s52, 896\n\t"
    "v_mfma_f32_16x16x32_bf16 v[80:83], v[156:159], v[200:203], v[80:83]\n\t"
    "buffer_load_dwordx4 v242, s[36:39], s57 offen offset:128 nt lds\n\t"
    "v_mfma_f32_16x16x32_bf16 v[80:83], v[160:163], v[212:215], v[80:83]\n\t"
    "s_waitcnt lgkmcnt(6)\n\t"
    "v_mfma_f32_16x16x32_bf16 v[92:95], v[164:167], v[212:215], v[92:95]\n\t"
    "s_waitcnt lgkmcnt(4)\n\t"
    "v_mfma_f32_16x16x32_bf16 v[92:95], v[168:171], v[200:203], v[92:95]\n\t"
    "buffer_load_dwordx4 v243, s[36:39], s57 offen offset:2176 nt lds\n\t"
    "v_mfma_f32_16x16x32_bf16 v[88:91], v[168:171], v[196:199], v[88:91]\n\t"
    "v_mfma_f32_16x16x32_bf16 v[88:91], v[164:167], v[208:211], v[88:91]\n\t"
    "v_mfma_f32_16x16x32_bf16 v[84:87], v[164:167], v[204:207], v[84:87]\n\t"
    "v_mfma_f32_16x16x32_bf16 v[84:87], v[168:171], v[192:195], v[84:87]\n\t"
    "ds_read_b128 v[204:207], v239 offset:21504\n\t"
    "ds_read_b128 v[200:203], v238 offset:21504\n\t"
    "s_waitcnt lgkmcnt(5)\n\t"
    "v_mfma_f32_16x16x32_bf16 v[144:147], v[180:183], v[96:99], 0\n\t"
    "ds_read_b128 v[196:199], v239 offset:20480\n\t"
    "ds_read_b128 v[192:195], v238 offset:20480\n\t"
    "s_waitcnt lgkmcnt(6)\n\t"
    "v_mfma_f32_16x16x32_bf16 v[144:147], v[184:187], v[100:103], v[144:147]\n\t"
    "s_waitcnt lgkmcnt(5)\n\t"
    "v_mfma_f32_16x16x32_bf16 v[144:147], v[188:191], v[104:107], v[144:147]\n\t"
    "s_waitcnt lgkmcnt(4)\n\t"
    "v_mfma_f32_16x16x32_bf16 v[144:147], v[216:219], v[108:111], v[144:147]\n\t"
    "v_mfma_f32_16x16x32_bf16 v[148:151], v[216:219], v[124:127], 0\n\t"
    "v_mfma_f32_16x16x32_bf16 v[148:151], v[188:191], v[120:123], v[148:151]\n\t"
    "v_mfma_f32_16x16x32_bf16 v[148:151], v[184:187], v[116:119], v[148:151]\n\t"
    "v_mfma_f32_16x16x32_bf16 v[148:151], v[180:183], v[112:115], v[148:151]\n\t"
    "v_mfma_f32_16x16x32_bf16 v[152:155], v[180:183], v[128:131], 0\n\t"
    "ds_read_b128 v[180:183], v238 offset:24576\n\t"
    "v_mfma_f32_16x16x32_bf16 v[152:155], v[184:187], v[132:135], v[152:155]\n\t"
    "ds_read_b128 v[184:187], v239 offset:24576\n\t"
    "v_mfma_f32_16x16x32_bf16 v[152:155], v[188:191], v[136:139], v[152:155]\n\t"
    "ds_read_b128 v[188:191], v238 offset:25600\n\t"
    "v_mfma_f32_16x16x32_bf16 v[152:155], v[216:219], v[140:143], v[152:155]\n\t"
    "ds_read_b128 v[216:219], v239 offset:25600\n\t"
    "s_waitcnt lgkmcnt(7)\n\t"
    "v_mfma_f32_16x16x32_bf16 v[164:167], v[204:207], v[140:143], 0\n\t"
    "s_waitcnt lgkmcnt(6)\n\t"
    "v_mfma_f32_16x16x32_bf16 v[164:167], v[200:203], v[136:139], v[164:167]\n\t"
    "s_waitcnt lgkmcnt(5)\n\t"
    "v_mfma_f32_16x16x32_bf16 v[164:167], v[196:199], v[132:135], v[164:167]\n\t"
    "s_waitcnt lgkmcnt(4)\n\t"
    "v_mfma_f32_16x16x32_bf16 v[164:167], v[192:195], v[128:131], v[164:167]\n\t"
    "v_mfma_f32_16x16x32_bf16 v[160:163], v[192:195], v[112:115], 0\n\t"
    "v_mfma_f32_16x16x32_bf16 v[160:163], v[196:199], v[116:119], v[160:163]\n\t"
    "v_mfma_f32_16x16x32_bf16 v[160:163], v[200:203], v[120:123], v[160:163]\n\t"
    "v_mfma_f32_16x16x32_bf16 v[160:163], v[204:207], v[124:127], v[160:163]\n\t"
    "v_mfma_f32_16x16x32_bf16 v[156:159], v[204:207], v[108:111], 0\n\t"
    "ds_read_b128 v[204:207], v239 offset:29696\n\t"
    "v_mfma_f32_16x16x32_bf16 v[156:159], v[200:203], v[104:107], v[156:159]\n\t"
    "ds_read_b128 v[200:203], v238 offset:29696\n\t"
    "v_mfma_f32_16x16x32_bf16 v[156:159], v[196:199], v[100:103], v[156:159]\n\t"
    "ds_read_b128 v[196:199], v239 offset:28672\n\t"
    "v_mfma_f32_16x16x32_bf16 v[156:159], v[192:195], v[96:99], v[156:159]\n\t"
    "ds_read_b128 v[192:195], v238 offset:28672\n\t"
    "s_waitcnt lgkmcnt(7)\n\t"
    "v_mfma_f32_16x16x32_bf16 v[168:171], v[180:183], v[96:99], 0\n\t"
    "s_waitcnt lgkmcnt(6)\n\t"
    "v_mfma_f32_16x16x32_bf16 v[168:171], v[184:187], v[100:103], v[168:171]\n\t"
    "s_waitcnt lgkmcnt(5)\n\t"
    "v_mfma_f32_16x16x32_bf16 v[168:171], v[188:191], v[104:107], v[168:171]\n\t"
    "s_waitcnt lgkmcnt(4)\n\t"
    "v_mfma_f32_16x16x32_bf16 v[168:171], v[216:219], v[108:111], v[168:171]\n\t"
    "v_mfma_f32_16x16x32_bf16 v[172:175], v[216:219], v[124:127], 0\n\t"
    "v_mfma_f32_16x16x32_bf16 v[172:175], v[188:191], v[120:123], v[172:175]\n\t"
    "v_mfma_f32_16x16x32_bf16 v[172:175], v[184:187], v[116:119], v[172:175]\n\t"
    "v_mfma_f32_16x16x32_bf16 v[172:175], v[180:183], v[112:115], v[172:175]\n\t"
    "v_mfma_f32_16x16x32_bf16 v[176:179], v[180:183], v[128:131], 0\n\t"
    "v_mfma_f32_16x16x32_bf16 v[176:179], v[184:187], v[132:135], v[176:179]\n\t"
    "v_mfma_f32_16x16x32_bf16 v[176:179], v[188:191], v[136:139], v[176:179]\n\t"
    "v_mfma_f32_16x16x32_bf16 v[176:179], v[216:219], v[140:143], v[176:179]\n\t"
    "s_waitcnt lgkmcnt(3)\n\t"
    "v_mfma_f32_16x16x32_bf16 v[188:191], v[204:207], v[140:143], 0\n\t"
    "s_waitcnt lgkmcnt(2)\n\t"
    "v_mfma_f32_16x16x32_bf16 v[188:191], v[200:203], v[136:139], v[188:191]\n\t"
    "s_waitcnt lgkmcnt(1)\n\t"
    "v_mfma_f32_16x16x32_bf16 v[188:191], v[196:199], v[132:135], v[188:191]\n\t"
    "s_waitcnt lgkmcnt(0)\n\t"
    "v_mfma_f32_16x16x32_bf16 v[188:191], v[192:195], v[128:131], v[188:191]\n\t"
    "v_mfma_f32_16x16x32_bf16 v[184:187], v[192:195], v[112:115], 0\n\t"
    "v_mfma_f32_16x16x32_bf16 v[184:187], v[196:199], v[116:119], v[184:187]\n\t"
    "v_mfma_f32_16x16x32_bf16 v[184:187], v[200:203], v[120:123], v[184:187]\n\t"
    "v_mfma_f32_16x16x32_bf16 v[184:187], v[204:207], v[124:127], v[184:187]\n\t"
    "v_mfma_f32_16x16x32_bf16 v[180:183], v[204:207], v[108:111], 0\n\t"
    "v_mfma_f32_16x16x32_bf16 v[180:183], v[200:203], v[104:107], v[180:183]\n\t"
    "v_mfma_f32_16x16x32_bf16 v[180:183], v[196:199], v[100:103], v[180:183]\n\t"
    "v_mfma_f32_16x16x32_bf16 v[180:183], v[192:195], v[96:99], v[180:183]\n\t"
    "s_waitcnt lgkmcnt(0)\n\t"
    "s_barrier\n\t"
    "s_setprio 0\n\t"
    "v_fma_f32 v220, v144, s48, v232\n\t"
    "v_fma_f32 v221, v145, s48, v232\n\t"
    "v_fma_f32 v222, v146, s48, v232\n\t"
    "v_fma_f32 v223, v147, s48, v232\n\t"
    "v_exp_f32 v220, v220\n\t"
    "v_exp_f32 v221, v221\n\t"
    "v_exp_f32 v222, v222\n\t"
    "v_exp_f32 v223, v223\n\t"
    "v_fma_f32 v224, v148, s48, v233\n\t"
    "v_fma_f32 v225, v149, s48, v233\n\t"
    "v_fma_f32 v226, v150, s48, v233\n\t"
    "v_fma_f32 v227, v151, s48, v233\n\t"
    "v_exp_f32 v224, v224\n\t"
    "v_exp_f32 v225, v225\n\t"
    "v_exp_f32 v226, v226\n\t"
    "v_exp_f32 v227, v227\n\t"
    "v_cvt_pk_bf16_f32 v192, v220, v221\n\t"
    "v_cvt_pk_bf16_f32 v193, v222, v223\n\t"
    "v_add_f32 v228, v220, v221\n\t"
    "v_add_f32 v228, v228, v222\n\t"
    "v_add_f32 v228, v228, v223\n\t"
    "v_fma_f32 v220, v152, s48, v234\n\t"
    "v_fma_f32 v221, v153, s48, v234\n\t"
    "v_fma_f32 v222, v154, s48, v234\n\t"
    "v_fma_f32 v223, v155, s48, v234\n\t"
    "v_exp_f32 v220, v220\n\t"
    "v_exp_f32 v221, v221\n\t"
    "v_exp_f32 v222, v222\n\t"
    "v_exp_f32 v223, v223\n\t"
    "v_cvt_pk_bf16_f32 v196, v224, v225\n\t"
    "v_cvt_pk_bf16_f32 v197, v226, v227\n\t"
    "v_add_f32 v229, v224, v225\n\t"
    "v_add_f32 v229, v229, v226\n\t"
    "v_add_f32 v229, v229, v227\n\t"
    "v_fma_f32 v224, v156, s48, v232\n\t"
    "v_fma_f32 v225, v157, s48, v232\n\t"
    "v_fma_f32 v226, v158, s48, v232\n\t"
    "v_fma_f32 v227, v159, s48, v232\n\t"
    "v_exp_f32 v224, v224\n\t"
    "v_exp_f32 v225, v225\n\t"
    "v_exp_f32 v226, v226\n\t"
    "v_exp_f32 v227, v227\n\t"
    "v_cvt_pk_bf16_f32 v200, v220, v221\n\t"
    "v_cvt_pk_bf16_f32 v201, v222, v223\n\t"
    "v_add_f32 v230, v220, v221\n\t"
    "v_add_f32 v230, v230, v222\n\t"
    "v_add_f32 v230, v230, v223\n\t"
    "v_fma_f32 v220, v160, s48, v233\n\t"
    "v_fma_f32 v221, v161, s48, v233\n\t"
    "v_fma_f32 v222, v162, s48, v233\n\t"
    "v_fma_f32 v223, v163, s48, v233\n\t"
    "v_exp_f32 v220, v220\n\t"
    "v_exp_f32 v221, v221\n\t"
    "v_exp_f32 v222, v222\n\t"
    "v_exp_f32 v223, v223\n\t"
    "v_cvt_pk_bf16_f32 v194, v224, v225\n\t"
    "v_cvt_pk_bf16_f32 v195, v226, v227\n\t"
    "v_add_f32 v228, v228, v224\n\t"
    "v_add_f32 v228, v228, v225\n\t"
    "v_add_f32 v228, v228, v226\n\t"
    "v_add_f32 v228, v228, v227\n\t"
    "v_fma_f32 v224, v164, s48, v234\n\t"
    "v_fma_f32 v225, v165, s48, v234\n\t"
    "v_fma_f32 v226, v166, s48, v234\n\t"
    "v_fma_f32 v227, v167, s48, v234\n\t"
    "v_exp_f32 v224, v224\n\t"
    "v_exp_f32 v225, v225\n\t"
    "v_exp_f32 v226, v226\n\t"
    "v_exp_f32 v227, v227\n\t"
    "v_cvt_pk_bf16_f32 v198, v220, v221\n\t"
    "v_cvt_pk_bf16_f32 v199, v222, v223\n\t"
    "v_add_f32 v229, v229, v220\n\t"
    "v_add_f32 v229, v229, v221\n\t"
    "v_add_f32 v229, v229, v222\n\t"
    "v_add_f32 v229, v229, v223\n\t"
    "v_fma_f32 v220, v168, s48, v232\n\t"
    "v_fma_f32 v221, v169, s48, v232\n\t"
    "v_fma_f32 v222, v170, s48, v232\n\t"
    "v_fma_f32 v223, v171, s48, v232\n\t"
    "v_exp_f32 v220, v220\n\t"
    "v_exp_f32 v221, v221\n\t"
    "v_exp_f32 v222, v222\n\t"
    "v_exp_f32 v223, v223\n\t"
    "v_cvt_pk_bf16_f32 v202, v224, v225\n\t"
    "v_cvt_pk_bf16_f32 v203, v226, v227\n\t"
    "v_add_f32 v230, v230, v224\n\t"
    "v_add_f32 v230, v230, v225\n\t"
    "v_add_f32 v230, v230, v226\n\t"
    "v_add_f32 v230, v230, v227\n\t"
    "v_fma_f32 v224, v172, s48, v233\n\t"
    "v_fma_f32 v225, v173, s48, v233\n\t"
    "v_fma_f32 v226, v174, s48, v233\n\t"
    "v_fma_f32 v227, v175, s48, v233\n\t"
    "v_exp_f32 v224, v224\n\t"
    "v_exp_f32 v225, v225\n\t"
    "v_exp_f32 v226, v226\n\t"
    "v_exp_f32 v227, v227\n\t"
    "v_cvt_pk_bf16_f32 v204, v220, v221\n\t"
    "v_cvt_pk_bf16_f32 v205, v222, v223\n\t"
    "v_add_f32 v228, v228, v220\n\t"
    "v_add_f32 v228, v228, v221\n\t"
    "v_add_f32 v228, v228, v222\n\t"
    "v_add_f32 v228, v228, v223\n\t"
    "v_fma_f32 v220, v176, s48, v234\n\t"
    "v_fma_f32 v221, v177, s48, v234\n\t"
    "v_fma_f32 v222, v178, s48, v234\n\t"
    "v_fma_f32 v223, v179, s48, v234\n\t"
    "v_exp_f32 v220, v220\n\t"
    "v_exp_f32 v221, v221\n\t"
    "v_exp_f32 v222, v222\n\t"
    "v_exp_f32 v223, v223\n\t"
    "v_cvt_pk_bf16_f32 v208, v224, v225\n\t"
    "v_cvt_pk_bf16_f32 v209, v226, v227\n\t"
    "v_add_f32 v229, v229, v224\n\t"
    "v_add_f32 v229, v229, v225\n\t"
    "v_add_f32 v229, v229, v226\n\t"
    "v_add_f32 v229, v229, v227\n\t"
    "v_fma_f32 v224, v180, s48, v232\n\t"
    "v_fma_f32 v225, v181, s48, v232\n\t"
    "v_fma_f32 v226, v182, s48, v232\n\t"
    "v_fma_f32 v227, v183, s48, v232\n\t"
    "v_exp_f32 v224, v224\n\t"
    "v_exp_f32 v225, v225\n\t"
    "v_exp_f32 v226, v226\n\t"
    "v_exp_f32 v227, v227\n\t"
    "v_cvt_pk_bf16_f32 v212, v220, v221\n\t"
    "v_cvt_pk_bf16_f32 v213, v222, v223\n\t"
    "v_add_f32 v230, v230, v220\n\t"
    "v_add_f32 v230, v230, v221\n\t"
    "v_add_f32 v230, v230, v222\n\t"
    "v_add_f32 v230, v230, v223\n\t"
    "v_fma_f32 v220, v184, s48, v233\n\t"
    "v_fma_f32 v221, v185, s48, v233\n\t"
    "v_fma_f32 v222, v186, s48, v233\n\t"
    "v_fma_f32 v223, v187, s48, v233\n\t"
    "v_exp_f32 v220, v220\n\t"
    "v_exp_f32 v221, v221\n\t"
    "v_exp_f32 v222, v222\n\t"
    "v_exp_f32 v223, v223\n\t"
    "v_cvt_pk_bf16_f32 v206, v224, v225\n\t"
    "v_cvt_pk_bf16_f32 v207, v226, v227\n\t"
    "v_add_f32 v228, v228, v224\n\t"
    "v_add_f32 v228, v228, v225\n\t"
    "v_add_f32 v228, v228, v226\n\t"
    "v_add_f32 v228, v228, v227\n\t"
    "v_fma_f32 v224, v188, s48, v234\n\t"
    "v_fma_f32 v225, v189, s48, v234\n\t"
    "v_fma_f32 v226, v190, s48, v234\n\t"
    "v_fma_f32 v227, v191, s48, v234\n\t"
    "v_exp_f32 v224, v224\n\t"
    "v_exp_f32 v225, v225\n\t"
    "v_exp_f32 v226, v226\n\t"
    "v_exp_f32 v227, v227\n\t"
    "v_cvt_pk_bf16_f32 v210, v220, v221\n\t"
    "v_cvt_pk_bf16_f32 v211, v222, v223\n\t"
    "v_add_f32 v229, v229, v220\n\t"
    "v_add_f32 v229, v229, v221\n\t"
    "v_add_f32 v229, v229, v222\n\t"
    "v_add_f32 v229, v229, v223\n\t"
    "v_cvt_pk_bf16_f32 v214, v224, v225\n\t"
    "v_cvt_pk_bf16_f32 v215, v226, v227\n\t"
    "v_add_f32 v230, v230, v224\n\t"
    "v_add_f32 v230, v230, v225\n\t"
    "v_add_f32 v230, v230, v226\n\t"
    "v_add_f32 v230, v230, v227\n\t"
    "v_max3_f32 v220, v228, v229, v230\n\t"
    "v_cmp_lt_f32 vcc, s49, v220\n\t"
    "s_cbranch_vccnz LFBA7%=\n\t"
    "LRETA7%=:\n\t"
    "v_add_f32 v235, v235, v228\n\t"
    "v_add_f32 v236, v236, v229\n\t"
    "v_add_f32 v237, v237, v230\n\t"
    "s_waitcnt vmcnt(0)\n\t"
    "s_barrier\n\t"
    "s_cmp_eq_u32 s56, 3\n\t"
    "s_cbranch_scc1 LTAILA%=\n\t"
    "v_xor_b32 v254, 64, v231\n\t"
    "s_setprio 1\n\t"
    "ds_read_b64_tr_b16 v[144:145], v240 offset:49152\n\t"
    "ds_read_b64_tr_b16 v[146:147], v240 offset:53248\n\t"
    "ds_read_b64_tr_b16 v[148:149], v240 offset:57344\n\t"
    "ds_read_b64_tr_b16 v[150:151], v240 offset:61440\n\t"
    "ds_read_b64_tr_b16 v[152:153], v241 offset:57344\n\t"
    "ds_read_b64_tr_b16 v[154:155], v241 offset:61440\n\t"
    "ds_read_b64_tr_b16 v[156:157], v241 offset:49152\n\t"
    "ds_read_b64_tr_b16 v[158:159], v241 offset:53248\n\t"
    "s_waitcnt lgkmcnt(6)\n\t"
    "v_mfma_f32_16x16x32_bf16 v[0:3], v[144:147], v[192:195], v[0:3]\n\t"
    "ds_read_b64_tr_b16 v[160:161], v240 offset:49664\n\t"
    "ds_read_b64_tr_b16 v[162:163], v240 offset:53760\n\t"
    "s_waitcnt lgkmcnt(6)\n\t"
    "v_mfma_f32_16x16x32_bf16 v[0:3], v[148:151], v[204:207], v[0:3]\n\t"
    "ds_read_b64_tr_b16 v[164:165], v240 offset:57856\n\t"
    "ds_read_b64_tr_b16 v[166:167], v240 offset:61952\n\t"
    "v_mfma_f32_16x16x32_bf16 v[4:7], v[148:151], v[208:211], v[4:7]\n\t"
    "ds_read_b128 v[96:99], v231 offset:0\n\t"
    "v_mfma_f32_16x16x32_bf16 v[4:7], v[144:147], v[196:199], v[4:7]\n\t"
    "s_mov_b32 s58, s51\n\t"
    "v_mfma_f32_16x16x32_bf16 v[8:11], v[144:147], v[200:203], v[8:11]\n\t"
    "s_add_u32 m0, s52, 32768\n\t"
    "v_mfma_f32_16x16x32_bf16 v[8:11], v[148:151], v[212:215], v[8:11]\n\t"
    "ds_read_b128 v[100:103], v254 offset:0\n\t"
    "s_waitcnt lgkmcnt(8)\n\t"
    "v_mfma_f32_16x16x32_bf16 v[20:23], v[152:155], v[212:215], v[20:23]\n\t"
    "ds_read_b64_tr_b16 v[168:169], v241 offset:57856\n\t"
    "ds_read_b64_tr_b16 v[170:171], v241 offset:61952\n\t"
    "s_waitcnt lgkmcnt(8)\n\t"
    "v_mfma_f32_16x16x32_bf16 v[20:23], v[156:159], v[200:203], v[20:23]\n\t"
    "ds_read_b64_tr_b16 v[172:173], v241 offset:49664\n\t"
    "ds_read_b64_tr_b16 v[174:175], v241 offset:53760\n\t"
    "v_mfma_f32_16x16x32_bf16 v[16:19], v[156:159], v[196:199], v[16:19]\n\t"
    "ds_read_b128 v[104:107], v231 offset:128\n\t"
    "v_mfma_f32_16x16x32_bf16 v[16:19], v[152:155], v[208:211], v[16:19]\n\t"
    "buffer_load_dwordx4 v244, s[40:43], s58 offen offset:0 nt lds\n\t"
    "v_mfma_f32_16x16x32_bf16 v[12:15], v[152:155], v[204:207], v[12:15]\n\t"
    "v_mfma_f32_16x16x32_bf16 v[12:15], v[156:159], v[192:195], v[12:15]\n\t"
    "ds_read_b128 v[108:111], v254 offset:128\n\t"
    "s_waitcnt lgkmcnt(10)\n\t"
    "v_mfma_f32_16x16x32_bf16 v[24:27], v[160:163], v[192:195], v[24:27]\n\t"
    "ds_read_b64_tr_b16 v[176:177], v240 offset:50176\n\t"
    "ds_read_b64_tr_b16 v[178:179], v240 offset:54272\n\t"
    "s_waitcnt lgkmcnt(10)\n\t"
    "v_mfma_f32_16x16x32_bf16 v[24:27], v[164:167], v[204:207], v[24:27]\n\t"
    "ds_read_b64_tr_b16 v[144:145], v240 offset:58368\n\t"
    "ds_read_b64_tr_b16 v[146:147], v240 offset:62464\n\t"
    "v_mfma_f32_16x16x32_bf16 v[28:31], v[164:167], v[208:211], v[28:31]\n\t"
    "ds_read_b128 v[112:115], v231 offset:4096\n\t"
    "v_mfma_f32_16x16x32_bf16 v[28:31], v[160:163], v[196:199], v[28:31]\n\t"
    "buffer_load_dwordx4 v244, s[40:43], s58 offen offset:2048 nt lds\n\t"
    "v_mfma_f32_16x16x32_bf16 v[32:35], v[160:163], v[200:203], v[32:35]\n\t"
    "v_mfma_f32_16x16x32_bf16 v[32:35], v[164:167], v[212:215], v[32:35]\n\t"
    "ds_read_b128 v[116:119], v254 offset:4096\n\t"
    "s_waitcnt lgkmcnt(10)\n\t"
    "v_mfma_f32_16x16x32_bf16 v[44:47], v[168:171], v[212:215], v[44:47]\n\t"
    "ds_read_b64_tr_b16 v[148:149], v241 offset:58368\n\t"
    "ds_read_b64_tr_b16 v[150:151], v241 offset:62464\n\t"
    "s_waitcnt lgkmcnt(10)\n\t"
    "v_mfma_f32_16x16x32_bf16 v[44:47], v[172:175], v[200:203], v[44:47]\n\t"
    "ds_read_b64_tr_b16 v[152:153], v241 offset:50176\n\t"
    "ds_read_b64_tr_b16 v[154:155], v241 offset:54272\n\t"
    "v_mfma_f32_16x16x32_bf16 v[40:43], v[172:175], v[196:199], v[40:43]\n\t"
    "ds_read_b128 v[120:123], v231 offset:4224\n\t"
    "v_mfma_f32_16x16x32_bf16 v[40:43], v[168:171], v[208:211], v[40:43]\n\t"
    "s_add_u32 m0, s52, 33664\n\t"
    "v_mfma_f32_16x16x32_bf16 v[36:39], v[168:171], v[204:207], v[36:39]\n\t"
    "buffer_load_dwordx4 v244, s[40:43], s58 offen offset:128 nt lds\n\t"
    "v_mfma_f32_16x16x32_bf16 v[36:39], v[172:175], v[192:195], v[36:39]\n\t"
    "ds_read_b128 v[124:127], v254 offset:4224\n\t"
    "s_waitcnt lgkmcnt(10)\n\t"
    "v_mfma_f32_16x16x32_bf16 v[48:51], v[176:179], v[192:195], v[48:51]\n\t"
    "ds_read_b64_tr_b16 v[156:157], v240 offset:50688\n\t"
    "ds_read_b64_tr_b16 v[158:159], v240 offset:54784\n\t"
    "s_waitcnt lgkmcnt(10)\n\t"
    "v_mfma_f32_16x16x32_bf16 v[48:51], v[144:147], v[204:207], v[48:51]\n\t"
    "ds_read_b64_tr_b16 v[160:161], v240 offset:58880\n\t"
    "ds_read_b64_tr_b16 v[162:163], v240 offset:62976\n\t"
    "v_mfma_f32_16x16x32_bf16 v[52:55], v[144:147], v[208:211], v[52:55]\n\t"
    "ds_read_b128 v[128:131], v231 offset:8192\n\t"
    "v_mfma_f32_16x16x32_bf16 v[52:55], v[176:179], v[196:199], v[52:55]\n\t"
    "buffer_load_dwordx4 v244, s[40:43], s58 offen offset:2176 nt lds\n\t"
    "v_mfma_f32_16x16x32_bf16 v[56:59], v[176:179], v[200:203], v[56:59]\n\t"
    "v_mfma_f32_16x16x32_bf16 v[56:59], v[144:147], v[212:215], v[56:59]\n\t"
    "ds_read_b128 v[132:135], v254 offset:8192\n\t"
    "s_waitcnt lgkmcnt(10)\n\t"
    "v_mfma_f32_16x16x32_bf16 v[68:71], v[148:151], v[212:215], v[68:71]\n\t"
    "ds_read_b64_tr_b16 v[164:165], v241 offset:58880\n\t"
    "ds_read_b64_tr_b16 v[166:167], v241 offset:62976\n\t"
    "s_waitcnt lgkmcnt(10)\n\t"
    "v_mfma_f32_16x16x32_bf16 v[68:71], v[152:155], v[200:203], v[68:71]\n\t"
    "ds_read_b64_tr_b16 v[168:169], v241 offset:50688\n\t"
    "ds_read_b64_tr_b16 v[170:171], v241 offset:54784\n\t"
    "v_mfma_f32_16x16x32_bf16 v[64:67], v[152:155], v[196:199], v[64:67]\n\t"
    "ds_read_b128 v[136:139], v231 offset:8320\n\t"
    "v_mfma_f32_16x16x32_bf16 v[64:67], v[148:151], v[208:211], v[64:67]\n\t"
    "s_add_u32 s57, s51, 16384\n\t"
    "v_mfma_f32_16x16x32_bf16 v[60:63], v[148:151], v[204:207], v[60:63]\n\t"
    "s_add_u32 m0, s52, 16384\n\t"
    "v_mfma_f32_16x16x32_bf16 v[60:63], v[152:155], v[192:195], v[60:63]\n\t"
    "ds_read_b128 v[140:143], v254 offset:8320\n\t"
    "s_waitcnt lgkmcnt(10)\n\t"
    "v_mfma_f32_16x16x32_bf16 v[72:75], v[156:159], v[192:195], v[72:75]\n\t"
    "ds_read_b128 v[180:183], v238 offset:0\n\t"
    "s_waitcnt lgkmcnt(9)\n\t"
    "v_mfma_f32_16x16x32_bf16 v[72:75], v[160:163], v[204:207], v[72:75]\n\t"
    "ds_read_b128 v[184:187], v239 offset:0\n\t"
    "v_mfma_f32_16x16x32_bf16 v[76:79], v[160:163], v[208:211], v[76:79]\n\t"
    "ds_read_b128 v[188:191], v238 offset:1024\n\t"
    "v_mfma_f32_16x16x32_bf16 v[76:79], v[156:159], v[196:199], v[76:79]\n\t"
    "ds_read_b128 v[216:219], v239 offset:1024\n\t"
    "v_mfma_f32_16x16x32_bf16 v[80:83], v[156:159], v[200:203], v[80:83]\n\t"
    "buffer_load_dwordx4 v242, s[36:39], s57 offen offset:0 nt lds\n\t"
    "v_mfma_f32_16x16x32_bf16 v[80:83], v[160:163], v[212:215], v[80:83]\n\t"
    "s_waitcnt lgkmcnt(8)\n\t"
    "v_mfma_f32_16x16x32_bf16 v[92:95], v[164:167], v[212:215], v[92:95]\n\t"
    "s_waitcnt lgkmcnt(6)\n\t"
    "v_mfma_f32_16x16x32_bf16 v[92:95], v[168:171], v[200:203], v[92:95]\n\t"
    "buffer_load_dwordx4 v243, s[36:39], s57 offen offset:2048 nt lds\n\t"
    "v_mfma_f32_16x16x32_bf16 v[88:91], v[168:171], v[196:199], v[88:91]\n\t"
    "v_mfma_f32_16x16x32_bf16 v[88:91], v[164:167], v[208:211], v[88:91]\n\t"
    "s_add_u32 m0, s52, 17280\n\t"
    "v_mfma_f32_16x16x32_bf16 v[84:87], v[164:167], v[204:207], v[84:87]\n\t"
    "buffer_load_dwordx4 v242, s[36:39], s57 offen offset:128 nt lds\n\t"
    "v_mfma_f32_16x16x32_bf16 v[84:87], v[168:171], v[192:195], v[84:87]\n\t"
    "s_waitcnt lgkmcnt(4)\n\t"
    "s_waitcnt lgkmcnt(3)\n\t"
    "v_mfma_f32_16x16x32_bf16 v[144:147], v[180:183], v[96:99], 0\n\t"
    "ds_read_b128 v[204:207], v239 offset:5120\n\t"
    "s_waitcnt lgkmcnt(3)\n\t"
    "v_mfma_f32_16x16x32_bf16 v[144:147], v[184:187], v[100:103], v[144:147]\n\t"
    "ds_read_b128 v[200:203], v238 offset:5120\n\t"
    "s_waitcnt lgkmcnt(3)\n\t"
    "v_mfma_f32_16x16x32_bf16 v[144:147], v[188:191], v[104:107], v[144:147]\n\t"
    "ds_read_b128 v[196:199], v239 offset:4096\n\t"
    "s_waitcnt lgkmcnt(3)\n\t"
    "v_mfma_f32_16x16x32_bf16 v[144:147], v[216:219], v[108:111], v[144:147]\n\t"
    "ds_read_b128 v[192:195], v238 offset:4096\n\t"
    "v_mfma_f32_16x16x32_bf16 v[148:151], v[216:219], v[124:127], 0\n\t"
    "buffer_load_dwordx4 v243, s[36:39], s57 offen offset:2176 nt lds\n\t"
    "v_mfma_f32_16x16x32_bf16 v[148:151], v[188:191], v[120:123], v[148:151]\n\t"
    "v_mfma_f32_16x16x32_bf16 v[148:151], v[184:187], v[116:119], v[148:151]\n\t"
    "v_mfma_f32_16x16x32_bf16 v[148:151], v[180:183], v[112:115], v[148:151]\n\t"
    "v_mfma_f32_16x16x32_bf16 v[152:155], v[180:183], v[128:131], 0\n\t"
    "v_mfma_f32_16x16x32_bf16 v[152:155], v[184:187], v[132:135], v[152:155]\n\t"
    "v_mfma_f32_16x16x32_bf16 v[152:155], v[188:191], v[136:139], v[152:155]\n\t"
    "v_mfma_f32_16x16x32_bf16 v[152:155], v[216:219], v[140:143], v[152:155]\n\t"
    "s_waitcnt lgkmcnt(3)\n\t"
    "v_mfma_f32_16x16x32_bf16 v[164:167], v[204:207], v[140:143], 0\n\t"
    "ds_read_b128 v[180:183], v238 offset:8192\n\t"
    "s_waitcnt lgkmcnt(3)\n\t"
    "v_mfma_f32_16x16x32_bf16 v[164:167], v[200:203], v[136:139], v[164:167]\n\t"
    "ds_read_b128 v[184:187], v239 offset:8192\n\t"
    "s_waitcnt lgkmcnt(3)\n\t"
    "v_mfma_f32_16x16x32_bf16 v[164:167], v[196:199], v[132:135], v[164:167]\n\t"
    "ds_read_b128 v[188:191], v238 offset:9216\n\t"
    "s_waitcnt lgkmcnt(3)\n\t"
    "v_mfma_f32_16x16x32_bf16 v[164:167], v[192:195], v[128:131], v[164:167]\n\t"
    "ds_read_b128 v[216:219], v239 offset:9216\n\t"
    "v_mfma_f32_16x16x32_bf16 v[160:163], v[192:195], v[112:115], 0\n\t"
    "v_mfma_f32_16x16x32_bf16 v[160:163], v[196:199], v[116:119], v[160:163]\n\t"
    "v_mfma_f32_16x16x32_bf16 v[160:163], v[200:203], v[120:123], v[160:163]\n\t"
    "v_mfma_f32_16x16x32_bf16 v[160:163], v[204:207], v[124:127], v[160:163]\n\t"
    "v_mfma_f32_16x16x32_bf16 v[156:159], v[204:207], v[108:111], 0\n\t"
    "v_mfma_f32_16x16x32_bf16 v[156:159], v[200:203], v[104:107], v[156:159]\n\t"
    "v_mfma_f32_16x16x32_bf16 v[156:159], v[196:199], v[100:103], v[156:159]\n\t"
    "v_mfma_f32_16x16x32_bf16 v[156:159], v[192:195], v[96:99], v[156:159]\n\t"
    "s_waitcnt lgkmcnt(3)\n\t"
    "v_mfma_f32_16x16x32_bf16 v[168:171], v[180:183], v[96:99], 0\n\t"
    "ds_read_b128 v[204:207], v239 offset:13312\n\t"
    "s_waitcnt lgkmcnt(3)\n\t"
    "v_mfma_f32_16x16x32_bf16 v[168:171], v[184:187], v[100:103], v[168:171]\n\t"
    "ds_read_b128 v[200:203], v238 offset:13312\n\t"
    "s_waitcnt lgkmcnt(3)\n\t"
    "v_mfma_f32_16x16x32_bf16 v[168:171], v[188:191], v[104:107], v[168:171]\n\t"
    "ds_read_b128 v[196:199], v239 offset:12288\n\t"
    "s_waitcnt lgkmcnt(3)\n\t"
    "v_mfma_f32_16x16x32_bf16 v[168:171], v[216:219], v[108:111], v[168:171]\n\t"
    "ds_read_b128 v[192:195], v238 offset:12288\n\t"
    "v_mfma_f32_16x16x32_bf16 v[172:175], v[216:219], v[124:127], 0\n\t"
    "v_mfma_f32_16x16x32_bf16 v[172:175], v[188:191], v[120:123], v[172:175]\n\t"
    "v_mfma_f32_16x16x32_bf16 v[172:175], v[184:187], v[116:119], v[172:175]\n\t"
    "v_mfma_f32_16x16x32_bf16 v[172:175], v[180:183], v[112:115], v[172:175]\n\t"
    "v_mfma_f32_16x16x32_bf16 v[176:179], v[180:183], v[128:131], 0\n\t"
    "v_mfma_f32_16x16x32_bf16 v[176:179], v[184:187], v[132:135], v[176:179]\n\t"
    "v_mfma_f32_16x16x32_bf16 v[176:179], v[188:191], v[136:139], v[176:179]\n\t"
    "v_mfma_f32_16x16x32_bf16 v[176:179], v[216:219], v[140:143], v[176:179]\n\t"
    "s_waitcnt lgkmcnt(3)\n\t"
    "v_mfma_f32_16x16x32_bf16 v[188:191], v[204:207], v[140:143], 0\n\t"
    "s_waitcnt lgkmcnt(2)\n\t"
    "v_mfma_f32_16x16x32_bf16 v[188:191], v[200:203], v[136:139], v[188:191]\n\t"
    "s_waitcnt lgkmcnt(1)\n\t"
    "v_mfma_f32_16x16x32_bf16 v[188:191], v[196:199], v[132:135], v[188:191]\n\t"
    "s_waitcnt lgkmcnt(0)\n\t"
    "v_mfma_f32_16x16x32_bf16 v[188:191], v[192:195], v[128:131], v[188:191]\n\t"
    "v_mfma_f32_16x16x32_bf16 v[184:187], v[192:195], v[112:115], 0\n\t"
    "v_mfma_f32_16x16x32_bf16 v[184:187], v[196:199], v[116:119], v[184:187]\n\t"
    "v_mfma_f32_16x16x32_bf16 v[184:187], v[200:203], v[120:123], v[184:187]\n\t"
    "v_mfma_f32_16x16x32_bf16 v[184:187], v[204:207], v[124:127], v[184:187]\n\t"
    "v_mfma_f32_16x16x32_bf16 v[180:183], v[204:207], v[108:111], 0\n\t"
    "v_mfma_f32_16x16x32_bf16 v[180:183], v[200:203], v[104:107], v[180:183]\n\t"
    "v_mfma_f32_16x16x32_bf16 v[180:183], v[196:199], v[100:103], v[180:183]\n\t"
    "v_mfma_f32_16x16x32_bf16 v[180:183], v[192:195], v[96:99], v[180:183]\n\t"
    "s_waitcnt lgkmcnt(0)\n\t"
    "s_barrier\n\t"
    "s_setprio 0\n\t"
    "s_add_u32 s56, s56, 1\n\t"
    "s_add_u32 s64, s64, 25165824\n\t"
    "s_mov_b32 s50, s51\n\t"
    "s_add_u32 s51, s51, 16777216\n\t"
    "s_cmp_eq_u32 s56, 3\n\t"
    "s_cselect_b32 s51, 2130706432, s51\n\t"
    "s_branch LBLKA%=\n\t"
    "LTAILA%=:\n\t"
    "s_waitcnt vmcnt(0)\n\t"
    "v_mov_b32 v220, v235\n\t"
    "v_mov_b32 v221, v236\n\t"
    "v_mov_b32 v222, v237\n\t"
    "v_permlane16_swap_b32 v235, v220\n\t"
    "v_permlane16_swap_b32 v236, v221\n\t"
    "v_permlane16_swap_b32 v237, v222\n\t"
    "v_add_f32 v235, v235, v220\n\t"
    "v_add_f32 v236, v236, v221\n\t"
    "v_add_f32 v237, v237, v222\n\t"
    "v_mov_b32 v220, v235\n\t"
    "v_mov_b32 v221, v236\n\t"
    "v_mov_b32 v222, v237\n\t"
    "v_permlane32_swap_b32 v235, v220\n\t"
    "v_permlane32_swap_b32 v236, v221\n\t"
    "v_permlane32_swap_b32 v237, v222\n\t"
    "v_add_f32 v235, v235, v220\n\t"
    "v_add_f32 v236, v236, v221\n\t"
    "v_add_f32 v237, v237, v222\n\t"
    "v_rcp_f32 v245, v235\n\t"
    "v_rcp_f32 v254, v236\n\t"
    "v_rcp_f32 v255, v237\n\t"
    "v_mbcnt_lo_u32_b32 v233, -1, 0\n\t"
    "v_mbcnt_hi_u32_b32 v233, -1, v233\n\t"
    "v_lshrrev_b32 v226, 4, v233\n\t"
    "v_and_b32 v227, 15, v233\n\t"
    "v_xor_b32 v227, v227, v226\n\t"
    "v_lshlrev_b32 v226, 8, v226\n\t"
    "v_lshl_add_u32 v242, v227, 4, v226\n\t"
    "v_xor_b32 v243, 64, v242\n\t"
    "v_lshlrev_b32 v232, 4, v233\n\t"
    "v_add_u32 v232, s63, v232\n\t"
    "s_setprio 1\n\t"
    "ds_read_b64_tr_b16 v[144:145], v240 offset:49152\n\t"
    "ds_read_b64_tr_b16 v[146:147], v240 offset:53248\n\t"
    "ds_read_b64_tr_b16 v[148:149], v240 offset:57344\n\t"
    "ds_read_b64_tr_b16 v[150:151], v240 offset:61440\n\t"
    "ds_read_b64_tr_b16 v[152:153], v241 offset:57344\n\t"
    "ds_read_b64_tr_b16 v[154:155], v241 offset:61440\n\t"
    "ds_read_b64_tr_b16 v[156:157], v241 offset:49152\n\t"
    "ds_read_b64_tr_b16 v[158:159], v241 offset:53248\n\t"
    "s_waitcnt lgkmcnt(6)\n\t"
    "v_mfma_f32_16x16x32_bf16 v[0:3], v[144:147], v[192:195], v[0:3]\n\t"
    "ds_read_b64_tr_b16 v[160:161], v240 offset:49664\n\t"
    "ds_read_b64_tr_b16 v[162:163], v240 offset:53760\n\t"
    "ds_read_b64_tr_b16 v[164:165], v240 offset:57856\n\t"
    "ds_read_b64_tr_b16 v[166:167], v240 offset:61952\n\t"
    "s_waitcnt lgkmcnt(8)\n\t"
    "v_mfma_f32_16x16x32_bf16 v[0:3], v[148:151], v[204:207], v[0:3]\n\t"
    "ds_read_b64_tr_b16 v[168:169], v241 offset:57856\n\t"
    "ds_read_b64_tr_b16 v[170:171], v241 offset:61952\n\t"
    "ds_read_b64_tr_b16 v[172:173], v241 offset:49664\n\t"
    "ds_read_b64_tr_b16 v[174:175], v241 offset:53760\n\t"
    "s_waitcnt lgkmcnt(10)\n\t"
    "v_mfma_f32_16x16x32_bf16 v[12:15], v[152:155], v[204:207], v[12:15]\n\t"
    "ds_read_b64_tr_b16 v[176:177], v240 offset:50176\n\t"
    "ds_read_b64_tr_b16 v[178:179], v240 offset:54272\n\t"
    "s_waitcnt lgkmcnt(8)\n\t"
    "ds_read_b64_tr_b16 v[180:181], v240 offset:58368\n\t"
    "ds_read_b64_tr_b16 v[182:183], v240 offset:62464\n\t"
    "v_mfma_f32_16x16x32_bf16 v[12:15], v[156:159], v[192:195], v[12:15]\n\t"
    "ds_read_b64_tr_b16 v[184:185], v241 offset:58368\n\t"
    "ds_read_b64_tr_b16 v[186:187], v241 offset:62464\n\t"
    "s_waitcnt lgkmcnt(8)\n\t"
    "ds_read_b64_tr_b16 v[188:189], v241 offset:50176\n\t"
    "ds_read_b64_tr_b16 v[190:191], v241 offset:54272\n\t"
    "v_mfma_f32_16x16x32_bf16 v[24:27], v[160:163], v[192:195], v[24:27]\n\t"
    "ds_read_b64_tr_b16 v[128:129], v240 offset:50688\n\t"
    "ds_read_b64_tr_b16 v[130:131], v240 offset:54784\n\t"
    "s_waitcnt lgkmcnt(8)\n\t"
    "ds_read_b64_tr_b16 v[132:133], v240 offset:58880\n\t"
    "ds_read_b64_tr_b16 v[134:135], v240 offset:62976\n\t"
    "v_mfma_f32_16x16x32_bf16 v[24:27], v[164:167], v[204:207], v[24:27]\n\t"
    "ds_read_b64_tr_b16 v[136:137], v241 offset:58880\n\t"
    "ds_read_b64_tr_b16 v[138:139], v241 offset:62976\n\t"
    "v_mfma_f32_16x16x32_bf16 v[36:39], v[168:171], v[204:207], v[36:39]\n\t"
    "s_waitcnt lgkmcnt(8)\n\t"
    "ds_read_b64_tr_b16 v[140:141], v241 offset:50688\n\t"
    "ds_read_b64_tr_b16 v[142:143], v241 offset:54784\n\t"
    "v_mfma_f32_16x16x32_bf16 v[36:39], v[172:175], v[192:195], v[36:39]\n\t"
    "v_mul_f32 v0, v0, v245\n\t"
    "v_mul_f32 v1, v1, v245\n\t"
    "v_mul_f32 v2, v2, v245\n\t"
    "v_mul_f32 v3, v3, v245\n\t"
    "v_mfma_f32_16x16x32_bf16 v[48:51], v[176:179], v[192:195], v[48:51]\n\t"
    "v_cvt_pk_bf16_f32 v0, v0, v1\n\t"
    "v_cvt_pk_bf16_f32 v1, v2, v3\n\t"
    "v_mfma_f32_16x16x32_bf16 v[48:51], v[180:183], v[204:207], v[48:51]\n\t"
    "ds_write_b64 v250, v[0:1] offset:0\n\t"
    "v_mul_f32 v12, v12, v245\n\t"
    "v_mul_f32 v13, v13, v245\n\t"
    "v_mul_f32 v14, v14, v245\n\t"
    "v_mul_f32 v15, v15, v245\n\t"
    "v_mfma_f32_16x16x32_bf16 v[60:63], v[184:187], v[204:207], v[60:63]\n\t"
    "v_cvt_pk_bf16_f32 v12, v12, v13\n\t"
    "v_cvt_pk_bf16_f32 v13, v14, v15\n\t"
    "s_waitcnt lgkmcnt(9)\n\t"
    "v_mfma_f32_16x16x32_bf16 v[60:63], v[188:191], v[192:195], v[60:63]\n\t"
    "ds_write_b64 v251, v[12:13] offset:0\n\t"
    "v_mul_f32 v24, v24, v245\n\t"
    "v_mul_f32 v25, v25, v245\n\t"
    "v_mul_f32 v26, v26, v245\n\t"
    "v_mul_f32 v27, v27, v245\n\t"
    "s_waitcnt lgkmcnt(8)\n\t"
    "v_mfma_f32_16x16x32_bf16 v[72:75], v[128:131], v[192:195], v[72:75]\n\t"
    "v_cvt_pk_bf16_f32 v24, v24, v25\n\t"
    "v_cvt_pk_bf16_f32 v25, v26, v27\n\t"
    "s_waitcnt lgkmcnt(6)\n\t"
    "v_mfma_f32_16x16x32_bf16 v[72:75], v[132:135], v[204:207], v[72:75]\n\t"
    "ds_write_b64 v252, v[24:25] offset:0\n\t"
    "v_mul_f32 v36, v36, v245\n\t"
    "v_mul_f32 v37, v37, v245\n\t"
    "v_mul_f32 v38, v38, v245\n\t"
    "v_mul_f32 v39, v39, v245\n\t"
    "s_waitcnt lgkmcnt(5)\n\t"
    "v_mfma_f32_16x16x32_bf16 v[84:87], v[136:139], v[204:207], v[84:87]\n\t"
    "v_cvt_pk_bf16_f32 v36, v36, v37\n\t"
    "v_cvt_pk_bf16_f32 v37, v38, v39\n\t"
    "s_waitcnt lgkmcnt(3)\n\t"
    "v_mfma_f32_16x16x32_bf16 v[84:87], v[140:143], v[192:195], v[84:87]\n\t"
    "ds_write_b64 v253, v[36:37] offset:0\n\t"
    "v_mul_f32 v48, v48, v245\n\t"
    "v_mul_f32 v49, v49, v245\n\t"
    "v_mul_f32 v50, v50, v245\n\t"
    "v_mul_f32 v51, v51, v245\n\t"
    "v_mfma_f32_16x16x32_bf16 v[88:91], v[140:143], v[196:199], v[88:91]\n\t"
    "v_cvt_pk_bf16_f32 v48, v48, v49\n\t"
    "v_cvt_pk_bf16_f32 v49, v50, v51\n\t"
    "v_mfma_f32_16x16x32_bf16 v[88:91], v[136:139], v[208:211], v[88:91]\n\t"
    "ds_write_b64 v250, v[48:49] offset:128\n\t"
    "v_mul_f32 v60, v60, v245\n\t"
    "v_mul_f32 v61, v61, v245\n\t"
    "v_mul_f32 v62, v62, v245\n\t"
    "v_mul_f32 v63, v63, v245\n\t"
    "v_mfma_f32_16x16x32_bf16 v[76:79], v[132:135], v[208:211], v[76:79]\n\t"
    "v_cvt_pk_bf16_f32 v60, v60, v61\n\t"
    "v_cvt_pk_bf16_f32 v61, v62, v63\n\t"
    "v_mfma_f32_16x16x32_bf16 v[76:79], v[128:131], v[196:199], v[76:79]\n\t"
    "ds_write_b64 v251, v[60:61] offset:128\n\t"
    "v_mul_f32 v72, v72, v245\n\t"
    "v_mul_f32 v73, v73, v245\n\t"
    "v_mul_f32 v74, v74, v245\n\t"
    "v_mul_f32 v75, v75, v245\n\t"
    "v_mfma_f32_16x16x32_bf16 v[64:67], v[188:191], v[196:199], v[64:67]\n\t"
    "v_cvt_pk_bf16_f32 v72, v72, v73\n\t"
    "v_cvt_pk_bf16_f32 v73, v74, v75\n\t"
    "v_mfma_f32_16x16x32_bf16 v[64:67], v[184:187], v[208:211], v[64:67]\n\t"
    "ds_write_b64 v252, v[72:73] offset:128\n\t"
    "v_mul_f32 v84, v84, v245\n\t"
    "v_mul_f32 v85, v85, v245\n\t"
    "v_mul_f32 v86, v86, v245\n\t"
    "v_mul_f32 v87, v87, v245\n\t"
    "v_mfma_f32_16x16x32_bf16 v[52:55], v[180:183], v[208:211], v[52:55]\n\t"
    "v_cvt_pk_bf16_f32 v84, v84, v85\n\t"
    "v_cvt_pk_bf16_f32 v85, v86, v87\n\t"
    "v_mfma_f32_16x16x32_bf16 v[52:55], v[176:179], v[196:199], v[52:55]\n\t"
    "ds_write_b64 v253, v[84:85] offset:128\n\t"
    "v_mul_f32 v88, v88, v254\n\t"
    "v_mul_f32 v89, v89, v254\n\t"
    "v_mul_f32 v90, v90, v254\n\t"
    "v_mul_f32 v91, v91, v254\n\t"
    "v_mfma_f32_16x16x32_bf16 v[40:43], v[172:175], v[196:199], v[40:43]\n\t"
    "v_cvt_pk_bf16_f32 v88, v88, v89\n\t"
    "v_cvt_pk_bf16_f32 v89, v90, v91\n\t"
    "ds_read_b128 v[96:99], v232 offset:0\n\t"
    "v_mfma_f32_16x16x32_bf16 v[40:43], v[168:171], v[208:211], v[40:43]\n\t"
    "v_mul_f32 v76, v76, v254\n\t"
    "v_mul_f32 v77, v77, v254\n\t"
    "v_mul_f32 v78, v78, v254\n\t"
    "v_mul_f32 v79, v79, v254\n\t"
    "ds_write_b64 v253, v[88:89] offset:4224\n\t"
    "ds_read_b128 v[100:103], v232 offset:1024\n\t"
    "v_mfma_f32_16x16x32_bf16 v[28:31], v[164:167], v[208:211], v[28:31]\n\t"
    "v_cvt_pk_bf16_f32 v76, v76, v77\n\t"
    "v_cvt_pk_bf16_f32 v77, v78, v79\n\t"
    "ds_read_b64 v[104:105], v232 offset:2056\n\t"
    "ds_read_b64 v[106:107], v232 offset:2048\n\t"
    "v_mfma_f32_16x16x32_bf16 v[28:31], v[160:163], v[196:199], v[28:31]\n\t"
    "v_mul_f32 v64, v64, v254\n\t"
    "v_mul_f32 v65, v65, v254\n\t"
    "v_mul_f32 v66, v66, v254\n\t"
    "v_mul_f32 v67, v67, v254\n\t"
    "ds_write_b64 v252, v[76:77] offset:4224\n\t"
    "s_waitcnt lgkmcnt(10)\n\t"
    "ds_read_b64 v[108:109], v232 offset:3080\n\t"
    "ds_read_b64 v[110:111], v232 offset:3072\n\t"
    "v_mfma_f32_16x16x32_bf16 v[16:19], v[156:159], v[196:199], v[16:19]\n\t"
    "v_cvt_pk_bf16_f32 v64, v64, v65\n\t"
    "v_cvt_pk_bf16_f32 v65, v66, v67\n\t"
    "v_mfma_f32_16x16x32_bf16 v[16:19], v[152:155], v[208:211], v[16:19]\n\t"
    "v_mul_f32 v52, v52, v254\n\t"
    "v_mul_f32 v53, v53, v254\n\t"
    "v_mul_f32 v54, v54, v254\n\t"
    "v_mul_f32 v55, v55, v254\n\t"
    "ds_write_b64 v251, v[64:65] offset:4224\n\t"
    "s_add_u32 s58, s64, 0\n\t"
    "s_waitcnt lgkmcnt(8)\n\t"
    "buffer_store_dwordx4 v[96:99], v242, s[84:87], s58 offen offset:0 nt\n\t"
    "v_mfma_f32_16x16x32_bf16 v[4:7], v[148:151], v[208:211], v[4:7]\n\t"
    "v_cvt_pk_bf16_f32 v52, v52, v53\n\t"
    "v_cvt_pk_bf16_f32 v53, v54, v55\n\t"
    "s_add_u32 s58, s64, 0\n\t"
    "s_waitcnt lgkmcnt(6)\n\t"
    "buffer_store_dwordx4 v[100:103], v243, s[84:87], s58 offen offset:1024 nt\n\t"
    "v_mfma_f32_16x16x32_bf16 v[4:7], v[144:147], v[196:199], v[4:7]\n\t"
    "v_mul_f32 v40, v40, v254\n\t"
    "v_mul_f32 v41, v41, v254\n\t"
    "v_mul_f32 v42, v42, v254\n\t"
    "v_mul_f32 v43, v43, v254\n\t"
    "ds_write_b64 v250, v[52:53] offset:4224\n\t"
    "s_add_u32 s58, s64, 0\n\t"
    "s_waitcnt lgkmcnt(5)\n\t"
    "buffer_store_dwordx4 v[104:107], v242, s[84:87], s58 offen offset:2048 nt\n\t"
    "v_mfma_f32_16x16x32_bf16 v[8:11], v[144:147], v[200:203], v[8:11]\n\t"
    "v_cvt_pk_bf16_f32 v40, v40, v41\n\t"
    "v_cvt_pk_bf16_f32 v41, v42, v43\n\t"
    "s_add_u32 s58, s64, 0\n\t"
    "s_waitcnt lgkmcnt(2)\n\t"
    "buffer_store_dwordx4 v[108:111], v243, s[84:87], s58 offen offset:3072 nt\n\t"
    "v_mfma_f32_16x16x32_bf16 v[8:11], v[148:151], v[212:215], v[8:11]\n\t"
    "v_mul_f32 v28, v28, v254\n\t"
    "v_mul_f32 v29, v29, v254\n\t"
    "v_mul_f32 v30, v30, v254\n\t"
    "v_mul_f32 v31, v31, v254\n\t"
    "ds_write_b64 v253, v[40:41] offset:4096\n\t"
    "v_mfma_f32_16x16x32_bf16 v[20:23], v[152:155], v[212:215], v[20:23]\n\t"
    "v_cvt_pk_bf16_f32 v28, v28, v29\n\t"
    "v_cvt_pk_bf16_f32 v29, v30, v31\n\t"
    "v_mfma_f32_16x16x32_bf16 v[20:23], v[156:159], v[200:203], v[20:23]\n\t"
    "v_mul_f32 v16, v16, v254\n\t"
    "v_mul_f32 v17, v17, v254\n\t"
    "v_mul_f32 v18, v18, v254\n\t"
    "v_mul_f32 v19, v19, v254\n\t"
    "ds_write_b64 v252, v[28:29] offset:4096\n\t"
    "v_mfma_f32_16x16x32_bf16 v[32:35], v[160:163], v[200:203], v[32:35]\n\t"
    "v_cvt_pk_bf16_f32 v16, v16, v17\n\t"
    "v_cvt_pk_bf16_f32 v17, v18, v19\n\t"
    "v_mfma_f32_16x16x32_bf16 v[32:35], v[164:167], v[212:215], v[32:35]\n\t"
    "v_mul_f32 v4, v4, v254\n\t"
    "v_mul_f32 v5, v5, v254\n\t"
    "v_mul_f32 v6, v6, v254\n\t"
    "v_mul_f32 v7, v7, v254\n\t"
    "ds_write_b64 v251, v[16:17] offset:4096\n\t"
    "v_mfma_f32_16x16x32_bf16 v[44:47], v[168:171], v[212:215], v[44:47]\n\t"
    "v_cvt_pk_bf16_f32 v4, v4, v5\n\t"
    "v_cvt_pk_bf16_f32 v5, v6, v7\n\t"
    "v_mfma_f32_16x16x32_bf16 v[44:47], v[172:175], v[200:203], v[44:47]\n\t"
    "ds_write_b64 v250, v[4:5] offset:4096\n\t"
    "v_mul_f32 v8, v8, v255\n\t"
    "v_mul_f32 v9, v9, v255\n\t"
    "v_mul_f32 v10, v10, v255\n\t"
    "v_mul_f32 v11, v11, v255\n\t"
    "v_mfma_f32_16x16x32_bf16 v[56:59], v[176:179], v[200:203], v[56:59]\n\t"
    "v_cvt_pk_bf16_f32 v8, v8, v9\n\t"
    "v_cvt_pk_bf16_f32 v9, v10, v11\n\t"
    "ds_read_b128 v[112:115], v232 offset:4096\n\t"
    "v_mfma_f32_16x16x32_bf16 v[56:59], v[180:183], v[212:215], v[56:59]\n\t"
    "ds_write_b64 v250, v[8:9] offset:8192\n\t"
    "v_mul_f32 v20, v20, v255\n\t"
    "v_mul_f32 v21, v21, v255\n\t"
    "v_mul_f32 v22, v22, v255\n\t"
    "v_mul_f32 v23, v23, v255\n\t"
    "ds_read_b128 v[116:119], v232 offset:5120\n\t"
    "v_mfma_f32_16x16x32_bf16 v[68:71], v[184:187], v[212:215], v[68:71]\n\t"
    "v_cvt_pk_bf16_f32 v20, v20, v21\n\t"
    "v_cvt_pk_bf16_f32 v21, v22, v23\n\t"
    "ds_read_b64 v[120:121], v232 offset:6152\n\t"
    "ds_read_b64 v[122:123], v232 offset:6144\n\t"
    "v_mfma_f32_16x16x32_bf16 v[68:71], v[188:191], v[200:203], v[68:71]\n\t"
    "ds_write_b64 v251, v[20:21] offset:8192\n\t"
    "v_mul_f32 v32, v32, v255\n\t"
    "v_mul_f32 v33, v33, v255\n\t"
    "v_mul_f32 v34, v34, v255\n\t"
    "v_mul_f32 v35, v35, v255\n\t"
    "ds_read_b64 v[124:125], v232 offset:7176\n\t"
    "ds_read_b64 v[126:127], v232 offset:7168\n\t"
    "v_mfma_f32_16x16x32_bf16 v[80:83], v[128:131], v[200:203], v[80:83]\n\t"
    "v_cvt_pk_bf16_f32 v32, v32, v33\n\t"
    "v_cvt_pk_bf16_f32 v33, v34, v35\n\t"
    "v_mfma_f32_16x16x32_bf16 v[80:83], v[132:135], v[212:215], v[80:83]\n\t"
    "ds_write_b64 v252, v[32:33] offset:8192\n\t"
    "v_mul_f32 v44, v44, v255\n\t"
    "v_mul_f32 v45, v45, v255\n\t"
    "v_mul_f32 v46, v46, v255\n\t"
    "v_mul_f32 v47, v47, v255\n\t"
    "s_add_u32 s58, s64, 4096\n\t"
    "s_waitcnt lgkmcnt(8)\n\t"
    "buffer_store_dwordx4 v[112:115], v242, s[84:87], s58 offen offset:0 nt\n\t"
    "v_mfma_f32_16x16x32_bf16 v[92:95], v[136:139], v[212:215], v[92:95]\n\t"
    "v_cvt_pk_bf16_f32 v44, v44, v45\n\t"
    "v_cvt_pk_bf16_f32 v45, v46, v47\n\t"
    "s_add_u32 s58, s64, 4096\n\t"
    "s_waitcnt lgkmcnt(6)\n\t"
    "buffer_store_dwordx4 v[116:119], v243, s[84:87], s58 offen offset:1024 nt\n\t"
    "v_mfma_f32_16x16x32_bf16 v[92:95], v[140:143], v[200:203], v[92:95]\n\t"
    "ds_write_b64 v253, v[44:45] offset:8192\n\t"
    "v_mul_f32 v56, v56, v255\n\t"
    "v_mul_f32 v57, v57, v255\n\t"
    "v_mul_f32 v58, v58, v255\n\t"
    "v_mul_f32 v59, v59, v255\n\t"
    "s_add_u32 s58, s64, 4096\n\t"
    "s_waitcnt lgkmcnt(5)\n\t"
    "buffer_store_dwordx4 v[120:123], v242, s[84:87], s58 offen offset:2048 nt\n\t"
    "v_cvt_pk_bf16_f32 v56, v56, v57\n\t"
    "v_cvt_pk_bf16_f32 v57, v58, v59\n\t"
    "s_add_u32 s58, s64, 4096\n\t"
    "s_waitcnt lgkmcnt(2)\n\t"
    "buffer_store_dwordx4 v[124:127], v243, s[84:87], s58 offen offset:3072 nt\n\t"
    "ds_write_b64 v250, v[56:57] offset:8320\n\t"
    "v_mul_f32 v68, v68, v255\n\t"
    "v_mul_f32 v69, v69, v255\n\t"
    "v_mul_f32 v70, v70, v255\n\t"
    "v_mul_f32 v71, v71, v255\n\t"
    "v_cvt_pk_bf16_f32 v68, v68, v69\n\t"
    "v_cvt_pk_bf16_f32 v69, v70, v71\n\t"
    "ds_write_b64 v251, v[68:69] offset:8320\n\t"
    "v_mul_f32 v80, v80, v255\n\t"
    "v_mul_f32 v81, v81, v255\n\t"
    "v_mul_f32 v82, v82, v255\n\t"
    "v_mul_f32 v83, v83, v255\n\t"
    "v_cvt_pk_bf16_f32 v80, v80, v81\n\t"
    "v_cvt_pk_bf16_f32 v81, v82, v83\n\t"
    "ds_write_b64 v252, v[80:81] offset:8320\n\t"
    "v_mul_f32 v92, v92, v255\n\t"
    "v_mul_f32 v93, v93, v255\n\t"
    "v_mul_f32 v94, v94, v255\n\t"
    "v_mul_f32 v95, v95, v255\n\t"
    "v_cvt_pk_bf16_f32 v92, v92, v93\n\t"
    "v_cvt_pk_bf16_f32 v93, v94, v95\n\t"
    "ds_write_b64 v253, v[92:93] offset:8320\n\t"
    "ds_read_b128 v[96:99], v232 offset:8192\n\t"
    "ds_read_b128 v[100:103], v232 offset:9216\n\t"
    "ds_read_b64 v[104:105], v232 offset:10248\n\t"
    "ds_read_b64 v[106:107], v232 offset:10240\n\t"
    "ds_read_b64 v[108:109], v232 offset:11272\n\t"
    "ds_read_b64 v[110:111], v232 offset:11264\n\t"
    "s_add_u32 s58, s64, 8192\n\t"
    "s_waitcnt lgkmcnt(5)\n\t"
    "buffer_store_dwordx4 v[96:99], v242, s[84:87], s58 offen offset:0 nt\n\t"
    "s_add_u32 s58, s64, 8192\n\t"
    "s_waitcnt lgkmcnt(4)\n\t"
    "buffer_store_dwordx4 v[100:103], v243, s[84:87], s58 offen offset:1024 nt\n\t"
    "s_add_u32 s58, s64, 8192\n\t"
    "s_waitcnt lgkmcnt(2)\n\t"
    "buffer_store_dwordx4 v[104:107], v242, s[84:87], s58 offen offset:2048 nt\n\t"
    "s_add_u32 s58, s64, 8192\n\t"
    "s_waitcnt lgkmcnt(0)\n\t"
    "buffer_store_dwordx4 v[108:111], v243, s[84:87], s58 offen offset:3072 nt\n\t"
    "s_endpgm\n\t"
    "LFBA1%=:\n\t"
    "s_getpc_b64 s[60:61]\n\t"
    "LPCA1%=:\n\t"
    "s_add_u32 s60, s60, LRETA1%=-LPCA1%=\n\t"
    "s_addc_u32 s61, s61, -1\n\t"
    "s_branch LFBC%=\n\t"
    "LFBA2%=:\n\t"
    "s_getpc_b64 s[60:61]\n\t"
    "LPCA2%=:\n\t"
    "s_add_u32 s60, s60, LRETA2%=-LPCA2%=\n\t"
    "s_addc_u32 s61, s61, -1\n\t"
    "s_branch LFBC%=\n\t"
    "LFBA3%=:\n\t"
    "s_getpc_b64 s[60:61]\n\t"
    "LPCA3%=:\n\t"
    "s_add_u32 s60, s60, LRETA3%=-LPCA3%=\n\t"
    "s_addc_u32 s61, s61, -1\n\t"
    "s_branch LFBC%=\n\t"
    "LFBA4%=:\n\t"
    "s_getpc_b64 s[60:61]\n\t"
    "LPCA4%=:\n\t"
    "s_add_u32 s60, s60, LRETA4%=-LPCA4%=\n\t"
    "s_addc_u32 s61, s61, -1\n\t"
    "s_branch LFBC%=\n\t"
    "LFBA5%=:\n\t"
    "s_getpc_b64 s[60:61]\n\t"
    "LPCA5%=:\n\t"
    "s_add_u32 s60, s60, LRETA5%=-LPCA5%=\n\t"
    "s_addc_u32 s61, s61, -1\n\t"
    "s_branch LFBC%=\n\t"
    "LFBA6%=:\n\t"
    "s_getpc_b64 s[60:61]\n\t"
    "LPCA6%=:\n\t"
    "s_add_u32 s60, s60, LRETA6%=-LPCA6%=\n\t"
    "s_addc_u32 s61, s61, -1\n\t"
    "s_branch LFBC%=\n\t"
    "LFBA7%=:\n\t"
    "s_getpc_b64 s[60:61]\n\t"
    "LPCA7%=:\n\t"
    "s_add_u32 s60, s60, LRETA7%=-LPCA7%=\n\t"
    "s_addc_u32 s61, s61, -1\n\t"
    "s_branch LFBC%=\n\t"
    "LSTARTB%=:\n\t"
    "v_lshlrev_b32 v244, 4, v150\n\t"
    "v_add_u32 v244, s55, v244\n\t"
    "v_mov_b32 v242, v156\n\t"
    "v_mov_b32 v243, v157\n\t"
    "s_barrier\n\t"
    "s_waitcnt vmcnt(1)\n\t"
    "v_xor_b32 v254, 64, v231\n\t"
    "ds_read_b128 v[96:99], v231 offset:0\n\t"
    "ds_read_b128 v[100:103], v254 offset:0\n\t"
    "ds_read_b128 v[104:107], v231 offset:128\n\t"
    "ds_read_b128 v[108:111], v254 offset:128\n\t"
    "ds_read_b128 v[112:115], v231 offset:4096\n\t"
    "ds_read_b128 v[116:119], v254 offset:4096\n\t"
    "ds_read_b128 v[120:123], v231 offset:4224\n\t"
    "ds_read_b128 v[124:127], v254 offset:4224\n\t"
    "ds_read_b128 v[128:131], v231 offset:8192\n\t"
    "ds_read_b128 v[132:135], v254 offset:8192\n\t"
    "ds_read_b128 v[136:139], v231 offset:8320\n\t"
    "ds_read_b128 v[140:143], v254 offset:8320\n\t"
    "s_waitcnt lgkmcnt(0)\n\t"
    "s_setprio 1\n\t"
    "ds_read_b128 v[180:183], v238 offset:0\n\t"
    "ds_read_b128 v[184:187], v239 offset:0\n\t"
    "ds_read_b128 v[188:191], v238 offset:1024\n\t"
    "ds_read_b128 v[216:219], v239 offset:1024\n\t"
    "s_waitcnt lgkmcnt(3)\n\t"
    "v_mfma_f32_16x16x32_bf16 v[144:147], v[180:183], v[96:99], 0\n\t"
    "ds_read_b128 v[204:207], v239 offset:5120\n\t"
    "s_waitcnt lgkmcnt(3)\n\t"
    "v_mfma_f32_16x16x32_bf16 v[144:147], v[184:187], v[100:103], v[144:147]\n\t"
    "ds_read_b128 v[200:203], v238 offset:5120\n\t"
    "s_waitcnt lgkmcnt(3)\n\t"
    "v_mfma_f32_16x16x32_bf16 v[144:147], v[188:191], v[104:107], v[144:147]\n\t"
    "ds_read_b128 v[196:199], v239 offset:4096\n\t"
    "s_waitcnt lgkmcnt(3)\n\t"
    "v_mfma_f32_16x16x32_bf16 v[144:147], v[216:219], v[108:111], v[144:147]\n\t"
    "ds_read_b128 v[192:195], v238 offset:4096\n\t"
    "v_mfma_f32_16x16x32_bf16 v[148:151], v[216:219], v[124:127], 0\n\t"
    "v_mfma_f32_16x16x32_bf16 v[148:151], v[188:191], v[120:123], v[148:151]\n\t"
    "v_mfma_f32_16x16x32_bf16 v[148:151], v[184:187], v[116:119], v[148:151]\n\t"
    "v_mfma_f32_16x16x32_bf16 v[148:151], v[180:183], v[112:115], v[148:151]\n\t"
    "v_mfma_f32_16x16x32_bf16 v[152:155], v[180:183], v[128:131], 0\n\t"
    "v_mfma_f32_16x16x32_bf16 v[152:155], v[184:187], v[132:135], v[152:155]\n\t"
    "v_mfma_f32_16x16x32_bf16 v[152:155], v[188:191], v[136:139], v[152:155]\n\t"
    "v_mfma_f32_16x16x32_bf16 v[152:155], v[216:219], v[140:143], v[152:155]\n\t"
    "s_waitcnt lgkmcnt(3)\n\t"
    "v_mfma_f32_16x16x32_bf16 v[164:167], v[204:207], v[140:143], 0\n\t"
    "ds_read_b128 v[180:183], v238 offset:8192\n\t"
    "s_waitcnt lgkmcnt(3)\n\t"
    "v_mfma_f32_16x16x32_bf16 v[164:167], v[200:203], v[136:139], v[164:167]\n\t"
    "ds_read_b128 v[184:187], v239 offset:8192\n\t"
    "s_waitcnt lgkmcnt(3)\n\t"
    "v_mfma_f32_16x16x32_bf16 v[164:167], v[196:199], v[132:135], v[164:167]\n\t"
    "ds_read_b128 v[188:191], v238 offset:9216\n\t"
    "s_waitcnt lgkmcnt(3)\n\t"
    "v_mfma_f32_16x16x32_bf16 v[164:167], v[192:195], v[128:131], v[164:167]\n\t"
    "ds_read_b128 v[216:219], v239 offset:9216\n\t"
    "v_mfma_f32_16x16x32_bf16 v[160:163], v[192:195], v[112:115], 0\n\t"
    "v_mfma_f32_16x16x32_bf16 v[160:163], v[196:199], v[116:119], v[160:163]\n\t"
    "v_mfma_f32_16x16x32_bf16 v[160:163], v[200:203], v[120:123], v[160:163]\n\t"
    "v_mfma_f32_16x16x32_bf16 v[160:163], v[204:207], v[124:127], v[160:163]\n\t"
    "v_mfma_f32_16x16x32_bf16 v[156:159], v[204:207], v[108:111], 0\n\t"
    "v_mfma_f32_16x16x32_bf16 v[156:159], v[200:203], v[104:107], v[156:159]\n\t"
    "v_mfma_f32_16x16x32_bf16 v[156:159], v[196:199], v[100:103], v[156:159]\n\t"
    "v_mfma_f32_16x16x32_bf16 v[156:159], v[192:195], v[96:99], v[156:159]\n\t"
    "s_waitcnt lgkmcnt(3)\n\t"
    "v_mfma_f32_16x16x32_bf16 v[168:171], v[180:183], v[96:99], 0\n\t"
    "ds_read_b128 v[204:207], v239 offset:13312\n\t"
    "s_waitcnt lgkmcnt(3)\n\t"
    "v_mfma_f32_16x16x32_bf16 v[168:171], v[184:187], v[100:103], v[168:171]\n\t"
    "ds_read_b128 v[200:203], v238 offset:13312\n\t"
    "s_waitcnt lgkmcnt(3)\n\t"
    "v_mfma_f32_16x16x32_bf16 v[168:171], v[188:191], v[104:107], v[168:171]\n\t"
    "ds_read_b128 v[196:199], v239 offset:12288\n\t"
    "s_waitcnt lgkmcnt(3)\n\t"
    "v_mfma_f32_16x16x32_bf16 v[168:171], v[216:219], v[108:111], v[168:171]\n\t"
    "ds_read_b128 v[192:195], v238 offset:12288\n\t"
    "v_mfma_f32_16x16x32_bf16 v[172:175], v[216:219], v[124:127], 0\n\t"
    "v_mfma_f32_16x16x32_bf16 v[172:175], v[188:191], v[120:123], v[172:175]\n\t"
    "v_mfma_f32_16x16x32_bf16 v[172:175], v[184:187], v[116:119], v[172:175]\n\t"
    "v_mfma_f32_16x16x32_bf16 v[172:175], v[180:183], v[112:115], v[172:175]\n\t"
    "v_mfma_f32_16x16x32_bf16 v[176:179], v[180:183], v[128:131], 0\n\t"
    "v_mfma_f32_16x16x32_bf16 v[176:179], v[184:187], v[132:135], v[176:179]\n\t"
    "v_mfma_f32_16x16x32_bf16 v[176:179], v[188:191], v[136:139], v[176:179]\n\t"
    "v_mfma_f32_16x16x32_bf16 v[176:179], v[216:219], v[140:143], v[176:179]\n\t"
    "s_waitcnt lgkmcnt(3)\n\t"
    "v_mfma_f32_16x16x32_bf16 v[188:191], v[204:207], v[140:143], 0\n\t"
    "s_waitcnt lgkmcnt(2)\n\t"
    "v_mfma_f32_16x16x32_bf16 v[188:191], v[200:203], v[136:139], v[188:191]\n\t"
    "s_waitcnt lgkmcnt(1)\n\t"
    "v_mfma_f32_16x16x32_bf16 v[188:191], v[196:199], v[132:135], v[188:191]\n\t"
    "s_waitcnt lgkmcnt(0)\n\t"
    "v_mfma_f32_16x16x32_bf16 v[188:191], v[192:195], v[128:131], v[188:191]\n\t"
    "v_mfma_f32_16x16x32_bf16 v[184:187], v[192:195], v[112:115], 0\n\t"
    "v_mfma_f32_16x16x32_bf16 v[184:187], v[196:199], v[116:119], v[184:187]\n\t"
    "v_mfma_f32_16x16x32_bf16 v[184:187], v[200:203], v[120:123], v[184:187]\n\t"
    "v_mfma_f32_16x16x32_bf16 v[184:187], v[204:207], v[124:127], v[184:187]\n\t"
    "v_mfma_f32_16x16x32_bf16 v[180:183], v[204:207], v[108:111], 0\n\t"
    "v_mfma_f32_16x16x32_bf16 v[180:183], v[200:203], v[104:107], v[180:183]\n\t"
    "v_mfma_f32_16x16x32_bf16 v[180:183], v[196:199], v[100:103], v[180:183]\n\t"
    "v_mfma_f32_16x16x32_bf16 v[180:183], v[192:195], v[96:99], v[180:183]\n\t"
    "s_waitcnt lgkmcnt(0)\n\t"
    "s_barrier\n\t"
    "s_setprio 0\n\t"
    "LBLKB%=:\n\t"
    "s_cmp_eq_u32 s56, 0\n\t"
    "s_cbranch_scc0 LCARB%=\n\t"
    "v_max3_f32 v232, v144, v145, v146\n\t"
    "v_max3_f32 v233, v148, v149, v150\n\t"
    "v_max3_f32 v234, v152, v153, v154\n\t"
    "v_max3_f32 v232, v232, v147, v156\n\t"
    "v_max3_f32 v233, v233, v151, v160\n\t"
    "v_max3_f32 v234, v234, v155, v164\n\t"
    "v_max3_f32 v232, v232, v157, v158\n\t"
    "v_max3_f32 v233, v233, v161, v162\n\t"
    "v_max3_f32 v234, v234, v165, v166\n\t"
    "v_max3_f32 v232, v232, v159, v168\n\t"
    "v_max3_f32 v233, v233, v163, v172\n\t"
    "v_max3_f32 v234, v234, v167, v176\n\t"
    "v_max3_f32 v232, v232, v169, v170\n\t"
    "v_max3_f32 v233, v233, v173, v174\n\t"
    "v_max3_f32 v234, v234, v177, v178\n\t"
    "v_max3_f32 v232, v232, v171, v180\n\t"
    "v_max3_f32 v233, v233, v175, v184\n\t"
    "v_max3_f32 v234, v234, v179, v188\n\t"
    "v_max3_f32 v232, v232, v181, v182\n\t"
    "v_max3_f32 v233, v233, v185, v186\n\t"
    "v_max3_f32 v234, v234, v189, v190\n\t"
    "v_max_f32 v232, v232, v183\n\t"
    "v_max_f32 v233, v233, v187\n\t"
    "v_max_f32 v234, v234, v191\n\t"
    "v_mov_b32 v224, v232\n\t"
    "v_mov_b32 v225, v233\n\t"
    "v_mov_b32 v226, v234\n\t"
    "v_permlane16_swap_b32 v232, v224\n\t"
    "v_permlane16_swap_b32 v233, v225\n\t"
    "v_permlane16_swap_b32 v234, v226\n\t"
    "v_max_f32 v232, v232, v224\n\t"
    "v_max_f32 v233, v233, v225\n\t"
    "v_max_f32 v234, v234, v226\n\t"
    "v_mov_b32 v224, v232\n\t"
    "v_mov_b32 v225, v233\n\t"
    "v_mov_b32 v226, v234\n\t"
    "v_permlane32_swap_b32 v232, v224\n\t"
    "v_permlane32_swap_b32 v233, v225\n\t"
    "v_permlane32_swap_b32 v234, v226\n\t"
    "v_max_f32 v232, v232, v224\n\t"
    "v_max_f32 v233, v233, v225\n\t"
    "v_max_f32 v234, v234, v226\n\t"
    "v_mul_f32_e64 v232, s48, -v232\n\t"
    "v_mul_f32_e64 v233, s48, -v233\n\t"
    "v_mul_f32_e64 v234, s48, -v234\n\t"
    "s_branch LCAR2B%=\n\t"
    "LCARB%=:\n\t"
    "v_max3_f32 v232, v144, v145, v146\n\t"
    "v_max3_f32 v233, v148, v149, v150\n\t"
    "v_max3_f32 v234, v152, v153, v154\n\t"
    "v_max3_f32 v232, v232, v147, v156\n\t"
    "v_max3_f32 v233, v233, v151, v160\n\t"
    "v_max3_f32 v234, v234, v155, v164\n\t"
    "v_max3_f32 v232, v232, v157, v158\n\t"
    "v_max3_f32 v233, v233, v161, v162\n\t"
    "v_max3_f32 v234, v234, v165, v166\n\t"
    "v_max3_f32 v232, v232, v159, v168\n\t"
    "v_max3_f32 v233, v233, v163, v172\n\t"
    "v_max3_f32 v234, v234, v167, v176\n\t"
    "v_max3_f32 v232, v232, v169, v170\n\t"
    "v_max3_f32 v233, v233, v173, v174\n\t"
    "v_max3_f32 v234, v234, v177, v178\n\t"
    "v_max3_f32 v232, v232, v171, v180\n\t"
    "v_max3_f32 v233, v233, v175, v184\n\t"
    "v_max3_f32 v234, v234, v179, v188\n\t"
    "v_max3_f32 v232, v232, v181, v182\n\t"
    "v_max3_f32 v233, v233, v185, v186\n\t"
    "v_max3_f32 v234, v234, v189, v190\n\t"
    "v_max_f32 v232, v232, v183\n\t"
    "v_max_f32 v233, v233, v187\n\t"
    "v_max_f32 v234, v234, v191\n\t"
    "v_mov_b32 v224, v232\n\t"
    "v_mov_b32 v225, v233\n\t"
    "v_mov_b32 v226, v234\n\t"
    "v_permlane16_swap_b32 v232, v224\n\t"
    "v_permlane16_swap_b32 v233, v225\n\t"
    "v_permlane16_swap_b32 v234, v226\n\t"
    "v_max_f32 v232, v232, v224\n\t"
    "v_max_f32 v233, v233, v225\n\t"
    "v_max_f32 v234, v234, v226\n\t"
    "v_mov_b32 v224, v232\n\t"
    "v_mov_b32 v225, v233\n\t"
    "v_mov_b32 v226, v234\n\t"
    "v_permlane32_swap_b32 v232, v224\n\t"
    "v_permlane32_swap_b32 v233, v225\n\t"
    "v_permlane32_swap_b32 v234, v226\n\t"
    "v_max_f32 v232, v232, v224\n\t"
    "v_max_f32 v233, v233, v225\n\t"
    "v_max_f32 v234, v234, v226\n\t"
    "v_mul_f32_e64 v232, s48, -v232\n\t"
    "v_mul_f32_e64 v233, s48, -v233\n\t"
    "v_mul_f32_e64 v234, s48, -v234\n\t"
    "LCAR2B%=:\n\t"
    "v_fma_f32 v220, v144, s48, v232\n\t"
    "v_fma_f32 v221, v145, s48, v232\n\t"
    "v_fma_f32 v222, v146, s48, v232\n\t"
    "v_fma_f32 v223, v147, s48, v232\n\t"
    "v_exp_f32 v220, v220\n\t"
    "v_exp_f32 v221, v221\n\t"
    "v_exp_f32 v222, v222\n\t"
    "v_exp_f32 v223, v223\n\t"
    "v_fma_f32 v224, v148, s48, v233\n\t"
    "v_fma_f32 v225, v149, s48, v233\n\t"
    "v_fma_f32 v226, v150, s48, v233\n\t"
    "v_fma_f32 v227, v151, s48, v233\n\t"
    "v_exp_f32 v224, v224\n\t"
    "v_exp_f32 v225, v225\n\t"
    "v_exp_f32 v226, v226\n\t"
    "v_exp_f32 v227, v227\n\t"
    "v_cvt_pk_bf16_f32 v192, v220, v221\n\t"
    "v_cvt_pk_bf16_f32 v193, v222, v223\n\t"
    "v_add_f32 v235, v220, v221\n\t"
    "v_add_f32 v235, v235, v222\n\t"
    "v_add_f32 v235, v235, v223\n\t"
    "v_fma_f32 v220, v152, s48, v234\n\t"
    "v_fma_f32 v221, v153, s48, v234\n\t"
    "v_fma_f32 v222, v154, s48, v234\n\t"
    "v_fma_f32 v223, v155, s48, v234\n\t"
    "v_exp_f32 v220, v220\n\t"
    "v_exp_f32 v221, v221\n\t"
    "v_exp_f32 v222, v222\n\t"
    "v_exp_f32 v223, v223\n\t"
    "v_cvt_pk_bf16_f32 v196, v224, v225\n\t"
    "v_cvt_pk_bf16_f32 v197, v226, v227\n\t"
    "v_add_f32 v236, v224, v225\n\t"
    "v_add_f32 v236, v236, v226\n\t"
    "v_add_f32 v236, v236, v227\n\t"
    "v_fma_f32 v224, v156, s48, v232\n\t"
    "v_fma_f32 v225, v157, s48, v232\n\t"
    "v_fma_f32 v226, v158, s48, v232\n\t"
    "v_fma_f32 v227, v159, s48, v232\n\t"
    "v_exp_f32 v224, v224\n\t"
    "v_exp_f32 v225, v225\n\t"
    "v_exp_f32 v226, v226\n\t"
    "v_exp_f32 v227, v227\n\t"
    "v_cvt_pk_bf16_f32 v200, v220, v221\n\t"
    "v_cvt_pk_bf16_f32 v201, v222, v223\n\t"
    "v_add_f32 v237, v220, v221\n\t"
    "v_add_f32 v237, v237, v222\n\t"
    "v_add_f32 v237, v237, v223\n\t"
    "v_fma_f32 v220, v160, s48, v233\n\t"
    "v_fma_f32 v221, v161, s48, v233\n\t"
    "v_fma_f32 v222, v162, s48, v233\n\t"
    "v_fma_f32 v223, v163, s48, v233\n\t"
    "v_exp_f32 v220, v220\n\t"
    "v_exp_f32 v221, v221\n\t"
    "v_exp_f32 v222, v222\n\t"
    "v_exp_f32 v223, v223\n\t"
    "v_cvt_pk_bf16_f32 v194, v224, v225\n\t"
    "v_cvt_pk_bf16_f32 v195, v226, v227\n\t"
    "v_add_f32 v235, v235, v224\n\t"
    "v_add_f32 v235, v235, v225\n\t"
    "v_add_f32 v235, v235, v226\n\t"
    "v_add_f32 v235, v235, v227\n\t"
    "v_fma_f32 v224, v164, s48, v234\n\t"
    "v_fma_f32 v225, v165, s48, v234\n\t"
    "v_fma_f32 v226, v166, s48, v234\n\t"
    "v_fma_f32 v227, v167, s48, v234\n\t"
    "v_exp_f32 v224, v224\n\t"
    "v_exp_f32 v225, v225\n\t"
    "v_exp_f32 v226, v226\n\t"
    "v_exp_f32 v227, v227\n\t"
    "v_cvt_pk_bf16_f32 v198, v220, v221\n\t"
    "v_cvt_pk_bf16_f32 v199, v222, v223\n\t"
    "v_add_f32 v236, v236, v220\n\t"
    "v_add_f32 v236, v236, v221\n\t"
    "v_add_f32 v236, v236, v222\n\t"
    "v_add_f32 v236, v236, v223\n\t"
    "v_fma_f32 v220, v168, s48, v232\n\t"
    "v_fma_f32 v221, v169, s48, v232\n\t"
    "v_fma_f32 v222, v170, s48, v232\n\t"
    "v_fma_f32 v223, v171, s48, v232\n\t"
    "v_exp_f32 v220, v220\n\t"
    "v_exp_f32 v221, v221\n\t"
    "v_exp_f32 v222, v222\n\t"
    "v_exp_f32 v223, v223\n\t"
    "v_cvt_pk_bf16_f32 v202, v224, v225\n\t"
    "v_cvt_pk_bf16_f32 v203, v226, v227\n\t"
    "v_add_f32 v237, v237, v224\n\t"
    "v_add_f32 v237, v237, v225\n\t"
    "v_add_f32 v237, v237, v226\n\t"
    "v_add_f32 v237, v237, v227\n\t"
    "v_fma_f32 v224, v172, s48, v233\n\t"
    "v_fma_f32 v225, v173, s48, v233\n\t"
    "v_fma_f32 v226, v174, s48, v233\n\t"
    "v_fma_f32 v227, v175, s48, v233\n\t"
    "v_exp_f32 v224, v224\n\t"
    "v_exp_f32 v225, v225\n\t"
    "v_exp_f32 v226, v226\n\t"
    "v_exp_f32 v227, v227\n\t"
    "v_cvt_pk_bf16_f32 v204, v220, v221\n\t"
    "v_cvt_pk_bf16_f32 v205, v222, v223\n\t"
    "v_add_f32 v235, v235, v220\n\t"
    "v_add_f32 v235, v235, v221\n\t"
    "v_add_f32 v235, v235, v222\n\t"
    "v_add_f32 v235, v235, v223\n\t"
    "v_fma_f32 v220, v176, s48, v234\n\t"
    "v_fma_f32 v221, v177, s48, v234\n\t"
    "v_fma_f32 v222, v178, s48, v234\n\t"
    "v_fma_f32 v223, v179, s48, v234\n\t"
    "v_exp_f32 v220, v220\n\t"
    "v_exp_f32 v221, v221\n\t"
    "v_exp_f32 v222, v222\n\t"
    "v_exp_f32 v223, v223\n\t"
    "v_cvt_pk_bf16_f32 v208, v224, v225\n\t"
    "v_cvt_pk_bf16_f32 v209, v226, v227\n\t"
    "v_add_f32 v236, v236, v224\n\t"
    "v_add_f32 v236, v236, v225\n\t"
    "v_add_f32 v236, v236, v226\n\t"
    "v_add_f32 v236, v236, v227\n\t"
    "v_fma_f32 v224, v180, s48, v232\n\t"
    "v_fma_f32 v225, v181, s48, v232\n\t"
    "v_fma_f32 v226, v182, s48, v232\n\t"
    "v_fma_f32 v227, v183, s48, v232\n\t"
    "v_exp_f32 v224, v224\n\t"
    "v_exp_f32 v225, v225\n\t"
    "v_exp_f32 v226, v226\n\t"
    "v_exp_f32 v227, v227\n\t"
    "v_cvt_pk_bf16_f32 v212, v220, v221\n\t"
    "v_cvt_pk_bf16_f32 v213, v222, v223\n\t"
    "v_add_f32 v237, v237, v220\n\t"
    "v_add_f32 v237, v237, v221\n\t"
    "v_add_f32 v237, v237, v222\n\t"
    "v_add_f32 v237, v237, v223\n\t"
    "v_fma_f32 v220, v184, s48, v233\n\t"
    "v_fma_f32 v221, v185, s48, v233\n\t"
    "v_fma_f32 v222, v186, s48, v233\n\t"
    "v_fma_f32 v223, v187, s48, v233\n\t"
    "v_exp_f32 v220, v220\n\t"
    "v_exp_f32 v221, v221\n\t"
    "v_exp_f32 v222, v222\n\t"
    "v_exp_f32 v223, v223\n\t"
    "v_cvt_pk_bf16_f32 v206, v224, v225\n\t"
    "v_cvt_pk_bf16_f32 v207, v226, v227\n\t"
    "v_add_f32 v235, v235, v224\n\t"
    "v_add_f32 v235, v235, v225\n\t"
    "v_add_f32 v235, v235, v226\n\t"
    "v_add_f32 v235, v235, v227\n\t"
    "v_fma_f32 v224, v188, s48, v234\n\t"
    "v_fma_f32 v225, v189, s48, v234\n\t"
    "v_fma_f32 v226, v190, s48, v234\n\t"
    "v_fma_f32 v227, v191, s48, v234\n\t"
    "v_exp_f32 v224, v224\n\t"
    "v_exp_f32 v225, v225\n\t"
    "v_exp_f32 v226, v226\n\t"
    "v_exp_f32 v227, v227\n\t"
    "v_cvt_pk_bf16_f32 v210, v220, v221\n\t"
    "v_cvt_pk_bf16_f32 v211, v222, v223\n\t"
    "v_add_f32 v236, v236, v220\n\t"
    "v_add_f32 v236, v236, v221\n\t"
    "v_add_f32 v236, v236, v222\n\t"
    "v_add_f32 v236, v236, v223\n\t"
    "v_cvt_pk_bf16_f32 v214, v224, v225\n\t"
    "v_cvt_pk_bf16_f32 v215, v226, v227\n\t"
    "v_add_f32 v237, v237, v224\n\t"
    "v_add_f32 v237, v237, v225\n\t"
    "v_add_f32 v237, v237, v226\n\t"
    "v_add_f32 v237, v237, v227\n\t"
    "s_waitcnt vmcnt(0)\n\t"
    "s_barrier\n\t"
    "s_setprio 1\n\t"
    "ds_read_b64_tr_b16 v[144:145], v240 offset:32768\n\t"
    "ds_read_b64_tr_b16 v[146:147], v240 offset:36864\n\t"
    "ds_read_b64_tr_b16 v[148:149], v240 offset:40960\n\t"
    "ds_read_b64_tr_b16 v[150:151], v240 offset:45056\n\t"
    "ds_read_b64_tr_b16 v[152:153], v241 offset:40960\n\t"
    "ds_read_b64_tr_b16 v[154:155], v241 offset:45056\n\t"
    "ds_read_b64_tr_b16 v[156:157], v241 offset:32768\n\t"
    "ds_read_b64_tr_b16 v[158:159], v241 offset:36864\n\t"
    "s_waitcnt lgkmcnt(6)\n\t"
    "v_mfma_f32_16x16x32_bf16 v[0:3], v[144:147], v[192:195], 0\n\t"
    "ds_read_b64_tr_b16 v[160:161], v240 offset:33280\n\t"
    "ds_read_b64_tr_b16 v[162:163], v240 offset:37376\n\t"
    "s_waitcnt lgkmcnt(6)\n\t"
    "v_mfma_f32_16x16x32_bf16 v[0:3], v[148:151], v[204:207], v[0:3]\n\t"
    "ds_read_b64_tr_b16 v[164:165], v240 offset:41472\n\t"
    "ds_read_b64_tr_b16 v[166:167], v240 offset:45568\n\t"
    "v_mfma_f32_16x16x32_bf16 v[4:7], v[148:151], v[208:211], 0\n\t"
    "ds_read_b64_tr_b16 v[168:169], v241 offset:41472\n\t"
    "ds_read_b64_tr_b16 v[170:171], v241 offset:45568\n\t"
    "v_mfma_f32_16x16x32_bf16 v[4:7], v[144:147], v[196:199], v[4:7]\n\t"
    "ds_read_b64_tr_b16 v[172:173], v241 offset:33280\n\t"
    "ds_read_b64_tr_b16 v[174:175], v241 offset:37376\n\t"
    "v_mfma_f32_16x16x32_bf16 v[8:11], v[144:147], v[200:203], 0\n\t"
    "ds_read_b128 v[246:249], v244 offset:0\n\t"
    "v_mfma_f32_16x16x32_bf16 v[8:11], v[148:151], v[212:215], v[8:11]\n\t"
    "s_waitcnt lgkmcnt(11)\n\t"
    "v_mfma_f32_16x16x32_bf16 v[20:23], v[152:155], v[212:215], 0\n\t"
    "s_waitcnt lgkmcnt(9)\n\t"
    "v_mfma_f32_16x16x32_bf16 v[20:23], v[156:159], v[200:203], v[20:23]\n\t"
    "v_mfma_f32_16x16x32_bf16 v[16:19], v[156:159], v[196:199], 0\n\t"
    "ds_read_b64_tr_b16 v[176:177], v240 offset:33792\n\t"
    "ds_read_b64_tr_b16 v[178:179], v240 offset:37888\n\t"
    "v_mfma_f32_16x16x32_bf16 v[16:19], v[152:155], v[208:211], v[16:19]\n\t"
    "ds_read_b64_tr_b16 v[144:145], v240 offset:41984\n\t"
    "ds_read_b64_tr_b16 v[146:147], v240 offset:46080\n\t"
    "v_mfma_f32_16x16x32_bf16 v[12:15], v[152:155], v[204:207], 0\n\t"
    "s_add_u32 s58, s54, 0\n\t"
    "v_mfma_f32_16x16x32_bf16 v[12:15], v[156:159], v[192:195], v[12:15]\n\t"
    "s_waitcnt lgkmcnt(11)\n\t"
    "v_mfma_f32_16x16x32_bf16 v[24:27], v[160:163], v[192:195], 0\n\t"
    "s_waitcnt lgkmcnt(9)\n\t"
    "v_mfma_f32_16x16x32_bf16 v[24:27], v[164:167], v[204:207], v[24:27]\n\t"
    "s_waitcnt lgkmcnt(4)\n\t"
    "buffer_store_dwordx4 v[246:249], v242, s[84:87], s58 offen offset:0 nt\n\t"
    "v_mfma_f32_16x16x32_bf16 v[28:31], v[164:167], v[208:211], 0\n\t"
    "ds_read_b64_tr_b16 v[148:149], v241 offset:41984\n\t"
    "ds_read_b64_tr_b16 v[150:151], v241 offset:46080\n\t"
    "v_mfma_f32_16x16x32_bf16 v[28:31], v[160:163], v[196:199], v[28:31]\n\t"
    "ds_read_b64_tr_b16 v[152:153], v241 offset:33792\n\t"
    "ds_read_b64_tr_b16 v[154:155], v241 offset:37888\n\t"
    "v_mfma_f32_16x16x32_bf16 v[32:35], v[160:163], v[200:203], 0\n\t"
    "s_add_u32 m0, s55, 0\n\t"
    "v_mfma_f32_16x16x32_bf16 v[32:35], v[164:167], v[212:215], v[32:35]\n\t"
    "v_mfma_f32_16x16x32_bf16 v[44:47], v[168:171], v[212:215], 0\n\t"
    "v_mfma_f32_16x16x32_bf16 v[44:47], v[172:175], v[200:203], v[44:47]\n\t"
    "s_add_u32 s57, s53, 0\n\t"
    "v_mfma_f32_16x16x32_bf16 v[40:43], v[172:175], v[196:199], 0\n\t"
    "ds_read_b64_tr_b16 v[156:157], v240 offset:34304\n\t"
    "ds_read_b64_tr_b16 v[158:159], v240 offset:38400\n\t"
    "v_mfma_f32_16x16x32_bf16 v[40:43], v[168:171], v[208:211], v[40:43]\n\t"
    "ds_read_b64_tr_b16 v[160:161], v240 offset:42496\n\t"
    "ds_read_b64_tr_b16 v[162:163], v240 offset:46592\n\t"
    "v_mfma_f32_16x16x32_bf16 v[36:39], v[168:171], v[204:207], 0\n\t"
    "buffer_load_dwordx4 v242, s[44:47], s57 offen offset:0 lds\n\t"
    "v_mfma_f32_16x16x32_bf16 v[36:39], v[172:175], v[192:195], v[36:39]\n\t"
    "s_waitcnt lgkmcnt(10)\n\t"
    "v_mfma_f32_16x16x32_bf16 v[48:51], v[176:179], v[192:195], 0\n\t"
    "s_waitcnt lgkmcnt(8)\n\t"
    "v_mfma_f32_16x16x32_bf16 v[48:51], v[144:147], v[204:207], v[48:51]\n\t"
    "ds_read_b128 v[246:249], v244 offset:1024\n\t"
    "v_mfma_f32_16x16x32_bf16 v[52:55], v[144:147], v[208:211], 0\n\t"
    "ds_read_b64_tr_b16 v[164:165], v241 offset:42496\n\t"
    "ds_read_b64_tr_b16 v[166:167], v241 offset:46592\n\t"
    "v_mfma_f32_16x16x32_bf16 v[52:55], v[176:179], v[196:199], v[52:55]\n\t"
    "ds_read_b64_tr_b16 v[168:169], v241 offset:34304\n\t"
    "ds_read_b64_tr_b16 v[170:171], v241 offset:38400\n\t"
    "v_mfma_f32_16x16x32_bf16 v[56:59], v[176:179], v[200:203], 0\n\t"
    "v_mfma_f32_16x16x32_bf16 v[56:59], v[144:147], v[212:215], v[56:59]\n\t"
    "s_waitcnt lgkmcnt(11)\n\t"
    "v_mfma_f32_16x16x32_bf16 v[68:71], v[148:151], v[212:215], 0\n\t"
    "s_waitcnt lgkmcnt(9)\n\t"
    "v_mfma_f32_16x16x32_bf16 v[68:71], v[152:155], v[200:203], v[68:71]\n\t"
    "s_add_u32 s58, s54, 0\n\t"
    "v_mfma_f32_16x16x32_bf16 v[64:67], v[152:155], v[196:199], 0\n\t"
    "ds_read_b128 v[180:183], v238 offset:16384\n\t"
    "v_mfma_f32_16x16x32_bf16 v[64:67], v[148:151], v[208:211], v[64:67]\n\t"
    "ds_read_b128 v[184:187], v239 offset:16384\n\t"
    "v_mfma_f32_16x16x32_bf16 v[60:63], v[148:151], v[204:207], 0\n\t"
    "ds_read_b128 v[188:191], v238 offset:17408\n\t"
    "v_mfma_f32_16x16x32_bf16 v[60:63], v[152:155], v[192:195], v[60:63]\n\t"
    "ds_read_b128 v[216:219], v239 offset:17408\n\t"
    "s_waitcnt lgkmcnt(11)\n\t"
    "v_mfma_f32_16x16x32_bf16 v[72:75], v[156:159], v[192:195], 0\n\t"
    "s_waitcnt lgkmcnt(9)\n\t"
    "v_mfma_f32_16x16x32_bf16 v[72:75], v[160:163], v[204:207], v[72:75]\n\t"
    "s_waitcnt lgkmcnt(8)\n\t"
    "buffer_store_dwordx4 v[246:249], v243, s[84:87], s58 offen offset:1024 nt\n\t"
    "v_mfma_f32_16x16x32_bf16 v[76:79], v[160:163], v[208:211], 0\n\t"
    "s_add_u32 m0, s55, 0\n\t"
    "v_mfma_f32_16x16x32_bf16 v[76:79], v[156:159], v[196:199], v[76:79]\n\t"
    "s_add_u32 s57, s53, 0\n\t"
    "v_mfma_f32_16x16x32_bf16 v[80:83], v[156:159], v[200:203], 0\n\t"
    "buffer_load_dwordx4 v243, s[44:47], s57 offen offset:1024 lds\n\t"
    "v_mfma_f32_16x16x32_bf16 v[80:83], v[160:163], v[212:215], v[80:83]\n\t"
    "s_waitcnt lgkmcnt(6)\n\t"
    "v_mfma_f32_16x16x32_bf16 v[92:95], v[164:167], v[212:215], 0\n\t"
    "s_waitcnt lgkmcnt(4)\n\t"
    "v_mfma_f32_16x16x32_bf16 v[92:95], v[168:171], v[200:203], v[92:95]\n\t"
    "ds_read_b64 v[246:247], v244 offset:2056\n\t"
    "v_mfma_f32_16x16x32_bf16 v[88:91], v[168:171], v[196:199], 0\n\t"
    "ds_read_b64 v[248:249], v244 offset:2048\n\t"
    "v_mfma_f32_16x16x32_bf16 v[88:91], v[164:167], v[208:211], v[88:91]\n\t"
    "v_mfma_f32_16x16x32_bf16 v[84:87], v[164:167], v[204:207], 0\n\t"
    "v_mfma_f32_16x16x32_bf16 v[84:87], v[168:171], v[192:195], v[84:87]\n\t"
    "ds_read_b128 v[204:207], v239 offset:21504\n\t"
    "ds_read_b128 v[200:203], v238 offset:21504\n\t"
    "s_waitcnt lgkmcnt(7)\n\t"
    "v_mfma_f32_16x16x32_bf16 v[144:147], v[180:183], v[96:99], 0\n\t"
    "ds_read_b128 v[196:199], v239 offset:20480\n\t"
    "ds_read_b128 v[192:195], v238 offset:20480\n\t"
    "s_waitcnt lgkmcnt(8)\n\t"
    "v_mfma_f32_16x16x32_bf16 v[144:147], v[184:187], v[100:103], v[144:147]\n\t"
    "s_waitcnt lgkmcnt(7)\n\t"
    "v_mfma_f32_16x16x32_bf16 v[144:147], v[188:191], v[104:107], v[144:147]\n\t"
    "s_waitcnt lgkmcnt(6)\n\t"
    "v_mfma_f32_16x16x32_bf16 v[144:147], v[216:219], v[108:111], v[144:147]\n\t"
    "s_add_u32 s58, s54, 0\n\t"
    "v_mfma_f32_16x16x32_bf16 v[148:151], v[216:219], v[124:127], 0\n\t"
    "v_mfma_f32_16x16x32_bf16 v[148:151], v[188:191], v[120:123], v[148:151]\n\t"
    "s_waitcnt lgkmcnt(4)\n\t"
    "buffer_store_dwordx4 v[246:249], v242, s[84:87], s58 offen offset:2048 nt\n\t"
    "v_mfma_f32_16x16x32_bf16 v[148:151], v[184:187], v[116:119], v[148:151]\n\t"
    "s_add_u32 m0, s55, 0\n\t"
    "v_mfma_f32_16x16x32_bf16 v[148:151], v[180:183], v[112:115], v[148:151]\n\t"
    "s_add_u32 s57, s53, 0\n\t"
    "v_mfma_f32_16x16x32_bf16 v[152:155], v[180:183], v[128:131], 0\n\t"
    "ds_read_b128 v[180:183], v238 offset:24576\n\t"
    "v_mfma_f32_16x16x32_bf16 v[152:155], v[184:187], v[132:135], v[152:155]\n\t"
    "ds_read_b128 v[184:187], v239 offset:24576\n\t"
    "v_mfma_f32_16x16x32_bf16 v[152:155], v[188:191], v[136:139], v[152:155]\n\t"
    "ds_read_b128 v[188:191], v238 offset:25600\n\t"
    "v_mfma_f32_16x16x32_bf16 v[152:155], v[216:219], v[140:143], v[152:155]\n\t"
    "ds_read_b128 v[216:219], v239 offset:25600\n\t"
    "s_waitcnt lgkmcnt(7)\n\t"
    "v_mfma_f32_16x16x32_bf16 v[164:167], v[204:207], v[140:143], 0\n\t"
    "s_waitcnt lgkmcnt(6)\n\t"
    "v_mfma_f32_16x16x32_bf16 v[164:167], v[200:203], v[136:139], v[164:167]\n\t"
    "s_waitcnt lgkmcnt(5)\n\t"
    "v_mfma_f32_16x16x32_bf16 v[164:167], v[196:199], v[132:135], v[164:167]\n\t"
    "s_waitcnt lgkmcnt(4)\n\t"
    "v_mfma_f32_16x16x32_bf16 v[164:167], v[192:195], v[128:131], v[164:167]\n\t"
    "buffer_load_dwordx4 v242, s[44:47], s57 offen offset:2048 lds\n\t"
    "v_mfma_f32_16x16x32_bf16 v[160:163], v[192:195], v[112:115], 0\n\t"
    "v_mfma_f32_16x16x32_bf16 v[160:163], v[196:199], v[116:119], v[160:163]\n\t"
    "v_mfma_f32_16x16x32_bf16 v[160:163], v[200:203], v[120:123], v[160:163]\n\t"
    "v_mfma_f32_16x16x32_bf16 v[160:163], v[204:207], v[124:127], v[160:163]\n\t"
    "v_mfma_f32_16x16x32_bf16 v[156:159], v[204:207], v[108:111], 0\n\t"
    "ds_read_b128 v[204:207], v239 offset:29696\n\t"
    "v_mfma_f32_16x16x32_bf16 v[156:159], v[200:203], v[104:107], v[156:159]\n\t"
    "ds_read_b128 v[200:203], v238 offset:29696\n\t"
    "v_mfma_f32_16x16x32_bf16 v[156:159], v[196:199], v[100:103], v[156:159]\n\t"
    "ds_read_b128 v[196:199], v239 offset:28672\n\t"
    "v_mfma_f32_16x16x32_bf16 v[156:159], v[192:195], v[96:99], v[156:159]\n\t"
    "ds_read_b128 v[192:195], v238 offset:28672\n\t"
    "s_waitcnt lgkmcnt(7)\n\t"
    "v_mfma_f32_16x16x32_bf16 v[168:171], v[180:183], v[96:99], 0\n\t"
    "s_waitcnt lgkmcnt(6)\n\t"
    "v_mfma_f32_16x16x32_bf16 v[168:171], v[184:187], v[100:103], v[168:171]\n\t"
    "s_waitcnt lgkmcnt(5)\n\t"
    "v_mfma_f32_16x16x32_bf16 v[168:171], v[188:191], v[104:107], v[168:171]\n\t"
    "s_waitcnt lgkmcnt(4)\n\t"
    "v_mfma_f32_16x16x32_bf16 v[168:171], v[216:219], v[108:111], v[168:171]\n\t"
    "v_mfma_f32_16x16x32_bf16 v[172:175], v[216:219], v[124:127], 0\n\t"
    "v_mfma_f32_16x16x32_bf16 v[172:175], v[188:191], v[120:123], v[172:175]\n\t"
    "v_mfma_f32_16x16x32_bf16 v[172:175], v[184:187], v[116:119], v[172:175]\n\t"
    "v_mfma_f32_16x16x32_bf16 v[172:175], v[180:183], v[112:115], v[172:175]\n\t"
    "v_mfma_f32_16x16x32_bf16 v[176:179], v[180:183], v[128:131], 0\n\t"
    "v_mfma_f32_16x16x32_bf16 v[176:179], v[184:187], v[132:135], v[176:179]\n\t"
    "v_mfma_f32_16x16x32_bf16 v[176:179], v[188:191], v[136:139], v[176:179]\n\t"
    "v_mfma_f32_16x16x32_bf16 v[176:179], v[216:219], v[140:143], v[176:179]\n\t"
    "s_waitcnt lgkmcnt(3)\n\t"
    "v_mfma_f32_16x16x32_bf16 v[188:191], v[204:207], v[140:143], 0\n\t"
    "s_waitcnt lgkmcnt(2)\n\t"
    "v_mfma_f32_16x16x32_bf16 v[188:191], v[200:203], v[136:139], v[188:191]\n\t"
    "s_waitcnt lgkmcnt(1)\n\t"
    "v_mfma_f32_16x16x32_bf16 v[188:191], v[196:199], v[132:135], v[188:191]\n\t"
    "s_waitcnt lgkmcnt(0)\n\t"
    "v_mfma_f32_16x16x32_bf16 v[188:191], v[192:195], v[128:131], v[188:191]\n\t"
    "v_mfma_f32_16x16x32_bf16 v[184:187], v[192:195], v[112:115], 0\n\t"
    "v_mfma_f32_16x16x32_bf16 v[184:187], v[196:199], v[116:119], v[184:187]\n\t"
    "v_mfma_f32_16x16x32_bf16 v[184:187], v[200:203], v[120:123], v[184:187]\n\t"
    "v_mfma_f32_16x16x32_bf16 v[184:187], v[204:207], v[124:127], v[184:187]\n\t"
    "v_mfma_f32_16x16x32_bf16 v[180:183], v[204:207], v[108:111], 0\n\t"
    "v_mfma_f32_16x16x32_bf16 v[180:183], v[200:203], v[104:107], v[180:183]\n\t"
    "v_mfma_f32_16x16x32_bf16 v[180:183], v[196:199], v[100:103], v[180:183]\n\t"
    "v_mfma_f32_16x16x32_bf16 v[180:183], v[192:195], v[96:99], v[180:183]\n\t"
    "s_waitcnt lgkmcnt(0)\n\t"
    "s_barrier\n\t"
    "s_setprio 0\n\t"
    "v_fma_f32 v220, v144, s48, v232\n\t"
    "v_fma_f32 v221, v145, s48, v232\n\t"
    "v_fma_f32 v222, v146, s48, v232\n\t"
    "v_fma_f32 v223, v147, s48, v232\n\t"
    "v_exp_f32 v220, v220\n\t"
    "v_exp_f32 v221, v221\n\t"
    "v_exp_f32 v222, v222\n\t"
    "v_exp_f32 v223, v223\n\t"
    "v_fma_f32 v224, v148, s48, v233\n\t"
    "v_fma_f32 v225, v149, s48, v233\n\t"
    "v_fma_f32 v226, v150, s48, v233\n\t"
    "v_fma_f32 v227, v151, s48, v233\n\t"
    "v_exp_f32 v224, v224\n\t"
    "v_exp_f32 v225, v225\n\t"
    "v_exp_f32 v226, v226\n\t"
    "v_exp_f32 v227, v227\n\t"
    "v_cvt_pk_bf16_f32 v192, v220, v221\n\t"
    "v_cvt_pk_bf16_f32 v193, v222, v223\n\t"
    "v_add_f32 v228, v220, v221\n\t"
    "v_add_f32 v228, v228, v222\n\t"
    "v_add_f32 v228, v228, v223\n\t"
    "v_fma_f32 v220, v152, s48, v234\n\t"
    "v_fma_f32 v221, v153, s48, v234\n\t"
    "v_fma_f32 v222, v154, s48, v234\n\t"
    "v_fma_f32 v223, v155, s48, v234\n\t"
    "v_exp_f32 v220, v220\n\t"
    "v_exp_f32 v221, v221\n\t"
    "v_exp_f32 v222, v222\n\t"
    "v_exp_f32 v223, v223\n\t"
    "v_cvt_pk_bf16_f32 v196, v224, v225\n\t"
    "v_cvt_pk_bf16_f32 v197, v226, v227\n\t"
    "v_add_f32 v229, v224, v225\n\t"
    "v_add_f32 v229, v229, v226\n\t"
    "v_add_f32 v229, v229, v227\n\t"
    "v_fma_f32 v224, v156, s48, v232\n\t"
    "v_fma_f32 v225, v157, s48, v232\n\t"
    "v_fma_f32 v226, v158, s48, v232\n\t"
    "v_fma_f32 v227, v159, s48, v232\n\t"
    "v_exp_f32 v224, v224\n\t"
    "v_exp_f32 v225, v225\n\t"
    "v_exp_f32 v226, v226\n\t"
    "v_exp_f32 v227, v227\n\t"
    "v_cvt_pk_bf16_f32 v200, v220, v221\n\t"
    "v_cvt_pk_bf16_f32 v201, v222, v223\n\t"
    "v_add_f32 v230, v220, v221\n\t"
    "v_add_f32 v230, v230, v222\n\t"
    "v_add_f32 v230, v230, v223\n\t"
    "v_fma_f32 v220, v160, s48, v233\n\t"
    "v_fma_f32 v221, v161, s48, v233\n\t"
    "v_fma_f32 v222, v162, s48, v233\n\t"
    "v_fma_f32 v223, v163, s48, v233\n\t"
    "v_exp_f32 v220, v220\n\t"
    "v_exp_f32 v221, v221\n\t"
    "v_exp_f32 v222, v222\n\t"
    "v_exp_f32 v223, v223\n\t"
    "v_cvt_pk_bf16_f32 v194, v224, v225\n\t"
    "v_cvt_pk_bf16_f32 v195, v226, v227\n\t"
    "v_add_f32 v228, v228, v224\n\t"
    "v_add_f32 v228, v228, v225\n\t"
    "v_add_f32 v228, v228, v226\n\t"
    "v_add_f32 v228, v228, v227\n\t"
    "v_fma_f32 v224, v164, s48, v234\n\t"
    "v_fma_f32 v225, v165, s48, v234\n\t"
    "v_fma_f32 v226, v166, s48, v234\n\t"
    "v_fma_f32 v227, v167, s48, v234\n\t"
    "v_exp_f32 v224, v224\n\t"
    "v_exp_f32 v225, v225\n\t"
    "v_exp_f32 v226, v226\n\t"
    "v_exp_f32 v227, v227\n\t"
    "v_cvt_pk_bf16_f32 v198, v220, v221\n\t"
    "v_cvt_pk_bf16_f32 v199, v222, v223\n\t"
    "v_add_f32 v229, v229, v220\n\t"
    "v_add_f32 v229, v229, v221\n\t"
    "v_add_f32 v229, v229, v222\n\t"
    "v_add_f32 v229, v229, v223\n\t"
    "v_fma_f32 v220, v168, s48, v232\n\t"
    "v_fma_f32 v221, v169, s48, v232\n\t"
    "v_fma_f32 v222, v170, s48, v232\n\t"
    "v_fma_f32 v223, v171, s48, v232\n\t"
    "v_exp_f32 v220, v220\n\t"
    "v_exp_f32 v221, v221\n\t"
    "v_exp_f32 v222, v222\n\t"
    "v_exp_f32 v223, v223\n\t"
    "v_cvt_pk_bf16_f32 v202, v224, v225\n\t"
    "v_cvt_pk_bf16_f32 v203, v226, v227\n\t"
    "v_add_f32 v230, v230, v224\n\t"
    "v_add_f32 v230, v230, v225\n\t"
    "v_add_f32 v230, v230, v226\n\t"
    "v_add_f32 v230, v230, v227\n\t"
    "v_fma_f32 v224, v172, s48, v233\n\t"
    "v_fma_f32 v225, v173, s48, v233\n\t"
    "v_fma_f32 v226, v174, s48, v233\n\t"
    "v_fma_f32 v227, v175, s48, v233\n\t"
    "v_exp_f32 v224, v224\n\t"
    "v_exp_f32 v225, v225\n\t"
    "v_exp_f32 v226, v226\n\t"
    "v_exp_f32 v227, v227\n\t"
    "v_cvt_pk_bf16_f32 v204, v220, v221\n\t"
    "v_cvt_pk_bf16_f32 v205, v222, v223\n\t"
    "v_add_f32 v228, v228, v220\n\t"
    "v_add_f32 v228, v228, v221\n\t"
    "v_add_f32 v228, v228, v222\n\t"
    "v_add_f32 v228, v228, v223\n\t"
    "v_fma_f32 v220, v176, s48, v234\n\t"
    "v_fma_f32 v221, v177, s48, v234\n\t"
    "v_fma_f32 v222, v178, s48, v234\n\t"
    "v_fma_f32 v223, v179, s48, v234\n\t"
    "v_exp_f32 v220, v220\n\t"
    "v_exp_f32 v221, v221\n\t"
    "v_exp_f32 v222, v222\n\t"
    "v_exp_f32 v223, v223\n\t"
    "v_cvt_pk_bf16_f32 v208, v224, v225\n\t"
    "v_cvt_pk_bf16_f32 v209, v226, v227\n\t"
    "v_add_f32 v229, v229, v224\n\t"
    "v_add_f32 v229, v229, v225\n\t"
    "v_add_f32 v229, v229, v226\n\t"
    "v_add_f32 v229, v229, v227\n\t"
    "v_fma_f32 v224, v180, s48, v232\n\t"
    "v_fma_f32 v225, v181, s48, v232\n\t"
    "v_fma_f32 v226, v182, s48, v232\n\t"
    "v_fma_f32 v227, v183, s48, v232\n\t"
    "v_exp_f32 v224, v224\n\t"
    "v_exp_f32 v225, v225\n\t"
    "v_exp_f32 v226, v226\n\t"
    "v_exp_f32 v227, v227\n\t"
    "v_cvt_pk_bf16_f32 v212, v220, v221\n\t"
    "v_cvt_pk_bf16_f32 v213, v222, v223\n\t"
    "v_add_f32 v230, v230, v220\n\t"
    "v_add_f32 v230, v230, v221\n\t"
    "v_add_f32 v230, v230, v222\n\t"
    "v_add_f32 v230, v230, v223\n\t"
    "v_fma_f32 v220, v184, s48, v233\n\t"
    "v_fma_f32 v221, v185, s48, v233\n\t"
    "v_fma_f32 v222, v186, s48, v233\n\t"
    "v_fma_f32 v223, v187, s48, v233\n\t"
    "v_exp_f32 v220, v220\n\t"
    "v_exp_f32 v221, v221\n\t"
    "v_exp_f32 v222, v222\n\t"
    "v_exp_f32 v223, v223\n\t"
    "v_cvt_pk_bf16_f32 v206, v224, v225\n\t"
    "v_cvt_pk_bf16_f32 v207, v226, v227\n\t"
    "v_add_f32 v228, v228, v224\n\t"
    "v_add_f32 v228, v228, v225\n\t"
    "v_add_f32 v228, v228, v226\n\t"
    "v_add_f32 v228, v228, v227\n\t"
    "v_fma_f32 v224, v188, s48, v234\n\t"
    "v_fma_f32 v225, v189, s48, v234\n\t"
    "v_fma_f32 v226, v190, s48, v234\n\t"
    "v_fma_f32 v227, v191, s48, v234\n\t"
    "v_exp_f32 v224, v224\n\t"
    "v_exp_f32 v225, v225\n\t"
    "v_exp_f32 v226, v226\n\t"
    "v_exp_f32 v227, v227\n\t"
    "v_cvt_pk_bf16_f32 v210, v220, v221\n\t"
    "v_cvt_pk_bf16_f32 v211, v222, v223\n\t"
    "v_add_f32 v229, v229, v220\n\t"
    "v_add_f32 v229, v229, v221\n\t"
    "v_add_f32 v229, v229, v222\n\t"
    "v_add_f32 v229, v229, v223\n\t"
    "v_cvt_pk_bf16_f32 v214, v224, v225\n\t"
    "v_cvt_pk_bf16_f32 v215, v226, v227\n\t"
    "v_add_f32 v230, v230, v224\n\t"
    "v_add_f32 v230, v230, v225\n\t"
    "v_add_f32 v230, v230, v226\n\t"
    "v_add_f32 v230, v230, v227\n\t"
    "v_max3_f32 v220, v228, v229, v230\n\t"
    "v_cmp_lt_f32 vcc, s49, v220\n\t"
    "s_cbranch_vccnz LFBB1%=\n\t"
    "LRETB1%=:\n\t"
    "v_add_f32 v235, v235, v228\n\t"
    "v_add_f32 v236, v236, v229\n\t"
    "v_add_f32 v237, v237, v230\n\t"
    "s_barrier\n\t"
    "s_setprio 1\n\t"
    "ds_read_b64_tr_b16 v[144:145], v240 offset:49152\n\t"
    "ds_read_b64_tr_b16 v[146:147], v240 offset:53248\n\t"
    "ds_read_b64_tr_b16 v[148:149], v240 offset:57344\n\t"
    "ds_read_b64_tr_b16 v[150:151], v240 offset:61440\n\t"
    "ds_read_b64_tr_b16 v[152:153], v241 offset:57344\n\t"
    "ds_read_b64_tr_b16 v[154:155], v241 offset:61440\n\t"
    "ds_read_b64_tr_b16 v[156:157], v241 offset:49152\n\t"
    "ds_read_b64_tr_b16 v[158:159], v241 offset:53248\n\t"
    "s_waitcnt lgkmcnt(6)\n\t"
    "v_mfma_f32_16x16x32_bf16 v[0:3], v[144:147], v[192:195], v[0:3]\n\t"
    "ds_read_b64_tr_b16 v[160:161], v240 offset:49664\n\t"
    "ds_read_b64_tr_b16 v[162:163], v240 offset:53760\n\t"
    "s_waitcnt lgkmcnt(6)\n\t"
    "v_mfma_f32_16x16x32_bf16 v[0:3], v[148:151], v[204:207], v[0:3]\n\t"
    "ds_read_b64_tr_b16 v[164:165], v240 offset:57856\n\t"
    "ds_read_b64_tr_b16 v[166:167], v240 offset:61952\n\t"
    "v_mfma_f32_16x16x32_bf16 v[4:7], v[148:151], v[208:211], v[4:7]\n\t"
    "ds_read_b64_tr_b16 v[168:169], v241 offset:57856\n\t"
    "ds_read_b64_tr_b16 v[170:171], v241 offset:61952\n\t"
    "v_mfma_f32_16x16x32_bf16 v[4:7], v[144:147], v[196:199], v[4:7]\n\t"
    "ds_read_b64_tr_b16 v[172:173], v241 offset:49664\n\t"
    "ds_read_b64_tr_b16 v[174:175], v241 offset:53760\n\t"
    "v_mfma_f32_16x16x32_bf16 v[8:11], v[144:147], v[200:203], v[8:11]\n\t"
    "ds_read_b64 v[246:247], v244 offset:3080\n\t"
    "v_mfma_f32_16x16x32_bf16 v[8:11], v[148:151], v[212:215], v[8:11]\n\t"
    "s_waitcnt lgkmcnt(11)\n\t"
    "v_mfma_f32_16x16x32_bf16 v[20:23], v[152:155], v[212:215], v[20:23]\n\t"
    "s_waitcnt lgkmcnt(9)\n\t"
    "v_mfma_f32_16x16x32_bf16 v[20:23], v[156:159], v[200:203], v[20:23]\n\t"
    "ds_read_b64 v[248:249], v244 offset:3072\n\t"
    "v_mfma_f32_16x16x32_bf16 v[16:19], v[156:159], v[196:199], v[16:19]\n\t"
    "ds_read_b64_tr_b16 v[176:177], v240 offset:50176\n\t"
    "ds_read_b64_tr_b16 v[178:179], v240 offset:54272\n\t"
    "v_mfma_f32_16x16x32_bf16 v[16:19], v[152:155], v[208:211], v[16:19]\n\t"
    "ds_read_b64_tr_b16 v[144:145], v240 offset:58368\n\t"
    "ds_read_b64_tr_b16 v[146:147], v240 offset:62464\n\t"
    "v_mfma_f32_16x16x32_bf16 v[12:15], v[152:155], v[204:207], v[12:15]\n\t"
    "v_mfma_f32_16x16x32_bf16 v[12:15], v[156:159], v[192:195], v[12:15]\n\t"
    "s_waitcnt lgkmcnt(12)\n\t"
    "v_mfma_f32_16x16x32_bf16 v[24:27], v[160:163], v[192:195], v[24:27]\n\t"
    "s_waitcnt lgkmcnt(10)\n\t"
    "v_mfma_f32_16x16x32_bf16 v[24:27], v[164:167], v[204:207], v[24:27]\n\t"
    "s_add_u32 s58, s54, 0\n\t"
    "v_mfma_f32_16x16x32_bf16 v[28:31], v[164:167], v[208:211], v[28:31]\n\t"
    "ds_read_b64_tr_b16 v[148:149], v241 offset:58368\n\t"
    "ds_read_b64_tr_b16 v[150:151], v241 offset:62464\n\t"
    "v_mfma_f32_16x16x32_bf16 v[28:31], v[160:163], v[196:199], v[28:31]\n\t"
    "ds_read_b64_tr_b16 v[152:153], v241 offset:50176\n\t"
    "ds_read_b64_tr_b16 v[154:155], v241 offset:54272\n\t"
    "v_mfma_f32_16x16x32_bf16 v[32:35], v[160:163], v[200:203], v[32:35]\n\t"
    "v_mfma_f32_16x16x32_bf16 v[32:35], v[164:167], v[212:215], v[32:35]\n\t"
    "s_waitcnt lgkmcnt(12)\n\t"
    "v_mfma_f32_16x16x32_bf16 v[44:47], v[168:171], v[212:215], v[44:47]\n\t"
    "s_waitcnt lgkmcnt(10)\n\t"
    "v_mfma_f32_16x16x32_bf16 v[44:47], v[172:175], v[200:203], v[44:47]\n\t"
    "s_waitcnt lgkmcnt(8)\n\t"
    "buffer_store_dwordx4 v[246:249], v243, s[84:87], s58 offen offset:3072 nt\n\t"
    "v_mfma_f32_16x16x32_bf16 v[40:43], v[172:175], v[196:199], v[40:43]\n\t"
    "ds_read_b64_tr_b16 v[156:157], v240 offset:50688\n\t"
    "ds_read_b64_tr_b16 v[158:159], v240 offset:54784\n\t"
    "v_mfma_f32_16x16x32_bf16 v[40:43], v[168:171], v[208:211], v[40:43]\n\t"
    "ds_read_b64_tr_b16 v[160:161], v240 offset:58880\n\t"
    "ds_read_b64_tr_b16 v[162:163], v240 offset:62976\n\t"
    "v_mfma_f32_16x16x32_bf16 v[36:39], v[168:171], v[204:207], v[36:39]\n\t"
    "s_add_u32 m0, s55, 0\n\t"
    "v_mfma_f32_16x16x32_bf16 v[36:39], v[172:175], v[192:195], v[36:39]\n\t"
    "s_waitcnt lgkmcnt(10)\n\t"
    "v_mfma_f32_16x16x32_bf16 v[48:51], v[176:179], v[192:195], v[48:51]\n\t"
    "s_waitcnt lgkmcnt(8)\n\t"
    "v_mfma_f32_16x16x32_bf16 v[48:51], v[144:147], v[204:207], v[48:51]\n\t"
    "s_add_u32 s57, s53, 0\n\t"
    "v_mfma_f32_16x16x32_bf16 v[52:55], v[144:147], v[208:211], v[52:55]\n\t"
    "ds_read_b64_tr_b16 v[164:165], v241 offset:58880\n\t"
    "ds_read_b64_tr_b16 v[166:167], v241 offset:62976\n\t"
    "v_mfma_f32_16x16x32_bf16 v[52:55], v[176:179], v[196:199], v[52:55]\n\t"
    "ds_read_b64_tr_b16 v[168:169], v241 offset:50688\n\t"
    "ds_read_b64_tr_b16 v[170:171], v241 offset:54784\n\t"
    "v_mfma_f32_16x16x32_bf16 v[56:59], v[176:179], v[200:203], v[56:59]\n\t"
    "buffer_load_dwordx4 v243, s[44:47], s57 offen offset:3072 lds\n\t"
    "v_mfma_f32_16x16x32_bf16 v[56:59], v[144:147], v[212:215], v[56:59]\n\t"
    "s_waitcnt lgkmcnt(10)\n\t"
    "v_mfma_f32_16x16x32_bf16 v[68:71], v[148:151], v[212:215], v[68:71]\n\t"
    "s_waitcnt lgkmcnt(8)\n\t"
    "v_mfma_f32_16x16x32_bf16 v[68:71], v[152:155], v[200:203], v[68:71]\n\t"
    "ds_read_b128 v[246:249], v244 offset:4096\n\t"
    "v_mfma_f32_16x16x32_bf16 v[64:67], v[152:155], v[196:199], v[64:67]\n\t"
    "ds_read_b128 v[180:183], v238 offset:0\n\t"
    "v_mfma_f32_16x16x32_bf16 v[64:67], v[148:151], v[208:211], v[64:67]\n\t"
    "ds_read_b128 v[184:187], v239 offset:0\n\t"
    "v_mfma_f32_16x16x32_bf16 v[60:63], v[148:151], v[204:207], v[60:63]\n\t"
    "ds_read_b128 v[188:191], v238 offset:1024\n\t"
    "v_mfma_f32_16x16x32_bf16 v[60:63], v[152:155], v[192:195], v[60:63]\n\t"
    "ds_read_b128 v[216:219], v239 offset:1024\n\t"
    "s_waitcnt lgkmcnt(11)\n\t"
    "v_mfma_f32_16x16x32_bf16 v[72:75], v[156:159], v[192:195], v[72:75]\n\t"
    "s_waitcnt lgkmcnt(9)\n\t"
    "v_mfma_f32_16x16x32_bf16 v[72:75], v[160:163], v[204:207], v[72:75]\n\t"
    "s_add_u32 s58, s54, 4096\n\t"
    "v_mfma_f32_16x16x32_bf16 v[76:79], v[160:163], v[208:211], v[76:79]\n\t"
    "v_mfma_f32_16x16x32_bf16 v[76:79], v[156:159], v[196:199], v[76:79]\n\t"
    "s_waitcnt lgkmcnt(4)\n\t"
    "buffer_store_dwordx4 v[246:249], v242, s[84:87], s58 offen offset:0 nt\n\t"
    "v_mfma_f32_16x16x32_bf16 v[80:83], v[156:159], v[200:203], v[80:83]\n\t"
    "s_add_u32 m0, s55, 4096\n\t"
    "v_mfma_f32_16x16x32_bf16 v[80:83], v[160:163], v[212:215], v[80:83]\n\t"
    "v_mfma_f32_16x16x32_bf16 v[92:95], v[164:167], v[212:215], v[92:95]\n\t"
    "v_mfma_f32_16x16x32_bf16 v[92:95], v[168:171], v[200:203], v[92:95]\n\t"
    "s_add_u32 s57, s53, 4096\n\t"
    "v_mfma_f32_16x16x32_bf16 v[88:91], v[168:171], v[196:199], v[88:91]\n\t"
    "buffer_load_dwordx4 v242, s[44:47], s57 offen offset:0 lds\n\t"
    "v_mfma_f32_16x16x32_bf16 v[88:91], v[164:167], v[208:211], v[88:91]\n\t"
    "v_mfma_f32_16x16x32_bf16 v[84:87], v[164:167], v[204:207], v[84:87]\n\t"
    "v_mfma_f32_16x16x32_bf16 v[84:87], v[168:171], v[192:195], v[84:87]\n\t"
    "ds_read_b128 v[204:207], v239 offset:5120\n\t"
    "ds_read_b128 v[200:203], v238 offset:5120\n\t"
    "s_waitcnt lgkmcnt(5)\n\t"
    "v_mfma_f32_16x16x32_bf16 v[144:147], v[180:183], v[96:99], 0\n\t"
    "ds_read_b128 v[196:199], v239 offset:4096\n\t"
    "ds_read_b128 v[192:195], v238 offset:4096\n\t"
    "s_waitcnt lgkmcnt(6)\n\t"
    "v_mfma_f32_16x16x32_bf16 v[144:147], v[184:187], v[100:103], v[144:147]\n\t"
    "s_waitcnt lgkmcnt(5)\n\t"
    "v_mfma_f32_16x16x32_bf16 v[144:147], v[188:191], v[104:107], v[144:147]\n\t"
    "s_waitcnt lgkmcnt(4)\n\t"
    "v_mfma_f32_16x16x32_bf16 v[144:147], v[216:219], v[108:111], v[144:147]\n\t"
    "ds_read_b128 v[246:249], v244 offset:5120\n\t"
    "v_mfma_f32_16x16x32_bf16 v[148:151], v[216:219], v[124:127], 0\n\t"
    "v_mfma_f32_16x16x32_bf16 v[148:151], v[188:191], v[120:123], v[148:151]\n\t"
    "v_mfma_f32_16x16x32_bf16 v[148:151], v[184:187], v[116:119], v[148:151]\n\t"
    "v_mfma_f32_16x16x32_bf16 v[148:151], v[180:183], v[112:115], v[148:151]\n\t"
    "s_add_u32 s58, s54, 4096\n\t"
    "v_mfma_f32_16x16x32_bf16 v[152:155], v[180:183], v[128:131], 0\n\t"
    "ds_read_b128 v[180:183], v238 offset:8192\n\t"
    "v_mfma_f32_16x16x32_bf16 v[152:155], v[184:187], v[132:135], v[152:155]\n\t"
    "ds_read_b128 v[184:187], v239 offset:8192\n\t"
    "v_mfma_f32_16x16x32_bf16 v[152:155], v[188:191], v[136:139], v[152:155]\n\t"
    "ds_read_b128 v[188:191], v238 offset:9216\n\t"
    "v_mfma_f32_16x16x32_bf16 v[152:155], v[216:219], v[140:143], v[152:155]\n\t"
    "ds_read_b128 v[216:219], v239 offset:9216\n\t"
    "s_waitcnt lgkmcnt(8)\n\t"
    "v_mfma_f32_16x16x32_bf16 v[164:167], v[204:207], v[140:143], 0\n\t"
    "s_waitcnt lgkmcnt(7)\n\t"
    "v_mfma_f32_16x16x32_bf16 v[164:167], v[200:203], v[136:139], v[164:167]\n\t"
    "s_waitcnt lgkmcnt(6)\n\t"
    "v_mfma_f32_16x16x32_bf16 v[164:167], v[196:199], v[132:135], v[164:167]\n\t"
    "s_waitcnt lgkmcnt(5)\n\t"
    "v_mfma_f32_16x16x32_bf16 v[164:167], v[192:195], v[128:131], v[164:167]\n\t"
    "s_waitcnt lgkmcnt(4)\n\t"
    "buffer_store_dwordx4 v[246:249], v243, s[84:87], s58 offen offset:1024 nt\n\t"
    "v_mfma_f32_16x16x32_bf16 v[160:163], v[192:195], v[112:115], 0\n\t"
    "s_add_u32 m0, s55, 4096\n\t"
    "v_mfma_f32_16x16x32_bf16 v[160:163], v[196:199], v[116:119], v[160:163]\n\t"
    "s_add_u32 s57, s53, 4096\n\t"
    "v_mfma_f32_16x16x32_bf16 v[160:163], v[200:203], v[120:123], v[160:163]\n\t"
    "buffer_load_dwordx4 v243, s[44:47], s57 offen offset:1024 lds\n\t"
    "v_mfma_f32_16x16x32_bf16 v[160:163], v[204:207], v[124:127], v[160:163]\n\t"
    "v_mfma_f32_16x16x32_bf16 v[156:159], v[204:207], v[108:111], 0\n\t"
    "ds_read_b128 v[204:207], v239 offset:13312\n\t"
    "v_mfma_f32_16x16x32_bf16 v[156:159], v[200:203], v[104:107], v[156:159]\n\t"
    "ds_read_b128 v[200:203], v238 offset:13312\n\t"
    "v_mfma_f32_16x16x32_bf16 v[156:159], v[196:199], v[100:103], v[156:159]\n\t"
    "ds_read_b128 v[196:199], v239 offset:12288\n\t"
    "v_mfma_f32_16x16x32_bf16 v[156:159], v[192:195], v[96:99], v[156:159]\n\t"
    "ds_read_b128 v[192:195], v238 offset:12288\n\t"
    "s_waitcnt lgkmcnt(7)\n\t"
    "v_mfma_f32_16x16x32_bf16 v[168:171], v[180:183], v[96:99], 0\n\t"
    "s_waitcnt lgkmcnt(6)\n\t"
    "v_mfma_f32_16x16x32_bf16 v[168:171], v[184:187], v[100:103], v[168:171]\n\t"
    "s_waitcnt lgkmcnt(5)\n\t"
    "v_mfma_f32_16x16x32_bf16 v[168:171], v[188:191], v[104:107], v[168:171]\n\t"
    "s_waitcnt lgkmcnt(4)\n\t"
    "v_mfma_f32_16x16x32_bf16 v[168:171], v[216:219], v[108:111], v[168:171]\n\t"
    "v_mfma_f32_16x16x32_bf16 v[172:175], v[216:219], v[124:127], 0\n\t"
    "v_mfma_f32_16x16x32_bf16 v[172:175], v[188:191], v[120:123], v[172:175]\n\t"
    "v_mfma_f32_16x16x32_bf16 v[172:175], v[184:187], v[116:119], v[172:175]\n\t"
    "v_mfma_f32_16x16x32_bf16 v[172:175], v[180:183], v[112:115], v[172:175]\n\t"
    "v_mfma_f32_16x16x32_bf16 v[176:179], v[180:183], v[128:131], 0\n\t"
    "v_mfma_f32_16x16x32_bf16 v[176:179], v[184:187], v[132:135], v[176:179]\n\t"
    "v_mfma_f32_16x16x32_bf16 v[176:179], v[188:191], v[136:139], v[176:179]\n\t"
    "v_mfma_f32_16x16x32_bf16 v[176:179], v[216:219], v[140:143], v[176:179]\n\t"
    "s_waitcnt lgkmcnt(3)\n\t"
    "v_mfma_f32_16x16x32_bf16 v[188:191], v[204:207], v[140:143], 0\n\t"
    "s_waitcnt lgkmcnt(2)\n\t"
    "v_mfma_f32_16x16x32_bf16 v[188:191], v[200:203], v[136:139], v[188:191]\n\t"
    "s_waitcnt lgkmcnt(1)\n\t"
    "v_mfma_f32_16x16x32_bf16 v[188:191], v[196:199], v[132:135], v[188:191]\n\t"
    "s_waitcnt lgkmcnt(0)\n\t"
    "v_mfma_f32_16x16x32_bf16 v[188:191], v[192:195], v[128:131], v[188:191]\n\t"
    "v_mfma_f32_16x16x32_bf16 v[184:187], v[192:195], v[112:115], 0\n\t"
    "v_mfma_f32_16x16x32_bf16 v[184:187], v[196:199], v[116:119], v[184:187]\n\t"
    "v_mfma_f32_16x16x32_bf16 v[184:187], v[200:203], v[120:123], v[184:187]\n\t"
    "v_mfma_f32_16x16x32_bf16 v[184:187], v[204:207], v[124:127], v[184:187]\n\t"
    "v_mfma_f32_16x16x32_bf16 v[180:183], v[204:207], v[108:111], 0\n\t"
    "v_mfma_f32_16x16x32_bf16 v[180:183], v[200:203], v[104:107], v[180:183]\n\t"
    "v_mfma_f32_16x16x32_bf16 v[180:183], v[196:199], v[100:103], v[180:183]\n\t"
    "v_mfma_f32_16x16x32_bf16 v[180:183], v[192:195], v[96:99], v[180:183]\n\t"
    "s_waitcnt lgkmcnt(0)\n\t"
    "s_barrier\n\t"
    "s_setprio 0\n\t"
    "v_fma_f32 v220, v144, s48, v232\n\t"
    "v_fma_f32 v221, v145, s48, v232\n\t"
    "v_fma_f32 v222, v146, s48, v232\n\t"
    "v_fma_f32 v223, v147, s48, v232\n\t"
    "v_exp_f32 v220, v220\n\t"
    "v_exp_f32 v221, v221\n\t"
    "v_exp_f32 v222, v222\n\t"
    "v_exp_f32 v223, v223\n\t"
    "v_fma_f32 v224, v148, s48, v233\n\t"
    "v_fma_f32 v225, v149, s48, v233\n\t"
    "v_fma_f32 v226, v150, s48, v233\n\t"
    "v_fma_f32 v227, v151, s48, v233\n\t"
    "v_exp_f32 v224, v224\n\t"
    "v_exp_f32 v225, v225\n\t"
    "v_exp_f32 v226, v226\n\t"
    "v_exp_f32 v227, v227\n\t"
    "v_cvt_pk_bf16_f32 v192, v220, v221\n\t"
    "v_cvt_pk_bf16_f32 v193, v222, v223\n\t"
    "v_add_f32 v228, v220, v221\n\t"
    "v_add_f32 v228, v228, v222\n\t"
    "v_add_f32 v228, v228, v223\n\t"
    "v_fma_f32 v220, v152, s48, v234\n\t"
    "v_fma_f32 v221, v153, s48, v234\n\t"
    "v_fma_f32 v222, v154, s48, v234\n\t"
    "v_fma_f32 v223, v155, s48, v234\n\t"
    "v_exp_f32 v220, v220\n\t"
    "v_exp_f32 v221, v221\n\t"
    "v_exp_f32 v222, v222\n\t"
    "v_exp_f32 v223, v223\n\t"
    "v_cvt_pk_bf16_f32 v196, v224, v225\n\t"
    "v_cvt_pk_bf16_f32 v197, v226, v227\n\t"
    "v_add_f32 v229, v224, v225\n\t"
    "v_add_f32 v229, v229, v226\n\t"
    "v_add_f32 v229, v229, v227\n\t"
    "v_fma_f32 v224, v156, s48, v232\n\t"
    "v_fma_f32 v225, v157, s48, v232\n\t"
    "v_fma_f32 v226, v158, s48, v232\n\t"
    "v_fma_f32 v227, v159, s48, v232\n\t"
    "v_exp_f32 v224, v224\n\t"
    "v_exp_f32 v225, v225\n\t"
    "v_exp_f32 v226, v226\n\t"
    "v_exp_f32 v227, v227\n\t"
    "v_cvt_pk_bf16_f32 v200, v220, v221\n\t"
    "v_cvt_pk_bf16_f32 v201, v222, v223\n\t"
    "v_add_f32 v230, v220, v221\n\t"
    "v_add_f32 v230, v230, v222\n\t"
    "v_add_f32 v230, v230, v223\n\t"
    "v_fma_f32 v220, v160, s48, v233\n\t"
    "v_fma_f32 v221, v161, s48, v233\n\t"
    "v_fma_f32 v222, v162, s48, v233\n\t"
    "v_fma_f32 v223, v163, s48, v233\n\t"
    "v_exp_f32 v220, v220\n\t"
    "v_exp_f32 v221, v221\n\t"
    "v_exp_f32 v222, v222\n\t"
    "v_exp_f32 v223, v223\n\t"
    "v_cvt_pk_bf16_f32 v194, v224, v225\n\t"
    "v_cvt_pk_bf16_f32 v195, v226, v227\n\t"
    "v_add_f32 v228, v228, v224\n\t"
    "v_add_f32 v228, v228, v225\n\t"
    "v_add_f32 v228, v228, v226\n\t"
    "v_add_f32 v228, v228, v227\n\t"
    "v_fma_f32 v224, v164, s48, v234\n\t"
    "v_fma_f32 v225, v165, s48, v234\n\t"
    "v_fma_f32 v226, v166, s48, v234\n\t"
    "v_fma_f32 v227, v167, s48, v234\n\t"
    "v_exp_f32 v224, v224\n\t"
    "v_exp_f32 v225, v225\n\t"
    "v_exp_f32 v226, v226\n\t"
    "v_exp_f32 v227, v227\n\t"
    "v_cvt_pk_bf16_f32 v198, v220, v221\n\t"
    "v_cvt_pk_bf16_f32 v199, v222, v223\n\t"
    "v_add_f32 v229, v229, v220\n\t"
    "v_add_f32 v229, v229, v221\n\t"
    "v_add_f32 v229, v229, v222\n\t"
    "v_add_f32 v229, v229, v223\n\t"
    "v_fma_f32 v220, v168, s48, v232\n\t"
    "v_fma_f32 v221, v169, s48, v232\n\t"
    "v_fma_f32 v222, v170, s48, v232\n\t"
    "v_fma_f32 v223, v171, s48, v232\n\t"
    "v_exp_f32 v220, v220\n\t"
    "v_exp_f32 v221, v221\n\t"
    "v_exp_f32 v222, v222\n\t"
    "v_exp_f32 v223, v223\n\t"
    "v_cvt_pk_bf16_f32 v202, v224, v225\n\t"
    "v_cvt_pk_bf16_f32 v203, v226, v227\n\t"
    "v_add_f32 v230, v230, v224\n\t"
    "v_add_f32 v230, v230, v225\n\t"
    "v_add_f32 v230, v230, v226\n\t"
    "v_add_f32 v230, v230, v227\n\t"
    "v_fma_f32 v224, v172, s48, v233\n\t"
    "v_fma_f32 v225, v173, s48, v233\n\t"
    "v_fma_f32 v226, v174, s48, v233\n\t"
    "v_fma_f32 v227, v175, s48, v233\n\t"
    "v_exp_f32 v224, v224\n\t"
    "v_exp_f32 v225, v225\n\t"
    "v_exp_f32 v226, v226\n\t"
    "v_exp_f32 v227, v227\n\t"
    "v_cvt_pk_bf16_f32 v204, v220, v221\n\t"
    "v_cvt_pk_bf16_f32 v205, v222, v223\n\t"
    "v_add_f32 v228, v228, v220\n\t"
    "v_add_f32 v228, v228, v221\n\t"
    "v_add_f32 v228, v228, v222\n\t"
    "v_add_f32 v228, v228, v223\n\t"
    "v_fma_f32 v220, v176, s48, v234\n\t"
    "v_fma_f32 v221, v177, s48, v234\n\t"
    "v_fma_f32 v222, v178, s48, v234\n\t"
    "v_fma_f32 v223, v179, s48, v234\n\t"
    "v_exp_f32 v220, v220\n\t"
    "v_exp_f32 v221, v221\n\t"
    "v_exp_f32 v222, v222\n\t"
    "v_exp_f32 v223, v223\n\t"
    "v_cvt_pk_bf16_f32 v208, v224, v225\n\t"
    "v_cvt_pk_bf16_f32 v209, v226, v227\n\t"
    "v_add_f32 v229, v229, v224\n\t"
    "v_add_f32 v229, v229, v225\n\t"
    "v_add_f32 v229, v229, v226\n\t"
    "v_add_f32 v229, v229, v227\n\t"
    "v_fma_f32 v224, v180, s48, v232\n\t"
    "v_fma_f32 v225, v181, s48, v232\n\t"
    "v_fma_f32 v226, v182, s48, v232\n\t"
    "v_fma_f32 v227, v183, s48, v232\n\t"
    "v_exp_f32 v224, v224\n\t"
    "v_exp_f32 v225, v225\n\t"
    "v_exp_f32 v226, v226\n\t"
    "v_exp_f32 v227, v227\n\t"
    "v_cvt_pk_bf16_f32 v212, v220, v221\n\t"
    "v_cvt_pk_bf16_f32 v213, v222, v223\n\t"
    "v_add_f32 v230, v230, v220\n\t"
    "v_add_f32 v230, v230, v221\n\t"
    "v_add_f32 v230, v230, v222\n\t"
    "v_add_f32 v230, v230, v223\n\t"
    "v_fma_f32 v220, v184, s48, v233\n\t"
    "v_fma_f32 v221, v185, s48, v233\n\t"
    "v_fma_f32 v222, v186, s48, v233\n\t"
    "v_fma_f32 v223, v187, s48, v233\n\t"
    "v_exp_f32 v220, v220\n\t"
    "v_exp_f32 v221, v221\n\t"
    "v_exp_f32 v222, v222\n\t"
    "v_exp_f32 v223, v223\n\t"
    "v_cvt_pk_bf16_f32 v206, v224, v225\n\t"
    "v_cvt_pk_bf16_f32 v207, v226, v227\n\t"
    "v_add_f32 v228, v228, v224\n\t"
    "v_add_f32 v228, v228, v225\n\t"
    "v_add_f32 v228, v228, v226\n\t"
    "v_add_f32 v228, v228, v227\n\t"
    "v_fma_f32 v224, v188, s48, v234\n\t"
    "v_fma_f32 v225, v189, s48, v234\n\t"
    "v_fma_f32 v226, v190, s48, v234\n\t"
    "v_fma_f32 v227, v191, s48, v234\n\t"
    "v_exp_f32 v224, v224\n\t"
    "v_exp_f32 v225, v225\n\t"
    "v_exp_f32 v226, v226\n\t"
    "v_exp_f32 v227, v227\n\t"
    "v_cvt_pk_bf16_f32 v210, v220, v221\n\t"
    "v_cvt_pk_bf16_f32 v211, v222, v223\n\t"
    "v_add_f32 v229, v229, v220\n\t"
    "v_add_f32 v229, v229, v221\n\t"
    "v_add_f32 v229, v229, v222\n\t"
    "v_add_f32 v229, v229, v223\n\t"
    "v_cvt_pk_bf16_f32 v214, v224, v225\n\t"
    "v_cvt_pk_bf16_f32 v215, v226, v227\n\t"
    "v_add_f32 v230, v230, v224\n\t"
    "v_add_f32 v230, v230, v225\n\t"
    "v_add_f32 v230, v230, v226\n\t"
    "v_add_f32 v230, v230, v227\n\t"
    "v_max3_f32 v220, v228, v229, v230\n\t"
    "v_cmp_lt_f32 vcc, s49, v220\n\t"
    "s_cbranch_vccnz LFBB2%=\n\t"
    "LRETB2%=:\n\t"
    "v_add_f32 v235, v235, v228\n\t"
    "v_add_f32 v236, v236, v229\n\t"
    "v_add_f32 v237, v237, v230\n\t"
    "s_barrier\n\t"
    "s_setprio 1\n\t"
    "ds_read_b64_tr_b16 v[144:145], v240 offset:32768\n\t"
    "ds_read_b64_tr_b16 v[146:147], v240 offset:36864\n\t"
    "ds_read_b64_tr_b16 v[148:149], v240 offset:40960\n\t"
    "ds_read_b64_tr_b16 v[150:151], v240 offset:45056\n\t"
    "ds_read_b64_tr_b16 v[152:153], v241 offset:40960\n\t"
    "ds_read_b64_tr_b16 v[154:155], v241 offset:45056\n\t"
    "ds_read_b64_tr_b16 v[156:157], v241 offset:32768\n\t"
    "ds_read_b64_tr_b16 v[158:159], v241 offset:36864\n\t"
    "s_waitcnt lgkmcnt(6)\n\t"
    "v_mfma_f32_16x16x32_bf16 v[0:3], v[144:147], v[192:195], v[0:3]\n\t"
    "ds_read_b64_tr_b16 v[160:161], v240 offset:33280\n\t"
    "ds_read_b64_tr_b16 v[162:163], v240 offset:37376\n\t"
    "s_waitcnt lgkmcnt(6)\n\t"
    "v_mfma_f32_16x16x32_bf16 v[0:3], v[148:151], v[204:207], v[0:3]\n\t"
    "ds_read_b64_tr_b16 v[164:165], v240 offset:41472\n\t"
    "ds_read_b64_tr_b16 v[166:167], v240 offset:45568\n\t"
    "v_mfma_f32_16x16x32_bf16 v[4:7], v[148:151], v[208:211], v[4:7]\n\t"
    "ds_read_b64_tr_b16 v[168:169], v241 offset:41472\n\t"
    "ds_read_b64_tr_b16 v[170:171], v241 offset:45568\n\t"
    "v_mfma_f32_16x16x32_bf16 v[4:7], v[144:147], v[196:199], v[4:7]\n\t"
    "ds_read_b64_tr_b16 v[172:173], v241 offset:33280\n\t"
    "ds_read_b64_tr_b16 v[174:175], v241 offset:37376\n\t"
    "v_mfma_f32_16x16x32_bf16 v[8:11], v[144:147], v[200:203], v[8:11]\n\t"
    "ds_read_b64 v[246:247], v244 offset:6152\n\t"
    "v_mfma_f32_16x16x32_bf16 v[8:11], v[148:151], v[212:215], v[8:11]\n\t"
    "s_waitcnt lgkmcnt(11)\n\t"
    "v_mfma_f32_16x16x32_bf16 v[20:23], v[152:155], v[212:215], v[20:23]\n\t"
    "s_waitcnt lgkmcnt(9)\n\t"
    "v_mfma_f32_16x16x32_bf16 v[20:23], v[156:159], v[200:203], v[20:23]\n\t"
    "ds_read_b64 v[248:249], v244 offset:6144\n\t"
    "v_mfma_f32_16x16x32_bf16 v[16:19], v[156:159], v[196:199], v[16:19]\n\t"
    "ds_read_b64_tr_b16 v[176:177], v240 offset:33792\n\t"
    "ds_read_b64_tr_b16 v[178:179], v240 offset:37888\n\t"
    "v_mfma_f32_16x16x32_bf16 v[16:19], v[152:155], v[208:211], v[16:19]\n\t"
    "ds_read_b64_tr_b16 v[144:145], v240 offset:41984\n\t"
    "ds_read_b64_tr_b16 v[146:147], v240 offset:46080\n\t"
    "v_mfma_f32_16x16x32_bf16 v[12:15], v[152:155], v[204:207], v[12:15]\n\t"
    "v_mfma_f32_16x16x32_bf16 v[12:15], v[156:159], v[192:195], v[12:15]\n\t"
    "s_waitcnt lgkmcnt(12)\n\t"
    "v_mfma_f32_16x16x32_bf16 v[24:27], v[160:163], v[192:195], v[24:27]\n\t"
    "s_waitcnt lgkmcnt(10)\n\t"
    "v_mfma_f32_16x16x32_bf16 v[24:27], v[164:167], v[204:207], v[24:27]\n\t"
    "s_add_u32 s58, s54, 4096\n\t"
    "v_mfma_f32_16x16x32_bf16 v[28:31], v[164:167], v[208:211], v[28:31]\n\t"
    "ds_read_b64_tr_b16 v[148:149], v241 offset:41984\n\t"
    "ds_read_b64_tr_b16 v[150:151], v241 offset:46080\n\t"
    "v_mfma_f32_16x16x32_bf16 v[28:31], v[160:163], v[196:199], v[28:31]\n\t"
    "ds_read_b64_tr_b16 v[152:153], v241 offset:33792\n\t"
    "ds_read_b64_tr_b16 v[154:155], v241 offset:37888\n\t"
    "v_mfma_f32_16x16x32_bf16 v[32:35], v[160:163], v[200:203], v[32:35]\n\t"
    "v_mfma_f32_16x16x32_bf16 v[32:35], v[164:167], v[212:215], v[32:35]\n\t"
    "s_waitcnt lgkmcnt(12)\n\t"
    "v_mfma_f32_16x16x32_bf16 v[44:47], v[168:171], v[212:215], v[44:47]\n\t"
    "s_waitcnt lgkmcnt(10)\n\t"
    "v_mfma_f32_16x16x32_bf16 v[44:47], v[172:175], v[200:203], v[44:47]\n\t"
    "s_waitcnt lgkmcnt(8)\n\t"
    "buffer_store_dwordx4 v[246:249], v242, s[84:87], s58 offen offset:2048 nt\n\t"
    "v_mfma_f32_16x16x32_bf16 v[40:43], v[172:175], v[196:199], v[40:43]\n\t"
    "ds_read_b64_tr_b16 v[156:157], v240 offset:34304\n\t"
    "ds_read_b64_tr_b16 v[158:159], v240 offset:38400\n\t"
    "v_mfma_f32_16x16x32_bf16 v[40:43], v[168:171], v[208:211], v[40:43]\n\t"
    "ds_read_b64_tr_b16 v[160:161], v240 offset:42496\n\t"
    "ds_read_b64_tr_b16 v[162:163], v240 offset:46592\n\t"
    "v_mfma_f32_16x16x32_bf16 v[36:39], v[168:171], v[204:207], v[36:39]\n\t"
    "s_add_u32 m0, s55, 4096\n\t"
    "v_mfma_f32_16x16x32_bf16 v[36:39], v[172:175], v[192:195], v[36:39]\n\t"
    "s_waitcnt lgkmcnt(10)\n\t"
    "v_mfma_f32_16x16x32_bf16 v[48:51], v[176:179], v[192:195], v[48:51]\n\t"
    "s_waitcnt lgkmcnt(8)\n\t"
    "v_mfma_f32_16x16x32_bf16 v[48:51], v[144:147], v[204:207], v[48:51]\n\t"
    "s_add_u32 s57, s53, 4096\n\t"
    "v_mfma_f32_16x16x32_bf16 v[52:55], v[144:147], v[208:211], v[52:55]\n\t"
    "ds_read_b64_tr_b16 v[164:165], v241 offset:42496\n\t"
    "ds_read_b64_tr_b16 v[166:167], v241 offset:46592\n\t"
    "v_mfma_f32_16x16x32_bf16 v[52:55], v[176:179], v[196:199], v[52:55]\n\t"
    "ds_read_b64_tr_b16 v[168:169], v241 offset:34304\n\t"
    "ds_read_b64_tr_b16 v[170:171], v241 offset:38400\n\t"
    "v_mfma_f32_16x16x32_bf16 v[56:59], v[176:179], v[200:203], v[56:59]\n\t"
    "buffer_load_dwordx4 v242, s[44:47], s57 offen offset:2048 lds\n\t"
    "v_mfma_f32_16x16x32_bf16 v[56:59], v[144:147], v[212:215], v[56:59]\n\t"
    "s_waitcnt lgkmcnt(10)\n\t"
    "v_mfma_f32_16x16x32_bf16 v[68:71], v[148:151], v[212:215], v[68:71]\n\t"
    "s_waitcnt lgkmcnt(8)\n\t"
    "v_mfma_f32_16x16x32_bf16 v[68:71], v[152:155], v[200:203], v[68:71]\n\t"
    "ds_read_b64 v[246:247], v244 offset:7176\n\t"
    "v_mfma_f32_16x16x32_bf16 v[64:67], v[152:155], v[196:199], v[64:67]\n\t"
    "ds_read_b128 v[180:183], v238 offset:16384\n\t"
    "v_mfma_f32_16x16x32_bf16 v[64:67], v[148:151], v[208:211], v[64:67]\n\t"
    "ds_read_b128 v[184:187], v239 offset:16384\n\t"
    "v_mfma_f32_16x16x32_bf16 v[60:63], v[148:151], v[204:207], v[60:63]\n\t"
    "ds_read_b128 v[188:191], v238 offset:17408\n\t"
    "v_mfma_f32_16x16x32_bf16 v[60:63], v[152:155], v[192:195], v[60:63]\n\t"
    "ds_read_b128 v[216:219], v239 offset:17408\n\t"
    "s_waitcnt lgkmcnt(11)\n\t"
    "v_mfma_f32_16x16x32_bf16 v[72:75], v[156:159], v[192:195], v[72:75]\n\t"
    "s_waitcnt lgkmcnt(9)\n\t"
    "v_mfma_f32_16x16x32_bf16 v[72:75], v[160:163], v[204:207], v[72:75]\n\t"
    "ds_read_b64 v[248:249], v244 offset:7168\n\t"
    "v_mfma_f32_16x16x32_bf16 v[76:79], v[160:163], v[208:211], v[76:79]\n\t"
    "v_mfma_f32_16x16x32_bf16 v[76:79], v[156:159], v[196:199], v[76:79]\n\t"
    "v_mfma_f32_16x16x32_bf16 v[80:83], v[156:159], v[200:203], v[80:83]\n\t"
    "v_mfma_f32_16x16x32_bf16 v[80:83], v[160:163], v[212:215], v[80:83]\n\t"
    "s_waitcnt lgkmcnt(8)\n\t"
    "v_mfma_f32_16x16x32_bf16 v[92:95], v[164:167], v[212:215], v[92:95]\n\t"
    "s_waitcnt lgkmcnt(6)\n\t"
    "v_mfma_f32_16x16x32_bf16 v[92:95], v[168:171], v[200:203], v[92:95]\n\t"
    "s_add_u32 s58, s54, 4096\n\t"
    "v_mfma_f32_16x16x32_bf16 v[88:91], v[168:171], v[196:199], v[88:91]\n\t"
    "v_mfma_f32_16x16x32_bf16 v[88:91], v[164:167], v[208:211], v[88:91]\n\t"
    "s_waitcnt lgkmcnt(0)\n\t"
    "buffer_store_dwordx4 v[246:249], v243, s[84:87], s58 offen offset:3072 nt\n\t"
    "v_mfma_f32_16x16x32_bf16 v[84:87], v[164:167], v[204:207], v[84:87]\n\t"
    "s_add_u32 m0, s55, 4096\n\t"
    "v_mfma_f32_16x16x32_bf16 v[84:87], v[168:171], v[192:195], v[84:87]\n\t"
    "ds_read_b128 v[204:207], v239 offset:21504\n\t"
    "ds_read_b128 v[200:203], v238 offset:21504\n\t"
    "v_mfma_f32_16x16x32_bf16 v[144:147], v[180:183], v[96:99], 0\n\t"
    "ds_read_b128 v[196:199], v239 offset:20480\n\t"
    "ds_read_b128 v[192:195], v238 offset:20480\n\t"
    "v_mfma_f32_16x16x32_bf16 v[144:147], v[184:187], v[100:103], v[144:147]\n\t"
    "v_mfma_f32_16x16x32_bf16 v[144:147], v[188:191], v[104:107], v[144:147]\n\t"
    "v_mfma_f32_16x16x32_bf16 v[144:147], v[216:219], v[108:111], v[144:147]\n\t"
    "s_add_u32 s57, s53, 4096\n\t"
    "v_mfma_f32_16x16x32_bf16 v[148:151], v[216:219], v[124:127], 0\n\t"
    "buffer_load_dwordx4 v243, s[44:47], s57 offen offset:3072 lds\n\t"
    "v_mfma_f32_16x16x32_bf16 v[148:151], v[188:191], v[120:123], v[148:151]\n\t"
    "v_mfma_f32_16x16x32_bf16 v[148:151], v[184:187], v[116:119], v[148:151]\n\t"
    "v_mfma_f32_16x16x32_bf16 v[148:151], v[180:183], v[112:115], v[148:151]\n\t"
    "ds_read_b128 v[246:249], v244 offset:8192\n\t"
    "v_mfma_f32_16x16x32_bf16 v[152:155], v[180:183], v[128:131], 0\n\t"
    "ds_read_b128 v[180:183], v238 offset:24576\n\t"
    "v_mfma_f32_16x16x32_bf16 v[152:155], v[184:187], v[132:135], v[152:155]\n\t"
    "ds_read_b128 v[184:187], v239 offset:24576\n\t"
    "v_mfma_f32_16x16x32_bf16 v[152:155], v[188:191], v[136:139], v[152:155]\n\t"
    "ds_read_b128 v[188:191], v238 offset:25600\n\t"
    "v_mfma_f32_16x16x32_bf16 v[152:155], v[216:219], v[140:143], v[152:155]\n\t"
    "ds_read_b128 v[216:219], v239 offset:25600\n\t"
    "s_waitcnt lgkmcnt(8)\n\t"
    "v_mfma_f32_16x16x32_bf16 v[164:167], v[204:207], v[140:143], 0\n\t"
    "s_waitcnt lgkmcnt(7)\n\t"
    "v_mfma_f32_16x16x32_bf16 v[164:167], v[200:203], v[136:139], v[164:167]\n\t"
    "s_waitcnt lgkmcnt(6)\n\t"
    "v_mfma_f32_16x16x32_bf16 v[164:167], v[196:199], v[132:135], v[164:167]\n\t"
    "s_waitcnt lgkmcnt(5)\n\t"
    "v_mfma_f32_16x16x32_bf16 v[164:167], v[192:195], v[128:131], v[164:167]\n\t"
    "s_add_u32 s58, s54, 8192\n\t"
    "v_mfma_f32_16x16x32_bf16 v[160:163], v[192:195], v[112:115], 0\n\t"
    "v_mfma_f32_16x16x32_bf16 v[160:163], v[196:199], v[116:119], v[160:163]\n\t"
    "s_waitcnt lgkmcnt(4)\n\t"
    "buffer_store_dwordx4 v[246:249], v242, s[84:87], s58 offen offset:0 nt\n\t"
    "v_mfma_f32_16x16x32_bf16 v[160:163], v[200:203], v[120:123], v[160:163]\n\t"
    "s_add_u32 m0, s55, 8192\n\t"
    "v_mfma_f32_16x16x32_bf16 v[160:163], v[204:207], v[124:127], v[160:163]\n\t"
    "s_add_u32 s57, s53, 8192\n\t"
    "v_mfma_f32_16x16x32_bf16 v[156:159], v[204:207], v[108:111], 0\n\t"
    "ds_read_b128 v[204:207], v239 offset:29696\n\t"
    "v_mfma_f32_16x16x32_bf16 v[156:159], v[200:203], v[104:107], v[156:159]\n\t"
    "ds_read_b128 v[200:203], v238 offset:29696\n\t"
    "v_mfma_f32_16x16x32_bf16 v[156:159], v[196:199], v[100:103], v[156:159]\n\t"
    "ds_read_b128 v[196:199], v239 offset:28672\n\t"
    "v_mfma_f32_16x16x32_bf16 v[156:159], v[192:195], v[96:99], v[156:159]\n\t"
    "ds_read_b128 v[192:195], v238 offset:28672\n\t"
    "s_waitcnt lgkmcnt(7)\n\t"
    "v_mfma_f32_16x16x32_bf16 v[168:171], v[180:183], v[96:99], 0\n\t"
    "s_waitcnt lgkmcnt(6)\n\t"
    "v_mfma_f32_16x16x32_bf16 v[168:171], v[184:187], v[100:103], v[168:171]\n\t"
    "s_waitcnt lgkmcnt(5)\n\t"
    "v_mfma_f32_16x16x32_bf16 v[168:171], v[188:191], v[104:107], v[168:171]\n\t"
    "s_waitcnt lgkmcnt(4)\n\t"
    "v_mfma_f32_16x16x32_bf16 v[168:171], v[216:219], v[108:111], v[168:171]\n\t"
    "buffer_load_dwordx4 v242, s[44:47], s57 offen offset:0 lds\n\t"
    "v_mfma_f32_16x16x32_bf16 v[172:175], v[216:219], v[124:127], 0\n\t"
    "v_mfma_f32_16x16x32_bf16 v[172:175], v[188:191], v[120:123], v[172:175]\n\t"
    "v_mfma_f32_16x16x32_bf16 v[172:175], v[184:187], v[116:119], v[172:175]\n\t"
    "ds_read_b128 v[246:249], v244 offset:9216\n\t"
    "v_mfma_f32_16x16x32_bf16 v[172:175], v[180:183], v[112:115], v[172:175]\n\t"
    "v_mfma_f32_16x16x32_bf16 v[176:179], v[180:183], v[128:131], 0\n\t"
    "v_mfma_f32_16x16x32_bf16 v[176:179], v[184:187], v[132:135], v[176:179]\n\t"
    "v_mfma_f32_16x16x32_bf16 v[176:179], v[188:191], v[136:139], v[176:179]\n\t"
    "s_add_u32 s58, s54, 8192\n\t"
    "v_mfma_f32_16x16x32_bf16 v[176:179], v[216:219], v[140:143], v[176:179]\n\t"
    "s_waitcnt lgkmcnt(4)\n\t"
    "v_mfma_f32_16x16x32_bf16 v[188:191], v[204:207], v[140:143], 0\n\t"
    "s_waitcnt lgkmcnt(3)\n\t"
    "v_mfma_f32_16x16x32_bf16 v[188:191], v[200:203], v[136:139], v[188:191]\n\t"
    "s_waitcnt lgkmcnt(2)\n\t"
    "v_mfma_f32_16x16x32_bf16 v[188:191], v[196:199], v[132:135], v[188:191]\n\t"
    "s_waitcnt lgkmcnt(1)\n\t"
    "v_mfma_f32_16x16x32_bf16 v[188:191], v[192:195], v[128:131], v[188:191]\n\t"
    "s_waitcnt lgkmcnt(0)\n\t"
    "buffer_store_dwordx4 v[246:249], v243, s[84:87], s58 offen offset:1024 nt\n\t"
    "v_mfma_f32_16x16x32_bf16 v[184:187], v[192:195], v[112:115], 0\n\t"
    "s_add_u32 m0, s55, 8192\n\t"
    "v_mfma_f32_16x16x32_bf16 v[184:187], v[196:199], v[116:119], v[184:187]\n\t"
    "s_add_u32 s57, s53, 8192\n\t"
    "v_mfma_f32_16x16x32_bf16 v[184:187], v[200:203], v[120:123], v[184:187]\n\t"
    "buffer_load_dwordx4 v243, s[44:47], s57 offen offset:1024 lds\n\t"
    "v_mfma_f32_16x16x32_bf16 v[184:187], v[204:207], v[124:127], v[184:187]\n\t"
    "v_mfma_f32_16x16x32_bf16 v[180:183], v[204:207], v[108:111], 0\n\t"
    "v_mfma_f32_16x16x32_bf16 v[180:183], v[200:203], v[104:107], v[180:183]\n\t"
    "v_mfma_f32_16x16x32_bf16 v[180:183], v[196:199], v[100:103], v[180:183]\n\t"
    "v_mfma_f32_16x16x32_bf16 v[180:183], v[192:195], v[96:99], v[180:183]\n\t"
    "s_waitcnt lgkmcnt(0)\n\t"
    "s_barrier\n\t"
    "s_setprio 0\n\t"
    "v_fma_f32 v220, v144, s48, v232\n\t"
    "v_fma_f32 v221, v145, s48, v232\n\t"
    "v_fma_f32 v222, v146, s48, v232\n\t"
    "v_fma_f32 v223, v147, s48, v232\n\t"
    "v_exp_f32 v220, v220\n\t"
    "v_exp_f32 v221, v221\n\t"
    "v_exp_f32 v222, v222\n\t"
    "v_exp_f32 v223, v223\n\t"
    "v_fma_f32 v224, v148, s48, v233\n\t"
    "v_fma_f32 v225, v149, s48, v233\n\t"
    "v_fma_f32 v226, v150, s48, v233\n\t"
    "v_fma_f32 v227, v151, s48, v233\n\t"
    "v_exp_f32 v224, v224\n\t"
    "v_exp_f32 v225, v225\n\t"
    "v_exp_f32 v226, v226\n\t"
    "v_exp_f32 v227, v227\n\t"
    "v_cvt_pk_bf16_f32 v192, v220, v221\n\t"
    "v_cvt_pk_bf16_f32 v193, v222, v223\n\t"
    "v_add_f32 v228, v220, v221\n\t"
    "v_add_f32 v228, v228, v222\n\t"
    "v_add_f32 v228, v228, v223\n\t"
    "v_fma_f32 v220, v152, s48, v234\n\t"
    "v_fma_f32 v221, v153, s48, v234\n\t"
    "v_fma_f32 v222, v154, s48, v234\n\t"
    "v_fma_f32 v223, v155, s48, v234\n\t"
    "v_exp_f32 v220, v220\n\t"
    "v_exp_f32 v221, v221\n\t"
    "v_exp_f32 v222, v222\n\t"
    "v_exp_f32 v223, v223\n\t"
    "v_cvt_pk_bf16_f32 v196, v224, v225\n\t"
    "v_cvt_pk_bf16_f32 v197, v226, v227\n\t"
    "v_add_f32 v229, v224, v225\n\t"
    "v_add_f32 v229, v229, v226\n\t"
    "v_add_f32 v229, v229, v227\n\t"
    "v_fma_f32 v224, v156, s48, v232\n\t"
    "v_fma_f32 v225, v157, s48, v232\n\t"
    "v_fma_f32 v226, v158, s48, v232\n\t"
    "v_fma_f32 v227, v159, s48, v232\n\t"
    "v_exp_f32 v224, v224\n\t"
    "v_exp_f32 v225, v225\n\t"
    "v_exp_f32 v226, v226\n\t"
    "v_exp_f32 v227, v227\n\t"
    "v_cvt_pk_bf16_f32 v200, v220, v221\n\t"
    "v_cvt_pk_bf16_f32 v201, v222, v223\n\t"
    "v_add_f32 v230, v220, v221\n\t"
    "v_add_f32 v230, v230, v222\n\t"
    "v_add_f32 v230, v230, v223\n\t"
    "v_fma_f32 v220, v160, s48, v233\n\t"
    "v_fma_f32 v221, v161, s48, v233\n\t"
    "v_fma_f32 v222, v162, s48, v233\n\t"
    "v_fma_f32 v223, v163, s48, v233\n\t"
    "v_exp_f32 v220, v220\n\t"
    "v_exp_f32 v221, v221\n\t"
    "v_exp_f32 v222, v222\n\t"
    "v_exp_f32 v223, v223\n\t"
    "v_cvt_pk_bf16_f32 v194, v224, v225\n\t"
    "v_cvt_pk_bf16_f32 v195, v226, v227\n\t"
    "v_add_f32 v228, v228, v224\n\t"
    "v_add_f32 v228, v228, v225\n\t"
    "v_add_f32 v228, v228, v226\n\t"
    "v_add_f32 v228, v228, v227\n\t"
    "v_fma_f32 v224, v164, s48, v234\n\t"
    "v_fma_f32 v225, v165, s48, v234\n\t"
    "v_fma_f32 v226, v166, s48, v234\n\t"
    "v_fma_f32 v227, v167, s48, v234\n\t"
    "v_exp_f32 v224, v224\n\t"
    "v_exp_f32 v225, v225\n\t"
    "v_exp_f32 v226, v226\n\t"
    "v_exp_f32 v227, v227\n\t"
    "v_cvt_pk_bf16_f32 v198, v220, v221\n\t"
    "v_cvt_pk_bf16_f32 v199, v222, v223\n\t"
    "v_add_f32 v229, v229, v220\n\t"
    "v_add_f32 v229, v229, v221\n\t"
    "v_add_f32 v229, v229, v222\n\t"
    "v_add_f32 v229, v229, v223\n\t"
    "v_fma_f32 v220, v168, s48, v232\n\t"
    "v_fma_f32 v221, v169, s48, v232\n\t"
    "v_fma_f32 v222, v170, s48, v232\n\t"
    "v_fma_f32 v223, v171, s48, v232\n\t"
    "v_exp_f32 v220, v220\n\t"
    "v_exp_f32 v221, v221\n\t"
    "v_exp_f32 v222, v222\n\t"
    "v_exp_f32 v223, v223\n\t"
    "v_cvt_pk_bf16_f32 v202, v224, v225\n\t"
    "v_cvt_pk_bf16_f32 v203, v226, v227\n\t"
    "v_add_f32 v230, v230, v224\n\t"
    "v_add_f32 v230, v230, v225\n\t"
    "v_add_f32 v230, v230, v226\n\t"
    "v_add_f32 v230, v230, v227\n\t"
    "v_fma_f32 v224, v172, s48, v233\n\t"
    "v_fma_f32 v225, v173, s48, v233\n\t"
    "v_fma_f32 v226, v174, s48, v233\n\t"
    "v_fma_f32 v227, v175, s48, v233\n\t"
    "v_exp_f32 v224, v224\n\t"
    "v_exp_f32 v225, v225\n\t"
    "v_exp_f32 v226, v226\n\t"
    "v_exp_f32 v227, v227\n\t"
    "v_cvt_pk_bf16_f32 v204, v220, v221\n\t"
    "v_cvt_pk_bf16_f32 v205, v222, v223\n\t"
    "v_add_f32 v228, v228, v220\n\t"
    "v_add_f32 v228, v228, v221\n\t"
    "v_add_f32 v228, v228, v222\n\t"
    "v_add_f32 v228, v228, v223\n\t"
    "v_fma_f32 v220, v176, s48, v234\n\t"
    "v_fma_f32 v221, v177, s48, v234\n\t"
    "v_fma_f32 v222, v178, s48, v234\n\t"
    "v_fma_f32 v223, v179, s48, v234\n\t"
    "v_exp_f32 v220, v220\n\t"
    "v_exp_f32 v221, v221\n\t"
    "v_exp_f32 v222, v222\n\t"
    "v_exp_f32 v223, v223\n\t"
    "v_cvt_pk_bf16_f32 v208, v224, v225\n\t"
    "v_cvt_pk_bf16_f32 v209, v226, v227\n\t"
    "v_add_f32 v229, v229, v224\n\t"
    "v_add_f32 v229, v229, v225\n\t"
    "v_add_f32 v229, v229, v226\n\t"
    "v_add_f32 v229, v229, v227\n\t"
    "v_fma_f32 v224, v180, s48, v232\n\t"
    "v_fma_f32 v225, v181, s48, v232\n\t"
    "v_fma_f32 v226, v182, s48, v232\n\t"
    "v_fma_f32 v227, v183, s48, v232\n\t"
    "v_exp_f32 v224, v224\n\t"
    "v_exp_f32 v225, v225\n\t"
    "v_exp_f32 v226, v226\n\t"
    "v_exp_f32 v227, v227\n\t"
    "v_cvt_pk_bf16_f32 v212, v220, v221\n\t"
    "v_cvt_pk_bf16_f32 v213, v222, v223\n\t"
    "v_add_f32 v230, v230, v220\n\t"
    "v_add_f32 v230, v230, v221\n\t"
    "v_add_f32 v230, v230, v222\n\t"
    "v_add_f32 v230, v230, v223\n\t"
    "v_fma_f32 v220, v184, s48, v233\n\t"
    "v_fma_f32 v221, v185, s48, v233\n\t"
    "v_fma_f32 v222, v186, s48, v233\n\t"
    "v_fma_f32 v223, v187, s48, v233\n\t"
    "v_exp_f32 v220, v220\n\t"
    "v_exp_f32 v221, v221\n\t"
    "v_exp_f32 v222, v222\n\t"
    "v_exp_f32 v223, v223\n\t"
    "v_cvt_pk_bf16_f32 v206, v224, v225\n\t"
    "v_cvt_pk_bf16_f32 v207, v226, v227\n\t"
    "v_add_f32 v228, v228, v224\n\t"
    "v_add_f32 v228, v228, v225\n\t"
    "v_add_f32 v228, v228, v226\n\t"
    "v_add_f32 v228, v228, v227\n\t"
    "v_fma_f32 v224, v188, s48, v234\n\t"
    "v_fma_f32 v225, v189, s48, v234\n\t"
    "v_fma_f32 v226, v190, s48, v234\n\t"
    "v_fma_f32 v227, v191, s48, v234\n\t"
    "v_exp_f32 v224, v224\n\t"
    "v_exp_f32 v225, v225\n\t"
    "v_exp_f32 v226, v226\n\t"
    "v_exp_f32 v227, v227\n\t"
    "v_cvt_pk_bf16_f32 v210, v220, v221\n\t"
    "v_cvt_pk_bf16_f32 v211, v222, v223\n\t"
    "v_add_f32 v229, v229, v220\n\t"
    "v_add_f32 v229, v229, v221\n\t"
    "v_add_f32 v229, v229, v222\n\t"
    "v_add_f32 v229, v229, v223\n\t"
    "v_cvt_pk_bf16_f32 v214, v224, v225\n\t"
    "v_cvt_pk_bf16_f32 v215, v226, v227\n\t"
    "v_add_f32 v230, v230, v224\n\t"
    "v_add_f32 v230, v230, v225\n\t"
    "v_add_f32 v230, v230, v226\n\t"
    "v_add_f32 v230, v230, v227\n\t"
    "v_max3_f32 v220, v228, v229, v230\n\t"
    "v_cmp_lt_f32 vcc, s49, v220\n\t"
    "s_cbranch_vccnz LFBB3%=\n\t"
    "LRETB3%=:\n\t"
    "v_add_f32 v235, v235, v228\n\t"
    "v_add_f32 v236, v236, v229\n\t"
    "v_add_f32 v237, v237, v230\n\t"
    "s_barrier\n\t"
    "s_setprio 1\n\t"
    "ds_read_b64_tr_b16 v[144:145], v240 offset:49152\n\t"
    "ds_read_b64_tr_b16 v[146:147], v240 offset:53248\n\t"
    "ds_read_b64_tr_b16 v[148:149], v240 offset:57344\n\t"
    "ds_read_b64_tr_b16 v[150:151], v240 offset:61440\n\t"
    "ds_read_b64_tr_b16 v[152:153], v241 offset:57344\n\t"
    "ds_read_b64_tr_b16 v[154:155], v241 offset:61440\n\t"
    "ds_read_b64_tr_b16 v[156:157], v241 offset:49152\n\t"
    "ds_read_b64_tr_b16 v[158:159], v241 offset:53248\n\t"
    "s_waitcnt lgkmcnt(6)\n\t"
    "v_mfma_f32_16x16x32_bf16 v[0:3], v[144:147], v[192:195], v[0:3]\n\t"
    "ds_read_b64_tr_b16 v[160:161], v240 offset:49664\n\t"
    "ds_read_b64_tr_b16 v[162:163], v240 offset:53760\n\t"
    "s_waitcnt lgkmcnt(6)\n\t"
    "v_mfma_f32_16x16x32_bf16 v[0:3], v[148:151], v[204:207], v[0:3]\n\t"
    "ds_read_b64_tr_b16 v[164:165], v240 offset:57856\n\t"
    "ds_read_b64_tr_b16 v[166:167], v240 offset:61952\n\t"
    "v_mfma_f32_16x16x32_bf16 v[4:7], v[148:151], v[208:211], v[4:7]\n\t"
    "ds_read_b64_tr_b16 v[168:169], v241 offset:57856\n\t"
    "ds_read_b64_tr_b16 v[170:171], v241 offset:61952\n\t"
    "v_mfma_f32_16x16x32_bf16 v[4:7], v[144:147], v[196:199], v[4:7]\n\t"
    "ds_read_b64_tr_b16 v[172:173], v241 offset:49664\n\t"
    "ds_read_b64_tr_b16 v[174:175], v241 offset:53760\n\t"
    "v_mfma_f32_16x16x32_bf16 v[8:11], v[144:147], v[200:203], v[8:11]\n\t"
    "ds_read_b64 v[246:247], v244 offset:10248\n\t"
    "v_mfma_f32_16x16x32_bf16 v[8:11], v[148:151], v[212:215], v[8:11]\n\t"
    "s_waitcnt lgkmcnt(11)\n\t"
    "v_mfma_f32_16x16x32_bf16 v[20:23], v[152:155], v[212:215], v[20:23]\n\t"
    "s_waitcnt lgkmcnt(9)\n\t"
    "v_mfma_f32_16x16x32_bf16 v[20:23], v[156:159], v[200:203], v[20:23]\n\t"
    "ds_read_b64 v[248:249], v244 offset:10240\n\t"
    "v_mfma_f32_16x16x32_bf16 v[16:19], v[156:159], v[196:199], v[16:19]\n\t"
    "ds_read_b64_tr_b16 v[176:177], v240 offset:50176\n\t"
    "ds_read_b64_tr_b16 v[178:179], v240 offset:54272\n\t"
    "v_mfma_f32_16x16x32_bf16 v[16:19], v[152:155], v[208:211], v[16:19]\n\t"
    "ds_read_b64_tr_b16 v[144:145], v240 offset:58368\n\t"
    "ds_read_b64_tr_b16 v[146:147], v240 offset:62464\n\t"
    "v_mfma_f32_16x16x32_bf16 v[12:15], v[152:155], v[204:207], v[12:15]\n\t"
    "v_mfma_f32_16x16x32_bf16 v[12:15], v[156:159], v[192:195], v[12:15]\n\t"
    "s_waitcnt lgkmcnt(12)\n\t"
    "v_mfma_f32_16x16x32_bf16 v[24:27], v[160:163], v[192:195], v[24:27]\n\t"
    "s_waitcnt lgkmcnt(10)\n\t"
    "v_mfma_f32_16x16x32_bf16 v[24:27], v[164:167], v[204:207], v[24:27]\n\t"
    "s_add_u32 s58, s54, 8192\n\t"
    "v_mfma_f32_16x16x32_bf16 v[28:31], v[164:167], v[208:211], v[28:31]\n\t"
    "ds_read_b64_tr_b16 v[148:149], v241 offset:58368\n\t"
    "ds_read_b64_tr_b16 v[150:151], v241 offset:62464\n\t"
    "v_mfma_f32_16x16x32_bf16 v[28:31], v[160:163], v[196:199], v[28:31]\n\t"
    "ds_read_b64_tr_b16 v[152:153], v241 offset:50176\n\t"
    "ds_read_b64_tr_b16 v[154:155], v241 offset:54272\n\t"
    "v_mfma_f32_16x16x32_bf16 v[32:35], v[160:163], v[200:203], v[32:35]\n\t"
    "v_mfma_f32_16x16x32_bf16 v[32:35], v[164:167], v[212:215], v[32:35]\n\t"
    "s_waitcnt lgkmcnt(12)\n\t"
    "v_mfma_f32_16x16x32_bf16 v[44:47], v[168:171], v[212:215], v[44:47]\n\t"
    "s_waitcnt lgkmcnt(10)\n\t"
    "v_mfma_f32_16x16x32_bf16 v[44:47], v[172:175], v[200:203], v[44:47]\n\t"
    "s_waitcnt lgkmcnt(8)\n\t"
    "buffer_store_dwordx4 v[246:249], v242, s[84:87], s58 offen offset:2048 nt\n\t"
    "v_mfma_f32_16x16x32_bf16 v[40:43], v[172:175], v[196:199], v[40:43]\n\t"
    "ds_read_b64_tr_b16 v[156:157], v240 offset:50688\n\t"
    "ds_read_b64_tr_b16 v[158:159], v240 offset:54784\n\t"
    "v_mfma_f32_16x16x32_bf16 v[40:43], v[168:171], v[208:211], v[40:43]\n\t"
    "ds_read_b64_tr_b16 v[160:161], v240 offset:58880\n\t"
    "ds_read_b64_tr_b16 v[162:163], v240 offset:62976\n\t"
    "v_mfma_f32_16x16x32_bf16 v[36:39], v[168:171], v[204:207], v[36:39]\n\t"
    "s_add_u32 m0, s55, 8192\n\t"
    "v_mfma_f32_16x16x32_bf16 v[36:39], v[172:175], v[192:195], v[36:39]\n\t"
    "s_waitcnt lgkmcnt(10)\n\t"
    "v_mfma_f32_16x16x32_bf16 v[48:51], v[176:179], v[192:195], v[48:51]\n\t"
    "s_waitcnt lgkmcnt(8)\n\t"
    "v_mfma_f32_16x16x32_bf16 v[48:51], v[144:147], v[204:207], v[48:51]\n\t"
    "s_add_u32 s57, s53, 8192\n\t"
    "v_mfma_f32_16x16x32_bf16 v[52:55], v[144:147], v[208:211], v[52:55]\n\t"
    "ds_read_b64_tr_b16 v[164:165], v241 offset:58880\n\t"
    "ds_read_b64_tr_b16 v[166:167], v241 offset:62976\n\t"
    "v_mfma_f32_16x16x32_bf16 v[52:55], v[176:179], v[196:199], v[52:55]\n\t"
    "ds_read_b64_tr_b16 v[168:169], v241 offset:50688\n\t"
    "ds_read_b64_tr_b16 v[170:171], v241 offset:54784\n\t"
    "v_mfma_f32_16x16x32_bf16 v[56:59], v[176:179], v[200:203], v[56:59]\n\t"
    "buffer_load_dwordx4 v242, s[44:47], s57 offen offset:2048 lds\n\t"
    "v_mfma_f32_16x16x32_bf16 v[56:59], v[144:147], v[212:215], v[56:59]\n\t"
    "s_waitcnt lgkmcnt(10)\n\t"
    "v_mfma_f32_16x16x32_bf16 v[68:71], v[148:151], v[212:215], v[68:71]\n\t"
    "s_waitcnt lgkmcnt(8)\n\t"
    "v_mfma_f32_16x16x32_bf16 v[68:71], v[152:155], v[200:203], v[68:71]\n\t"
    "ds_read_b64 v[246:247], v244 offset:11272\n\t"
    "v_mfma_f32_16x16x32_bf16 v[64:67], v[152:155], v[196:199], v[64:67]\n\t"
    "ds_read_b128 v[180:183], v238 offset:0\n\t"
    "v_mfma_f32_16x16x32_bf16 v[64:67], v[148:151], v[208:211], v[64:67]\n\t"
    "ds_read_b128 v[184:187], v239 offset:0\n\t"
    "v_mfma_f32_16x16x32_bf16 v[60:63], v[148:151], v[204:207], v[60:63]\n\t"
    "ds_read_b128 v[188:191], v238 offset:1024\n\t"
    "v_mfma_f32_16x16x32_bf16 v[60:63], v[152:155], v[192:195], v[60:63]\n\t"
    "ds_read_b128 v[216:219], v239 offset:1024\n\t"
    "s_waitcnt lgkmcnt(11)\n\t"
    "v_mfma_f32_16x16x32_bf16 v[72:75], v[156:159], v[192:195], v[72:75]\n\t"
    "s_waitcnt lgkmcnt(9)\n\t"
    "v_mfma_f32_16x16x32_bf16 v[72:75], v[160:163], v[204:207], v[72:75]\n\t"
    "ds_read_b64 v[248:249], v244 offset:11264\n\t"
    "v_mfma_f32_16x16x32_bf16 v[76:79], v[160:163], v[208:211], v[76:79]\n\t"
    "v_mfma_f32_16x16x32_bf16 v[76:79], v[156:159], v[196:199], v[76:79]\n\t"
    "v_mfma_f32_16x16x32_bf16 v[80:83], v[156:159], v[200:203], v[80:83]\n\t"
    "v_mfma_f32_16x16x32_bf16 v[80:83], v[160:163], v[212:215], v[80:83]\n\t"
    "s_waitcnt lgkmcnt(8)\n\t"
    "v_mfma_f32_16x16x32_bf16 v[92:95], v[164:167], v[212:215], v[92:95]\n\t"
    "s_waitcnt lgkmcnt(6)\n\t"
    "v_mfma_f32_16x16x32_bf16 v[92:95], v[168:171], v[200:203], v[92:95]\n\t"
    "s_add_u32 s58, s54, 8192\n\t"
    "v_mfma_f32_16x16x32_bf16 v[88:91], v[168:171], v[196:199], v[88:91]\n\t"
    "v_mfma_f32_16x16x32_bf16 v[88:91], v[164:167], v[208:211], v[88:91]\n\t"
    "s_waitcnt lgkmcnt(0)\n\t"
    "buffer_store_dwordx4 v[246:249], v243, s[84:87], s58 offen offset:3072 nt\n\t"
    "v_mfma_f32_16x16x32_bf16 v[84:87], v[164:167], v[204:207], v[84:87]\n\t"
    "s_add_u32 m0, s55, 8192\n\t"
    "v_mfma_f32_16x16x32_bf16 v[84:87], v[168:171], v[192:195], v[84:87]\n\t"
    "ds_read_b128 v[204:207], v239 offset:5120\n\t"
    "ds_read_b128 v[200:203], v238 offset:5120\n\t"
    "v_mfma_f32_16x16x32_bf16 v[144:147], v[180:183], v[96:99], 0\n\t"
    "ds_read_b128 v[196:199], v239 offset:4096\n\t"
    "ds_read_b128 v[192:195], v238 offset:4096\n\t"
    "v_mfma_f32_16x16x32_bf16 v[144:147], v[184:187], v[100:103], v[144:147]\n\t"
    "v_mfma_f32_16x16x32_bf16 v[144:147], v[188:191], v[104:107], v[144:147]\n\t"
    "v_mfma_f32_16x16x32_bf16 v[144:147], v[216:219], v[108:111], v[144:147]\n\t"
    "s_add_u32 s57, s53, 8192\n\t"
    "v_mfma_f32_16x16x32_bf16 v[148:151], v[216:219], v[124:127], 0\n\t"
    "buffer_load_dwordx4 v243, s[44:47], s57 offen offset:3072 lds\n\t"
    "v_mfma_f32_16x16x32_bf16 v[148:151], v[188:191], v[120:123], v[148:151]\n\t"
    "v_mfma_f32_16x16x32_bf16 v[148:151], v[184:187], v[116:119], v[148:151]\n\t"
    "v_mfma_f32_16x16x32_bf16 v[148:151], v[180:183], v[112:115], v[148:151]\n\t"
    "ds_read_b128 v[246:249], v244 offset:49152\n\t"
    "v_mfma_f32_16x16x32_bf16 v[152:155], v[180:183], v[128:131], 0\n\t"
    "ds_read_b128 v[180:183], v238 offset:8192\n\t"
    "v_mfma_f32_16x16x32_bf16 v[152:155], v[184:187], v[132:135], v[152:155]\n\t"
    "ds_read_b128 v[184:187], v239 offset:8192\n\t"
    "v_mfma_f32_16x16x32_bf16 v[152:155], v[188:191], v[136:139], v[152:155]\n\t"
    "ds_read_b128 v[188:191], v238 offset:9216\n\t"
    "v_mfma_f32_16x16x32_bf16 v[152:155], v[216:219], v[140:143], v[152:155]\n\t"
    "ds_read_b128 v[216:219], v239 offset:9216\n\t"
    "s_waitcnt lgkmcnt(8)\n\t"
    "v_mfma_f32_16x16x32_bf16 v[164:167], v[204:207], v[140:143], 0\n\t"
    "s_waitcnt lgkmcnt(7)\n\t"
    "v_mfma_f32_16x16x32_bf16 v[164:167], v[200:203], v[136:139], v[164:167]\n\t"
    "s_waitcnt lgkmcnt(6)\n\t"
    "v_mfma_f32_16x16x32_bf16 v[164:167], v[196:199], v[132:135], v[164:167]\n\t"
    "s_waitcnt lgkmcnt(5)\n\t"
    "v_mfma_f32_16x16x32_bf16 v[164:167], v[192:195], v[128:131], v[164:167]\n\t"
    "s_add_u32 s58, s54, 49152\n\t"
    "v_mfma_f32_16x16x32_bf16 v[160:163], v[192:195], v[112:115], 0\n\t"
    "v_mfma_f32_16x16x32_bf16 v[160:163], v[196:199], v[116:119], v[160:163]\n\t"
    "s_waitcnt lgkmcnt(4)\n\t"
    "buffer_store_dwordx4 v[246:249], v242, s[84:87], s58 offen offset:0 nt\n\t"
    "v_mfma_f32_16x16x32_bf16 v[160:163], v[200:203], v[120:123], v[160:163]\n\t"
    "s_add_u32 m0, s55, 49152\n\t"
    "v_mfma_f32_16x16x32_bf16 v[160:163], v[204:207], v[124:127], v[160:163]\n\t"
    "s_add_u32 s57, s53, 49152\n\t"
    "v_mfma_f32_16x16x32_bf16 v[156:159], v[204:207], v[108:111], 0\n\t"
    "ds_read_b128 v[204:207], v239 offset:13312\n\t"
    "v_mfma_f32_16x16x32_bf16 v[156:159], v[200:203], v[104:107], v[156:159]\n\t"
    "ds_read_b128 v[200:203], v238 offset:13312\n\t"
    "v_mfma_f32_16x16x32_bf16 v[156:159], v[196:199], v[100:103], v[156:159]\n\t"
    "ds_read_b128 v[196:199], v239 offset:12288\n\t"
    "v_mfma_f32_16x16x32_bf16 v[156:159], v[192:195], v[96:99], v[156:159]\n\t"
    "ds_read_b128 v[192:195], v238 offset:12288\n\t"
    "s_waitcnt lgkmcnt(7)\n\t"
    "v_mfma_f32_16x16x32_bf16 v[168:171], v[180:183], v[96:99], 0\n\t"
    "s_waitcnt lgkmcnt(6)\n\t"
    "v_mfma_f32_16x16x32_bf16 v[168:171], v[184:187], v[100:103], v[168:171]\n\t"
    "s_waitcnt lgkmcnt(5)\n\t"
    "v_mfma_f32_16x16x32_bf16 v[168:171], v[188:191], v[104:107], v[168:171]\n\t"
    "s_waitcnt lgkmcnt(4)\n\t"
    "v_mfma_f32_16x16x32_bf16 v[168:171], v[216:219], v[108:111], v[168:171]\n\t"
    "buffer_load_dwordx4 v242, s[44:47], s57 offen offset:0 lds\n\t"
    "v_mfma_f32_16x16x32_bf16 v[172:175], v[216:219], v[124:127], 0\n\t"
    "v_mfma_f32_16x16x32_bf16 v[172:175], v[188:191], v[120:123], v[172:175]\n\t"
    "v_mfma_f32_16x16x32_bf16 v[172:175], v[184:187], v[116:119], v[172:175]\n\t"
    "v_mfma_f32_16x16x32_bf16 v[172:175], v[180:183], v[112:115], v[172:175]\n\t"
    "v_mfma_f32_16x16x32_bf16 v[176:179], v[180:183], v[128:131], 0\n\t"
    "v_mfma_f32_16x16x32_bf16 v[176:179], v[184:187], v[132:135], v[176:179]\n\t"
    "v_mfma_f32_16x16x32_bf16 v[176:179], v[188:191], v[136:139], v[176:179]\n\t"
    "v_mfma_f32_16x16x32_bf16 v[176:179], v[216:219], v[140:143], v[176:179]\n\t"
    "s_waitcnt lgkmcnt(3)\n\t"
    "v_mfma_f32_16x16x32_bf16 v[188:191], v[204:207], v[140:143], 0\n\t"
    "s_waitcnt lgkmcnt(2)\n\t"
    "v_mfma_f32_16x16x32_bf16 v[188:191], v[200:203], v[136:139], v[188:191]\n\t"
    "s_waitcnt lgkmcnt(1)\n\t"
    "v_mfma_f32_16x16x32_bf16 v[188:191], v[196:199], v[132:135], v[188:191]\n\t"
    "s_waitcnt lgkmcnt(0)\n\t"
    "v_mfma_f32_16x16x32_bf16 v[188:191], v[192:195], v[128:131], v[188:191]\n\t"
    "v_mfma_f32_16x16x32_bf16 v[184:187], v[192:195], v[112:115], 0\n\t"
    "v_mfma_f32_16x16x32_bf16 v[184:187], v[196:199], v[116:119], v[184:187]\n\t"
    "v_mfma_f32_16x16x32_bf16 v[184:187], v[200:203], v[120:123], v[184:187]\n\t"
    "v_mfma_f32_16x16x32_bf16 v[184:187], v[204:207], v[124:127], v[184:187]\n\t"
    "v_mfma_f32_16x16x32_bf16 v[180:183], v[204:207], v[108:111], 0\n\t"
    "v_mfma_f32_16x16x32_bf16 v[180:183], v[200:203], v[104:107], v[180:183]\n\t"
    "v_mfma_f32_16x16x32_bf16 v[180:183], v[196:199], v[100:103], v[180:183]\n\t"
    "v_mfma_f32_16x16x32_bf16 v[180:183], v[192:195], v[96:99], v[180:183]\n\t"
    "s_waitcnt lgkmcnt(0)\n\t"
    "s_barrier\n\t"
    "s_setprio 0\n\t"
    "v_fma_f32 v220, v144, s48, v232\n\t"
    "v_fma_f32 v221, v145, s48, v232\n\t"
    "v_fma_f32 v222, v146, s48, v232\n\t"
    "v_fma_f32 v223, v147, s48, v232\n\t"
    "v_exp_f32 v220, v220\n\t"
    "v_exp_f32 v221, v221\n\t"
    "v_exp_f32 v222, v222\n\t"
    "v_exp_f32 v223, v223\n\t"
    "v_fma_f32 v224, v148, s48, v233\n\t"
    "v_fma_f32 v225, v149, s48, v233\n\t"
    "v_fma_f32 v226, v150, s48, v233\n\t"
    "v_fma_f32 v227, v151, s48, v233\n\t"
    "v_exp_f32 v224, v224\n\t"
    "v_exp_f32 v225, v225\n\t"
    "v_exp_f32 v226, v226\n\t"
    "v_exp_f32 v227, v227\n\t"
    "v_cvt_pk_bf16_f32 v192, v220, v221\n\t"
    "v_cvt_pk_bf16_f32 v193, v222, v223\n\t"
    "v_add_f32 v228, v220, v221\n\t"
    "v_add_f32 v228, v228, v222\n\t"
    "v_add_f32 v228, v228, v223\n\t"
    "v_fma_f32 v220, v152, s48, v234\n\t"
    "v_fma_f32 v221, v153, s48, v234\n\t"
    "v_fma_f32 v222, v154, s48, v234\n\t"
    "v_fma_f32 v223, v155, s48, v234\n\t"
    "v_exp_f32 v220, v220\n\t"
    "v_exp_f32 v221, v221\n\t"
    "v_exp_f32 v222, v222\n\t"
    "v_exp_f32 v223, v223\n\t"
    "v_cvt_pk_bf16_f32 v196, v224, v225\n\t"
    "v_cvt_pk_bf16_f32 v197, v226, v227\n\t"
    "v_add_f32 v229, v224, v225\n\t"
    "v_add_f32 v229, v229, v226\n\t"
    "v_add_f32 v229, v229, v227\n\t"
    "v_fma_f32 v224, v156, s48, v232\n\t"
    "v_fma_f32 v225, v157, s48, v232\n\t"
    "v_fma_f32 v226, v158, s48, v232\n\t"
    "v_fma_f32 v227, v159, s48, v232\n\t"
    "v_exp_f32 v224, v224\n\t"
    "v_exp_f32 v225, v225\n\t"
    "v_exp_f32 v226, v226\n\t"
    "v_exp_f32 v227, v227\n\t"
    "v_cvt_pk_bf16_f32 v200, v220, v221\n\t"
    "v_cvt_pk_bf16_f32 v201, v222, v223\n\t"
    "v_add_f32 v230, v220, v221\n\t"
    "v_add_f32 v230, v230, v222\n\t"
    "v_add_f32 v230, v230, v223\n\t"
    "v_fma_f32 v220, v160, s48, v233\n\t"
    "v_fma_f32 v221, v161, s48, v233\n\t"
    "v_fma_f32 v222, v162, s48, v233\n\t"
    "v_fma_f32 v223, v163, s48, v233\n\t"
    "v_exp_f32 v220, v220\n\t"
    "v_exp_f32 v221, v221\n\t"
    "v_exp_f32 v222, v222\n\t"
    "v_exp_f32 v223, v223\n\t"
    "v_cvt_pk_bf16_f32 v194, v224, v225\n\t"
    "v_cvt_pk_bf16_f32 v195, v226, v227\n\t"
    "v_add_f32 v228, v228, v224\n\t"
    "v_add_f32 v228, v228, v225\n\t"
    "v_add_f32 v228, v228, v226\n\t"
    "v_add_f32 v228, v228, v227\n\t"
    "v_fma_f32 v224, v164, s48, v234\n\t"
    "v_fma_f32 v225, v165, s48, v234\n\t"
    "v_fma_f32 v226, v166, s48, v234\n\t"
    "v_fma_f32 v227, v167, s48, v234\n\t"
    "v_exp_f32 v224, v224\n\t"
    "v_exp_f32 v225, v225\n\t"
    "v_exp_f32 v226, v226\n\t"
    "v_exp_f32 v227, v227\n\t"
    "v_cvt_pk_bf16_f32 v198, v220, v221\n\t"
    "v_cvt_pk_bf16_f32 v199, v222, v223\n\t"
    "v_add_f32 v229, v229, v220\n\t"
    "v_add_f32 v229, v229, v221\n\t"
    "v_add_f32 v229, v229, v222\n\t"
    "v_add_f32 v229, v229, v223\n\t"
    "v_fma_f32 v220, v168, s48, v232\n\t"
    "v_fma_f32 v221, v169, s48, v232\n\t"
    "v_fma_f32 v222, v170, s48, v232\n\t"
    "v_fma_f32 v223, v171, s48, v232\n\t"
    "v_exp_f32 v220, v220\n\t"
    "v_exp_f32 v221, v221\n\t"
    "v_exp_f32 v222, v222\n\t"
    "v_exp_f32 v223, v223\n\t"
    "v_cvt_pk_bf16_f32 v202, v224, v225\n\t"
    "v_cvt_pk_bf16_f32 v203, v226, v227\n\t"
    "v_add_f32 v230, v230, v224\n\t"
    "v_add_f32 v230, v230, v225\n\t"
    "v_add_f32 v230, v230, v226\n\t"
    "v_add_f32 v230, v230, v227\n\t"
    "v_fma_f32 v224, v172, s48, v233\n\t"
    "v_fma_f32 v225, v173, s48, v233\n\t"
    "v_fma_f32 v226, v174, s48, v233\n\t"
    "v_fma_f32 v227, v175, s48, v233\n\t"
    "v_exp_f32 v224, v224\n\t"
    "v_exp_f32 v225, v225\n\t"
    "v_exp_f32 v226, v226\n\t"
    "v_exp_f32 v227, v227\n\t"
    "v_cvt_pk_bf16_f32 v204, v220, v221\n\t"
    "v_cvt_pk_bf16_f32 v205, v222, v223\n\t"
    "v_add_f32 v228, v228, v220\n\t"
    "v_add_f32 v228, v228, v221\n\t"
    "v_add_f32 v228, v228, v222\n\t"
    "v_add_f32 v228, v228, v223\n\t"
    "v_fma_f32 v220, v176, s48, v234\n\t"
    "v_fma_f32 v221, v177, s48, v234\n\t"
    "v_fma_f32 v222, v178, s48, v234\n\t"
    "v_fma_f32 v223, v179, s48, v234\n\t"
    "v_exp_f32 v220, v220\n\t"
    "v_exp_f32 v221, v221\n\t"
    "v_exp_f32 v222, v222\n\t"
    "v_exp_f32 v223, v223\n\t"
    "v_cvt_pk_bf16_f32 v208, v224, v225\n\t"
    "v_cvt_pk_bf16_f32 v209, v226, v227\n\t"
    "v_add_f32 v229, v229, v224\n\t"
    "v_add_f32 v229, v229, v225\n\t"
    "v_add_f32 v229, v229, v226\n\t"
    "v_add_f32 v229, v229, v227\n\t"
    "v_fma_f32 v224, v180, s48, v232\n\t"
    "v_fma_f32 v225, v181, s48, v232\n\t"
    "v_fma_f32 v226, v182, s48, v232\n\t"
    "v_fma_f32 v227, v183, s48, v232\n\t"
    "v_exp_f32 v224, v224\n\t"
    "v_exp_f32 v225, v225\n\t"
    "v_exp_f32 v226, v226\n\t"
    "v_exp_f32 v227, v227\n\t"
    "v_cvt_pk_bf16_f32 v212, v220, v221\n\t"
    "v_cvt_pk_bf16_f32 v213, v222, v223\n\t"
    "v_add_f32 v230, v230, v220\n\t"
    "v_add_f32 v230, v230, v221\n\t"
    "v_add_f32 v230, v230, v222\n\t"
    "v_add_f32 v230, v230, v223\n\t"
    "v_fma_f32 v220, v184, s48, v233\n\t"
    "v_fma_f32 v221, v185, s48, v233\n\t"
    "v_fma_f32 v222, v186, s48, v233\n\t"
    "v_fma_f32 v223, v187, s48, v233\n\t"
    "v_exp_f32 v220, v220\n\t"
    "v_exp_f32 v221, v221\n\t"
    "v_exp_f32 v222, v222\n\t"
    "v_exp_f32 v223, v223\n\t"
    "v_cvt_pk_bf16_f32 v206, v224, v225\n\t"
    "v_cvt_pk_bf16_f32 v207, v226, v227\n\t"
    "v_add_f32 v228, v228, v224\n\t"
    "v_add_f32 v228, v228, v225\n\t"
    "v_add_f32 v228, v228, v226\n\t"
    "v_add_f32 v228, v228, v227\n\t"
    "v_fma_f32 v224, v188, s48, v234\n\t"
    "v_fma_f32 v225, v189, s48, v234\n\t"
    "v_fma_f32 v226, v190, s48, v234\n\t"
    "v_fma_f32 v227, v191, s48, v234\n\t"
    "v_exp_f32 v224, v224\n\t"
    "v_exp_f32 v225, v225\n\t"
    "v_exp_f32 v226, v226\n\t"
    "v_exp_f32 v227, v227\n\t"
    "v_cvt_pk_bf16_f32 v210, v220, v221\n\t"
    "v_cvt_pk_bf16_f32 v211, v222, v223\n\t"
    "v_add_f32 v229, v229, v220\n\t"
    "v_add_f32 v229, v229, v221\n\t"
    "v_add_f32 v229, v229, v222\n\t"
    "v_add_f32 v229, v229, v223\n\t"
    "v_cvt_pk_bf16_f32 v214, v224, v225\n\t"
    "v_cvt_pk_bf16_f32 v215, v226, v227\n\t"
    "v_add_f32 v230, v230, v224\n\t"
    "v_add_f32 v230, v230, v225\n\t"
    "v_add_f32 v230, v230, v226\n\t"
    "v_add_f32 v230, v230, v227\n\t"
    "v_max3_f32 v220, v228, v229, v230\n\t"
    "v_cmp_lt_f32 vcc, s49, v220\n\t"
    "s_cbranch_vccnz LFBB4%=\n\t"
    "LRETB4%=:\n\t"
    "v_add_f32 v235, v235, v228\n\t"
    "v_add_f32 v236, v236, v229\n\t"
    "v_add_f32 v237, v237, v230\n\t"
    "s_barrier\n\t"
    "s_setprio 1\n\t"
    "ds_read_b64_tr_b16 v[144:145], v240 offset:32768\n\t"
    "ds_read_b64_tr_b16 v[146:147], v240 offset:36864\n\t"
    "ds_read_b64_tr_b16 v[148:149], v240 offset:40960\n\t"
    "ds_read_b64_tr_b16 v[150:151], v240 offset:45056\n\t"
    "ds_read_b64_tr_b16 v[152:153], v241 offset:40960\n\t"
    "ds_read_b64_tr_b16 v[154:155], v241 offset:45056\n\t"
    "ds_read_b64_tr_b16 v[156:157], v241 offset:32768\n\t"
    "ds_read_b64_tr_b16 v[158:159], v241 offset:36864\n\t"
    "s_waitcnt lgkmcnt(6)\n\t"
    "v_mfma_f32_16x16x32_bf16 v[0:3], v[144:147], v[192:195], v[0:3]\n\t"
    "ds_read_b64_tr_b16 v[160:161], v240 offset:33280\n\t"
    "ds_read_b64_tr_b16 v[162:163], v240 offset:37376\n\t"
    "s_waitcnt lgkmcnt(6)\n\t"
    "v_mfma_f32_16x16x32_bf16 v[0:3], v[148:151], v[204:207], v[0:3]\n\t"
    "ds_read_b64_tr_b16 v[164:165], v240 offset:41472\n\t"
    "ds_read_b64_tr_b16 v[166:167], v240 offset:45568\n\t"
    "v_mfma_f32_16x16x32_bf16 v[4:7], v[148:151], v[208:211], v[4:7]\n\t"
    "ds_read_b64_tr_b16 v[168:169], v241 offset:41472\n\t"
    "ds_read_b64_tr_b16 v[170:171], v241 offset:45568\n\t"
    "v_mfma_f32_16x16x32_bf16 v[4:7], v[144:147], v[196:199], v[4:7]\n\t"
    "ds_read_b64_tr_b16 v[172:173], v241 offset:33280\n\t"
    "ds_read_b64_tr_b16 v[174:175], v241 offset:37376\n\t"
    "v_mfma_f32_16x16x32_bf16 v[8:11], v[144:147], v[200:203], v[8:11]\n\t"
    "ds_read_b128 v[246:249], v244 offset:50176\n\t"
    "v_mfma_f32_16x16x32_bf16 v[8:11], v[148:151], v[212:215], v[8:11]\n\t"
    "s_waitcnt lgkmcnt(11)\n\t"
    "v_mfma_f32_16x16x32_bf16 v[20:23], v[152:155], v[212:215], v[20:23]\n\t"
    "s_waitcnt lgkmcnt(9)\n\t"
    "v_mfma_f32_16x16x32_bf16 v[20:23], v[156:159], v[200:203], v[20:23]\n\t"
    "v_mfma_f32_16x16x32_bf16 v[16:19], v[156:159], v[196:199], v[16:19]\n\t"
    "ds_read_b64_tr_b16 v[176:177], v240 offset:33792\n\t"
    "ds_read_b64_tr_b16 v[178:179], v240 offset:37888\n\t"
    "v_mfma_f32_16x16x32_bf16 v[16:19], v[152:155], v[208:211], v[16:19]\n\t"
    "ds_read_b64_tr_b16 v[144:145], v240 offset:41984\n\t"
    "ds_read_b64_tr_b16 v[146:147], v240 offset:46080\n\t"
    "v_mfma_f32_16x16x32_bf16 v[12:15], v[152:155], v[204:207], v[12:15]\n\t"
    "s_add_u32 s58, s54, 49152\n\t"
    "v_mfma_f32_16x16x32_bf16 v[12:15], v[156:159], v[192:195], v[12:15]\n\t"
    "s_waitcnt lgkmcnt(11)\n\t"
    "v_mfma_f32_16x16x32_bf16 v[24:27], v[160:163], v[192:195], v[24:27]\n\t"
    "s_waitcnt lgkmcnt(9)\n\t"
    "v_mfma_f32_16x16x32_bf16 v[24:27], v[164:167], v[204:207], v[24:27]\n\t"
    "s_waitcnt lgkmcnt(4)\n\t"
    "buffer_store_dwordx4 v[246:249], v243, s[84:87], s58 offen offset:1024 nt\n\t"
    "v_mfma_f32_16x16x32_bf16 v[28:31], v[164:167], v[208:211], v[28:31]\n\t"
    "ds_read_b64_tr_b16 v[148:149], v241 offset:41984\n\t"
    "ds_read_b64_tr_b16 v[150:151], v241 offset:46080\n\t"
    "v_mfma_f32_16x16x32_bf16 v[28:31], v[160:163], v[196:199], v[28:31]\n\t"
    "ds_read_b64_tr_b16 v[152:153], v241 offset:33792\n\t"
    "ds_read_b64_tr_b16 v[154:155], v241 offset:37888\n\t"
    "v_mfma_f32_16x16x32_bf16 v[32:35], v[160:163], v[200:203], v[32:35]\n\t"
    "s_add_u32 m0, s55, 49152\n\t"
    "v_mfma_f32_16x16x32_bf16 v[32:35], v[164:167], v[212:215], v[32:35]\n\t"
    "v_mfma_f32_16x16x32_bf16 v[44:47], v[168:171], v[212:215], v[44:47]\n\t"
    "v_mfma_f32_16x16x32_bf16 v[44:47], v[172:175], v[200:203], v[44:47]\n\t"
    "s_add_u32 s57, s53, 49152\n\t"
    "v_mfma_f32_16x16x32_bf16 v[40:43], v[172:175], v[196:199], v[40:43]\n\t"
    "ds_read_b64_tr_b16 v[156:157], v240 offset:34304\n\t"
    "ds_read_b64_tr_b16 v[158:159], v240 offset:38400\n\t"
    "v_mfma_f32_16x16x32_bf16 v[40:43], v[168:171], v[208:211], v[40:43]\n\t"
    "ds_read_b64_tr_b16 v[160:161], v240 offset:42496\n\t"
    "ds_read_b64_tr_b16 v[162:163], v240 offset:46592\n\t"
    "v_mfma_f32_16x16x32_bf16 v[36:39], v[168:171], v[204:207], v[36:39]\n\t"
    "buffer_load_dwordx4 v243, s[44:47], s57 offen offset:1024 lds\n\t"
    "v_mfma_f32_16x16x32_bf16 v[36:39], v[172:175], v[192:195], v[36:39]\n\t"
    "s_waitcnt lgkmcnt(10)\n\t"
    "v_mfma_f32_16x16x32_bf16 v[48:51], v[176:179], v[192:195], v[48:51]\n\t"
    "s_waitcnt lgkmcnt(8)\n\t"
    "v_mfma_f32_16x16x32_bf16 v[48:51], v[144:147], v[204:207], v[48:51]\n\t"
    "ds_read_b64 v[246:247], v244 offset:51208\n\t"
    "v_mfma_f32_16x16x32_bf16 v[52:55], v[144:147], v[208:211], v[52:55]\n\t"
    "ds_read_b64_tr_b16 v[164:165], v241 offset:42496\n\t"
    "ds_read_b64_tr_b16 v[166:167], v241 offset:46592\n\t"
    "v_mfma_f32_16x16x32_bf16 v[52:55], v[176:179], v[196:199], v[52:55]\n\t"
    "ds_read_b64_tr_b16 v[168:169], v241 offset:34304\n\t"
    "ds_read_b64_tr_b16 v[170:171], v241 offset:38400\n\t"
    "v_mfma_f32_16x16x32_bf16 v[56:59], v[176:179], v[200:203], v[56:59]\n\t"
    "ds_read_b64 v[248:249], v244 offset:51200\n\t"
    "v_mfma_f32_16x16x32_bf16 v[56:59], v[144:147], v[212:215], v[56:59]\n\t"
    "s_waitcnt lgkmcnt(12)\n\t"
    "v_mfma_f32_16x16x32_bf16 v[68:71], v[148:151], v[212:215], v[68:71]\n\t"
    "s_waitcnt lgkmcnt(10)\n\t"
    "v_mfma_f32_16x16x32_bf16 v[68:71], v[152:155], v[200:203], v[68:71]\n\t"
    "v_mfma_f32_16x16x32_bf16 v[64:67], v[152:155], v[196:199], v[64:67]\n\t"
    "ds_read_b128 v[180:183], v238 offset:16384\n\t"
    "v_mfma_f32_16x16x32_bf16 v[64:67], v[148:151], v[208:211], v[64:67]\n\t"
    "ds_read_b128 v[184:187], v239 offset:16384\n\t"
    "v_mfma_f32_16x16x32_bf16 v[60:63], v[148:151], v[204:207], v[60:63]\n\t"
    "ds_read_b128 v[188:191], v238 offset:17408\n\t"
    "v_mfma_f32_16x16x32_bf16 v[60:63], v[152:155], v[192:195], v[60:63]\n\t"
    "ds_read_b128 v[216:219], v239 offset:17408\n\t"
    "s_waitcnt lgkmcnt(12)\n\t"
    "v_mfma_f32_16x16x32_bf16 v[72:75], v[156:159], v[192:195], v[72:75]\n\t"
    "s_waitcnt lgkmcnt(10)\n\t"
    "v_mfma_f32_16x16x32_bf16 v[72:75], v[160:163], v[204:207], v[72:75]\n\t"
    "s_add_u32 s58, s54, 49152\n\t"
    "v_mfma_f32_16x16x32_bf16 v[76:79], v[160:163], v[208:211], v[76:79]\n\t"
    "v_mfma_f32_16x16x32_bf16 v[76:79], v[156:159], v[196:199], v[76:79]\n\t"
    "s_waitcnt lgkmcnt(4)\n\t"
    "buffer_store_dwordx4 v[246:249], v242, s[84:87], s58 offen offset:2048 nt\n\t"
    "v_mfma_f32_16x16x32_bf16 v[80:83], v[156:159], v[200:203], v[80:83]\n\t"
    "s_add_u32 m0, s55, 49152\n\t"
    "v_mfma_f32_16x16x32_bf16 v[80:83], v[160:163], v[212:215], v[80:83]\n\t"
    "v_mfma_f32_16x16x32_bf16 v[92:95], v[164:167], v[212:215], v[92:95]\n\t"
    "v_mfma_f32_16x16x32_bf16 v[92:95], v[168:171], v[200:203], v[92:95]\n\t"
    "s_add_u32 s57, s53, 49152\n\t"
    "v_mfma_f32_16x16x32_bf16 v[88:91], v[168:171], v[196:199], v[88:91]\n\t"
    "buffer_load_dwordx4 v242, s[44:47], s57 offen offset:2048 lds\n\t"
    "v_mfma_f32_16x16x32_bf16 v[88:91], v[164:167], v[208:211], v[88:91]\n\t"
    "v_mfma_f32_16x16x32_bf16 v[84:87], v[164:167], v[204:207], v[84:87]\n\t"
    "v_mfma_f32_16x16x32_bf16 v[84:87], v[168:171], v[192:195], v[84:87]\n\t"
    "ds_read_b128 v[204:207], v239 offset:21504\n\t"
    "ds_read_b128 v[200:203], v238 offset:21504\n\t"
    "s_waitcnt lgkmcnt(5)\n\t"
    "v_mfma_f32_16x16x32_bf16 v[144:147], v[180:183], v[96:99], 0\n\t"
    "ds_read_b128 v[196:199], v239 offset:20480\n\t"
    "ds_read_b128 v[192:195], v238 offset:20480\n\t"
    "s_waitcnt lgkmcnt(6)\n\t"
    "v_mfma_f32_16x16x32_bf16 v[144:147], v[184:187], v[100:103], v[144:147]\n\t"
    "s_waitcnt lgkmcnt(5)\n\t"
    "v_mfma_f32_16x16x32_bf16 v[144:147], v[188:191], v[104:107], v[144:147]\n\t"
    "s_waitcnt lgkmcnt(4)\n\t"
    "v_mfma_f32_16x16x32_bf16 v[144:147], v[216:219], v[108:111], v[144:147]\n\t"
    "ds_read_b64 v[246:247], v244 offset:52232\n\t"
    "v_mfma_f32_16x16x32_bf16 v[148:151], v[216:219], v[124:127], 0\n\t"
    "ds_read_b64 v[248:249], v244 offset:52224\n\t"
    "v_mfma_f32_16x16x32_bf16 v[148:151], v[188:191], v[120:123], v[148:151]\n\t"
    "v_mfma_f32_16x16x32_bf16 v[148:151], v[184:187], v[116:119], v[148:151]\n\t"
    "v_mfma_f32_16x16x32_bf16 v[148:151], v[180:183], v[112:115], v[148:151]\n\t"
    "v_mfma_f32_16x16x32_bf16 v[152:155], v[180:183], v[128:131], 0\n\t"
    "ds_read_b128 v[180:183], v238 offset:24576\n\t"
    "v_mfma_f32_16x16x32_bf16 v[152:155], v[184:187], v[132:135], v[152:155]\n\t"
    "ds_read_b128 v[184:187], v239 offset:24576\n\t"
    "v_mfma_f32_16x16x32_bf16 v[152:155], v[188:191], v[136:139], v[152:155]\n\t"
    "ds_read_b128 v[188:191], v238 offset:25600\n\t"
    "v_mfma_f32_16x16x32_bf16 v[152:155], v[216:219], v[140:143], v[152:155]\n\t"
    "ds_read_b128 v[216:219], v239 offset:25600\n\t"
    "s_waitcnt lgkmcnt(9)\n\t"
    "v_mfma_f32_16x16x32_bf16 v[164:167], v[204:207], v[140:143], 0\n\t"
    "s_waitcnt lgkmcnt(8)\n\t"
    "v_mfma_f32_16x16x32_bf16 v[164:167], v[200:203], v[136:139], v[164:167]\n\t"
    "s_waitcnt lgkmcnt(7)\n\t"
    "v_mfma_f32_16x16x32_bf16 v[164:167], v[196:199], v[132:135], v[164:167]\n\t"
    "s_waitcnt lgkmcnt(6)\n\t"
    "v_mfma_f32_16x16x32_bf16 v[164:167], v[192:195], v[128:131], v[164:167]\n\t"
    "s_add_u32 s58, s54, 49152\n\t"
    "v_mfma_f32_16x16x32_bf16 v[160:163], v[192:195], v[112:115], 0\n\t"
    "v_mfma_f32_16x16x32_bf16 v[160:163], v[196:199], v[116:119], v[160:163]\n\t"
    "s_waitcnt lgkmcnt(4)\n\t"
    "buffer_store_dwordx4 v[246:249], v243, s[84:87], s58 offen offset:3072 nt\n\t"
    "v_mfma_f32_16x16x32_bf16 v[160:163], v[200:203], v[120:123], v[160:163]\n\t"
    "s_add_u32 m0, s55, 49152\n\t"
    "v_mfma_f32_16x16x32_bf16 v[160:163], v[204:207], v[124:127], v[160:163]\n\t"
    "s_add_u32 s57, s53, 49152\n\t"
    "v_mfma_f32_16x16x32_bf16 v[156:159], v[204:207], v[108:111], 0\n\t"
    "ds_read_b128 v[204:207], v239 offset:29696\n\t"
    "v_mfma_f32_16x16x32_bf16 v[156:159], v[200:203], v[104:107], v[156:159]\n\t"
    "ds_read_b128 v[200:203], v238 offset:29696\n\t"
    "v_mfma_f32_16x16x32_bf16 v[156:159], v[196:199], v[100:103], v[156:159]\n\t"
    "ds_read_b128 v[196:199], v239 offset:28672\n\t"
    "v_mfma_f32_16x16x32_bf16 v[156:159], v[192:195], v[96:99], v[156:159]\n\t"
    "ds_read_b128 v[192:195], v238 offset:28672\n\t"
    "s_waitcnt lgkmcnt(7)\n\t"
    "v_mfma_f32_16x16x32_bf16 v[168:171], v[180:183], v[96:99], 0\n\t"
    "s_waitcnt lgkmcnt(6)\n\t"
    "v_mfma_f32_16x16x32_bf16 v[168:171], v[184:187], v[100:103], v[168:171]\n\t"
    "s_waitcnt lgkmcnt(5)\n\t"
    "v_mfma_f32_16x16x32_bf16 v[168:171], v[188:191], v[104:107], v[168:171]\n\t"
    "s_waitcnt lgkmcnt(4)\n\t"
    "v_mfma_f32_16x16x32_bf16 v[168:171], v[216:219], v[108:111], v[168:171]\n\t"
    "buffer_load_dwordx4 v243, s[44:47], s57 offen offset:3072 lds\n\t"
    "v_mfma_f32_16x16x32_bf16 v[172:175], v[216:219], v[124:127], 0\n\t"
    "v_mfma_f32_16x16x32_bf16 v[172:175], v[188:191], v[120:123], v[172:175]\n\t"
    "v_mfma_f32_16x16x32_bf16 v[172:175], v[184:187], v[116:119], v[172:175]\n\t"
    "ds_read_b128 v[246:249], v244 offset:53248\n\t"
    "v_mfma_f32_16x16x32_bf16 v[172:175], v[180:183], v[112:115], v[172:175]\n\t"
    "v_mfma_f32_16x16x32_bf16 v[176:179], v[180:183], v[128:131], 0\n\t"
    "v_mfma_f32_16x16x32_bf16 v[176:179], v[184:187], v[132:135], v[176:179]\n\t"
    "v_mfma_f32_16x16x32_bf16 v[176:179], v[188:191], v[136:139], v[176:179]\n\t"
    "s_add_u32 s58, s54, 53248\n\t"
    "v_mfma_f32_16x16x32_bf16 v[176:179], v[216:219], v[140:143], v[176:179]\n\t"
    "s_waitcnt lgkmcnt(4)\n\t"
    "v_mfma_f32_16x16x32_bf16 v[188:191], v[204:207], v[140:143], 0\n\t"
    "s_waitcnt lgkmcnt(3)\n\t"
    "v_mfma_f32_16x16x32_bf16 v[188:191], v[200:203], v[136:139], v[188:191]\n\t"
    "s_waitcnt lgkmcnt(2)\n\t"
    "v_mfma_f32_16x16x32_bf16 v[188:191], v[196:199], v[132:135], v[188:191]\n\t"
    "s_waitcnt lgkmcnt(1)\n\t"
    "v_mfma_f32_16x16x32_bf16 v[188:191], v[192:195], v[128:131], v[188:191]\n\t"
    "s_waitcnt lgkmcnt(0)\n\t"
    "buffer_store_dwordx4 v[246:249], v242, s[84:87], s58 offen offset:0 nt\n\t"
    "v_mfma_f32_16x16x32_bf16 v[184:187], v[192:195], v[112:115], 0\n\t"
    "s_add_u32 m0, s55, 53248\n\t"
    "v_mfma_f32_16x16x32_bf16 v[184:187], v[196:199], v[116:119], v[184:187]\n\t"
    "s_add_u32 s57, s53, 53248\n\t"
    "v_mfma_f32_16x16x32_bf16 v[184:187], v[200:203], v[120:123], v[184:187]\n\t"
    "buffer_load_dwordx4 v242, s[44:47], s57 offen offset:0 lds\n\t"
    "v_mfma_f32_16x16x32_bf16 v[184:187], v[204:207], v[124:127], v[184:187]\n\t"
    "v_mfma_f32_16x16x32_bf16 v[180:183], v[204:207], v[108:111], 0\n\t"
    "v_mfma_f32_16x16x32_bf16 v[180:183], v[200:203], v[104:107], v[180:183]\n\t"
    "v_mfma_f32_16x16x32_bf16 v[180:183], v[196:199], v[100:103], v[180:183]\n\t"
    "v_mfma_f32_16x16x32_bf16 v[180:183], v[192:195], v[96:99], v[180:183]\n\t"
    "s_waitcnt lgkmcnt(0)\n\t"
    "s_barrier\n\t"
    "s_setprio 0\n\t"
    "v_fma_f32 v220, v144, s48, v232\n\t"
    "v_fma_f32 v221, v145, s48, v232\n\t"
    "v_fma_f32 v222, v146, s48, v232\n\t"
    "v_fma_f32 v223, v147, s48, v232\n\t"
    "v_exp_f32 v220, v220\n\t"
    "v_exp_f32 v221, v221\n\t"
    "v_exp_f32 v222, v222\n\t"
    "v_exp_f32 v223, v223\n\t"
    "v_fma_f32 v224, v148, s48, v233\n\t"
    "v_fma_f32 v225, v149, s48, v233\n\t"
    "v_fma_f32 v226, v150, s48, v233\n\t"
    "v_fma_f32 v227, v151, s48, v233\n\t"
    "v_exp_f32 v224, v224\n\t"
    "v_exp_f32 v225, v225\n\t"
    "v_exp_f32 v226, v226\n\t"
    "v_exp_f32 v227, v227\n\t"
    "v_cvt_pk_bf16_f32 v192, v220, v221\n\t"
    "v_cvt_pk_bf16_f32 v193, v222, v223\n\t"
    "v_add_f32 v228, v220, v221\n\t"
    "v_add_f32 v228, v228, v222\n\t"
    "v_add_f32 v228, v228, v223\n\t"
    "v_fma_f32 v220, v152, s48, v234\n\t"
    "v_fma_f32 v221, v153, s48, v234\n\t"
    "v_fma_f32 v222, v154, s48, v234\n\t"
    "v_fma_f32 v223, v155, s48, v234\n\t"
    "v_exp_f32 v220, v220\n\t"
    "v_exp_f32 v221, v221\n\t"
    "v_exp_f32 v222, v222\n\t"
    "v_exp_f32 v223, v223\n\t"
    "v_cvt_pk_bf16_f32 v196, v224, v225\n\t"
    "v_cvt_pk_bf16_f32 v197, v226, v227\n\t"
    "v_add_f32 v229, v224, v225\n\t"
    "v_add_f32 v229, v229, v226\n\t"
    "v_add_f32 v229, v229, v227\n\t"
    "v_fma_f32 v224, v156, s48, v232\n\t"
    "v_fma_f32 v225, v157, s48, v232\n\t"
    "v_fma_f32 v226, v158, s48, v232\n\t"
    "v_fma_f32 v227, v159, s48, v232\n\t"
    "v_exp_f32 v224, v224\n\t"
    "v_exp_f32 v225, v225\n\t"
    "v_exp_f32 v226, v226\n\t"
    "v_exp_f32 v227, v227\n\t"
    "v_cvt_pk_bf16_f32 v200, v220, v221\n\t"
    "v_cvt_pk_bf16_f32 v201, v222, v223\n\t"
    "v_add_f32 v230, v220, v221\n\t"
    "v_add_f32 v230, v230, v222\n\t"
    "v_add_f32 v230, v230, v223\n\t"
    "v_fma_f32 v220, v160, s48, v233\n\t"
    "v_fma_f32 v221, v161, s48, v233\n\t"
    "v_fma_f32 v222, v162, s48, v233\n\t"
    "v_fma_f32 v223, v163, s48, v233\n\t"
    "v_exp_f32 v220, v220\n\t"
    "v_exp_f32 v221, v221\n\t"
    "v_exp_f32 v222, v222\n\t"
    "v_exp_f32 v223, v223\n\t"
    "v_cvt_pk_bf16_f32 v194, v224, v225\n\t"
    "v_cvt_pk_bf16_f32 v195, v226, v227\n\t"
    "v_add_f32 v228, v228, v224\n\t"
    "v_add_f32 v228, v228, v225\n\t"
    "v_add_f32 v228, v228, v226\n\t"
    "v_add_f32 v228, v228, v227\n\t"
    "v_fma_f32 v224, v164, s48, v234\n\t"
    "v_fma_f32 v225, v165, s48, v234\n\t"
    "v_fma_f32 v226, v166, s48, v234\n\t"
    "v_fma_f32 v227, v167, s48, v234\n\t"
    "v_exp_f32 v224, v224\n\t"
    "v_exp_f32 v225, v225\n\t"
    "v_exp_f32 v226, v226\n\t"
    "v_exp_f32 v227, v227\n\t"
    "v_cvt_pk_bf16_f32 v198, v220, v221\n\t"
    "v_cvt_pk_bf16_f32 v199, v222, v223\n\t"
    "v_add_f32 v229, v229, v220\n\t"
    "v_add_f32 v229, v229, v221\n\t"
    "v_add_f32 v229, v229, v222\n\t"
    "v_add_f32 v229, v229, v223\n\t"
    "v_fma_f32 v220, v168, s48, v232\n\t"
    "v_fma_f32 v221, v169, s48, v232\n\t"
    "v_fma_f32 v222, v170, s48, v232\n\t"
    "v_fma_f32 v223, v171, s48, v232\n\t"
    "v_exp_f32 v220, v220\n\t"
    "v_exp_f32 v221, v221\n\t"
    "v_exp_f32 v222, v222\n\t"
    "v_exp_f32 v223, v223\n\t"
    "v_cvt_pk_bf16_f32 v202, v224, v225\n\t"
    "v_cvt_pk_bf16_f32 v203, v226, v227\n\t"
    "v_add_f32 v230, v230, v224\n\t"
    "v_add_f32 v230, v230, v225\n\t"
    "v_add_f32 v230, v230, v226\n\t"
    "v_add_f32 v230, v230, v227\n\t"
    "v_fma_f32 v224, v172, s48, v233\n\t"
    "v_fma_f32 v225, v173, s48, v233\n\t"
    "v_fma_f32 v226, v174, s48, v233\n\t"
    "v_fma_f32 v227, v175, s48, v233\n\t"
    "v_exp_f32 v224, v224\n\t"
    "v_exp_f32 v225, v225\n\t"
    "v_exp_f32 v226, v226\n\t"
    "v_exp_f32 v227, v227\n\t"
    "v_cvt_pk_bf16_f32 v204, v220, v221\n\t"
    "v_cvt_pk_bf16_f32 v205, v222, v223\n\t"
    "v_add_f32 v228, v228, v220\n\t"
    "v_add_f32 v228, v228, v221\n\t"
    "v_add_f32 v228, v228, v222\n\t"
    "v_add_f32 v228, v228, v223\n\t"
    "v_fma_f32 v220, v176, s48, v234\n\t"
    "v_fma_f32 v221, v177, s48, v234\n\t"
    "v_fma_f32 v222, v178, s48, v234\n\t"
    "v_fma_f32 v223, v179, s48, v234\n\t"
    "v_exp_f32 v220, v220\n\t"
    "v_exp_f32 v221, v221\n\t"
    "v_exp_f32 v222, v222\n\t"
    "v_exp_f32 v223, v223\n\t"
    "v_cvt_pk_bf16_f32 v208, v224, v225\n\t"
    "v_cvt_pk_bf16_f32 v209, v226, v227\n\t"
    "v_add_f32 v229, v229, v224\n\t"
    "v_add_f32 v229, v229, v225\n\t"
    "v_add_f32 v229, v229, v226\n\t"
    "v_add_f32 v229, v229, v227\n\t"
    "v_fma_f32 v224, v180, s48, v232\n\t"
    "v_fma_f32 v225, v181, s48, v232\n\t"
    "v_fma_f32 v226, v182, s48, v232\n\t"
    "v_fma_f32 v227, v183, s48, v232\n\t"
    "v_exp_f32 v224, v224\n\t"
    "v_exp_f32 v225, v225\n\t"
    "v_exp_f32 v226, v226\n\t"
    "v_exp_f32 v227, v227\n\t"
    "v_cvt_pk_bf16_f32 v212, v220, v221\n\t"
    "v_cvt_pk_bf16_f32 v213, v222, v223\n\t"
    "v_add_f32 v230, v230, v220\n\t"
    "v_add_f32 v230, v230, v221\n\t"
    "v_add_f32 v230, v230, v222\n\t"
    "v_add_f32 v230, v230, v223\n\t"
    "v_fma_f32 v220, v184, s48, v233\n\t"
    "v_fma_f32 v221, v185, s48, v233\n\t"
    "v_fma_f32 v222, v186, s48, v233\n\t"
    "v_fma_f32 v223, v187, s48, v233\n\t"
    "v_exp_f32 v220, v220\n\t"
    "v_exp_f32 v221, v221\n\t"
    "v_exp_f32 v222, v222\n\t"
    "v_exp_f32 v223, v223\n\t"
    "v_cvt_pk_bf16_f32 v206, v224, v225\n\t"
    "v_cvt_pk_bf16_f32 v207, v226, v227\n\t"
    "v_add_f32 v228, v228, v224\n\t"
    "v_add_f32 v228, v228, v225\n\t"
    "v_add_f32 v228, v228, v226\n\t"
    "v_add_f32 v228, v228, v227\n\t"
    "v_fma_f32 v224, v188, s48, v234\n\t"
    "v_fma_f32 v225, v189, s48, v234\n\t"
    "v_fma_f32 v226, v190, s48, v234\n\t"
    "v_fma_f32 v227, v191, s48, v234\n\t"
    "v_exp_f32 v224, v224\n\t"
    "v_exp_f32 v225, v225\n\t"
    "v_exp_f32 v226, v226\n\t"
    "v_exp_f32 v227, v227\n\t"
    "v_cvt_pk_bf16_f32 v210, v220, v221\n\t"
    "v_cvt_pk_bf16_f32 v211, v222, v223\n\t"
    "v_add_f32 v229, v229, v220\n\t"
    "v_add_f32 v229, v229, v221\n\t"
    "v_add_f32 v229, v229, v222\n\t"
    "v_add_f32 v229, v229, v223\n\t"
    "v_cvt_pk_bf16_f32 v214, v224, v225\n\t"
    "v_cvt_pk_bf16_f32 v215, v226, v227\n\t"
    "v_add_f32 v230, v230, v224\n\t"
    "v_add_f32 v230, v230, v225\n\t"
    "v_add_f32 v230, v230, v226\n\t"
    "v_add_f32 v230, v230, v227\n\t"
    "v_max3_f32 v220, v228, v229, v230\n\t"
    "v_cmp_lt_f32 vcc, s49, v220\n\t"
    "s_cbranch_vccnz LFBB5%=\n\t"
    "LRETB5%=:\n\t"
    "v_add_f32 v235, v235, v228\n\t"
    "v_add_f32 v236, v236, v229\n\t"
    "v_add_f32 v237, v237, v230\n\t"
    "s_barrier\n\t"
    "s_setprio 1\n\t"
    "ds_read_b64_tr_b16 v[144:145], v240 offset:49152\n\t"
    "ds_read_b64_tr_b16 v[146:147], v240 offset:53248\n\t"
    "ds_read_b64_tr_b16 v[148:149], v240 offset:57344\n\t"
    "ds_read_b64_tr_b16 v[150:151], v240 offset:61440\n\t"
    "ds_read_b64_tr_b16 v[152:153], v241 offset:57344\n\t"
    "ds_read_b64_tr_b16 v[154:155], v241 offset:61440\n\t"
    "ds_read_b64_tr_b16 v[156:157], v241 offset:49152\n\t"
    "ds_read_b64_tr_b16 v[158:159], v241 offset:53248\n\t"
    "s_waitcnt lgkmcnt(6)\n\t"
    "v_mfma_f32_16x16x32_bf16 v[0:3], v[144:147], v[192:195], v[0:3]\n\t"
    "ds_read_b64_tr_b16 v[160:161], v240 offset:49664\n\t"
    "ds_read_b64_tr_b16 v[162:163], v240 offset:53760\n\t"
    "s_waitcnt lgkmcnt(6)\n\t"
    "v_mfma_f32_16x16x32_bf16 v[0:3], v[148:151], v[204:207], v[0:3]\n\t"
    "ds_read_b64_tr_b16 v[164:165], v240 offset:57856\n\t"
    "ds_read_b64_tr_b16 v[166:167], v240 offset:61952\n\t"
    "v_mfma_f32_16x16x32_bf16 v[4:7], v[148:151], v[208:211], v[4:7]\n\t"
    "ds_read_b64_tr_b16 v[168:169], v241 offset:57856\n\t"
    "ds_read_b64_tr_b16 v[170:171], v241 offset:61952\n\t"
    "v_mfma_f32_16x16x32_bf16 v[4:7], v[144:147], v[196:199], v[4:7]\n\t"
    "ds_read_b64_tr_b16 v[172:173], v241 offset:49664\n\t"
    "ds_read_b64_tr_b16 v[174:175], v241 offset:53760\n\t"
    "v_mfma_f32_16x16x32_bf16 v[8:11], v[144:147], v[200:203], v[8:11]\n\t"
    "ds_read_b128 v[246:249], v244 offset:54272\n\t"
    "v_mfma_f32_16x16x32_bf16 v[8:11], v[148:151], v[212:215], v[8:11]\n\t"
    "s_waitcnt lgkmcnt(11)\n\t"
    "v_mfma_f32_16x16x32_bf16 v[20:23], v[152:155], v[212:215], v[20:23]\n\t"
    "s_waitcnt lgkmcnt(9)\n\t"
    "v_mfma_f32_16x16x32_bf16 v[20:23], v[156:159], v[200:203], v[20:23]\n\t"
    "v_mfma_f32_16x16x32_bf16 v[16:19], v[156:159], v[196:199], v[16:19]\n\t"
    "ds_read_b64_tr_b16 v[176:177], v240 offset:50176\n\t"
    "ds_read_b64_tr_b16 v[178:179], v240 offset:54272\n\t"
    "v_mfma_f32_16x16x32_bf16 v[16:19], v[152:155], v[208:211], v[16:19]\n\t"
    "ds_read_b64_tr_b16 v[144:145], v240 offset:58368\n\t"
    "ds_read_b64_tr_b16 v[146:147], v240 offset:62464\n\t"
    "v_mfma_f32_16x16x32_bf16 v[12:15], v[152:155], v[204:207], v[12:15]\n\t"
    "s_add_u32 s58, s54, 53248\n\t"
    "v_mfma_f32_16x16x32_bf16 v[12:15], v[156:159], v[192:195], v[12:15]\n\t"
    "s_waitcnt lgkmcnt(11)\n\t"
    "v_mfma_f32_16x16x32_bf16 v[24:27], v[160:163], v[192:195], v[24:27]\n\t"
    "s_waitcnt lgkmcnt(9)\n\t"
    "v_mfma_f32_16x16x32_bf16 v[24:27], v[164:167], v[204:207], v[24:27]\n\t"
    "s_waitcnt lgkmcnt(4)\n\t"
    "buffer_store_dwordx4 v[246:249], v243, s[84:87], s58 offen offset:1024 nt\n\t"
    "v_mfma_f32_16x16x32_bf16 v[28:31], v[164:167], v[208:211], v[28:31]\n\t"
    "ds_read_b64_tr_b16 v[148:149], v241 offset:58368\n\t"
    "ds_read_b64_tr_b16 v[150:151], v241 offset:62464\n\t"
    "v_mfma_f32_16x16x32_bf16 v[28:31], v[160:163], v[196:199], v[28:31]\n\t"
    "ds_read_b64_tr_b16 v[152:153], v241 offset:50176\n\t"
    "ds_read_b64_tr_b16 v[154:155], v241 offset:54272\n\t"
    "v_mfma_f32_16x16x32_bf16 v[32:35], v[160:163], v[200:203], v[32:35]\n\t"
    "s_add_u32 m0, s55, 53248\n\t"
    "v_mfma_f32_16x16x32_bf16 v[32:35], v[164:167], v[212:215], v[32:35]\n\t"
    "v_mfma_f32_16x16x32_bf16 v[44:47], v[168:171], v[212:215], v[44:47]\n\t"
    "v_mfma_f32_16x16x32_bf16 v[44:47], v[172:175], v[200:203], v[44:47]\n\t"
    "s_add_u32 s57, s53, 53248\n\t"
    "v_mfma_f32_16x16x32_bf16 v[40:43], v[172:175], v[196:199], v[40:43]\n\t"
    "ds_read_b64_tr_b16 v[156:157], v240 offset:50688\n\t"
    "ds_read_b64_tr_b16 v[158:159], v240 offset:54784\n\t"
    "v_mfma_f32_16x16x32_bf16 v[40:43], v[168:171], v[208:211], v[40:43]\n\t"
    "ds_read_b64_tr_b16 v[160:161], v240 offset:58880\n\t"
    "ds_read_b64_tr_b16 v[162:163], v240 offset:62976\n\t"
    "v_mfma_f32_16x16x32_bf16 v[36:39], v[168:171], v[204:207], v[36:39]\n\t"
    "buffer_load_dwordx4 v243, s[44:47], s57 offen offset:1024 lds\n\t"
    "v_mfma_f32_16x16x32_bf16 v[36:39], v[172:175], v[192:195], v[36:39]\n\t"
    "s_waitcnt lgkmcnt(10)\n\t"
    "v_mfma_f32_16x16x32_bf16 v[48:51], v[176:179], v[192:195], v[48:51]\n\t"
    "s_waitcnt lgkmcnt(8)\n\t"
    "v_mfma_f32_16x16x32_bf16 v[48:51], v[144:147], v[204:207], v[48:51]\n\t"
    "ds_read_b64 v[246:247], v244 offset:55304\n\t"
    "v_mfma_f32_16x16x32_bf16 v[52:55], v[144:147], v[208:211], v[52:55]\n\t"
    "ds_read_b64_tr_b16 v[164:165], v241 offset:58880\n\t"
    "ds_read_b64_tr_b16 v[166:167], v241 offset:62976\n\t"
    "v_mfma_f32_16x16x32_bf16 v[52:55], v[176:179], v[196:199], v[52:55]\n\t"
    "ds_read_b64_tr_b16 v[168:169], v241 offset:50688\n\t"
    "ds_read_b64_tr_b16 v[170:171], v241 offset:54784\n\t"
    "v_mfma_f32_16x16x32_bf16 v[56:59], v[176:179], v[200:203], v[56:59]\n\t"
    "ds_read_b64 v[248:249], v244 offset:55296\n\t"
    "v_mfma_f32_16x16x32_bf16 v[56:59], v[144:147], v[212:215], v[56:59]\n\t"
    "s_waitcnt lgkmcnt(12)\n\t"
    "v_mfma_f32_16x16x32_bf16 v[68:71], v[148:151], v[212:215], v[68:71]\n\t"
    "s_waitcnt lgkmcnt(10)\n\t"
    "v_mfma_f32_16x16x32_bf16 v[68:71], v[152:155], v[200:203], v[68:71]\n\t"
    "v_mfma_f32_16x16x32_bf16 v[64:67], v[152:155], v[196:199], v[64:67]\n\t"
    "ds_read_b128 v[180:183], v238 offset:0\n\t"
    "v_mfma_f32_16x16x32_bf16 v[64:67], v[148:151], v[208:211], v[64:67]\n\t"
    "ds_read_b128 v[184:187], v239 offset:0\n\t"
    "v_mfma_f32_16x16x32_bf16 v[60:63], v[148:151], v[204:207], v[60:63]\n\t"
    "ds_read_b128 v[188:191], v238 offset:1024\n\t"
    "v_mfma_f32_16x16x32_bf16 v[60:63], v[152:155], v[192:195], v[60:63]\n\t"
    "ds_read_b128 v[216:219], v239 offset:1024\n\t"
    "s_waitcnt lgkmcnt(12)\n\t"
    "v_mfma_f32_16x16x32_bf16 v[72:75], v[156:159], v[192:195], v[72:75]\n\t"
    "s_waitcnt lgkmcnt(10)\n\t"
    "v_mfma_f32_16x16x32_bf16 v[72:75], v[160:163], v[204:207], v[72:75]\n\t"
    "s_add_u32 s58, s54, 53248\n\t"
    "v_mfma_f32_16x16x32_bf16 v[76:79], v[160:163], v[208:211], v[76:79]\n\t"
    "v_mfma_f32_16x16x32_bf16 v[76:79], v[156:159], v[196:199], v[76:79]\n\t"
    "s_waitcnt lgkmcnt(4)\n\t"
    "buffer_store_dwordx4 v[246:249], v242, s[84:87], s58 offen offset:2048 nt\n\t"
    "v_mfma_f32_16x16x32_bf16 v[80:83], v[156:159], v[200:203], v[80:83]\n\t"
    "s_add_u32 m0, s55, 53248\n\t"
    "v_mfma_f32_16x16x32_bf16 v[80:83], v[160:163], v[212:215], v[80:83]\n\t"
    "v_mfma_f32_16x16x32_bf16 v[92:95], v[164:167], v[212:215], v[92:95]\n\t"
    "v_mfma_f32_16x16x32_bf16 v[92:95], v[168:171], v[200:203], v[92:95]\n\t"
    "s_add_u32 s57, s53, 53248\n\t"
    "v_mfma_f32_16x16x32_bf16 v[88:91], v[168:171], v[196:199], v[88:91]\n\t"
    "buffer_load_dwordx4 v242, s[44:47], s57 offen offset:2048 lds\n\t"
    "v_mfma_f32_16x16x32_bf16 v[88:91], v[164:167], v[208:211], v[88:91]\n\t"
    "v_mfma_f32_16x16x32_bf16 v[84:87], v[164:167], v[204:207], v[84:87]\n\t"
    "v_mfma_f32_16x16x32_bf16 v[84:87], v[168:171], v[192:195], v[84:87]\n\t"
    "ds_read_b128 v[204:207], v239 offset:5120\n\t"
    "ds_read_b128 v[200:203], v238 offset:5120\n\t"
    "s_waitcnt lgkmcnt(5)\n\t"
    "v_mfma_f32_16x16x32_bf16 v[144:147], v[180:183], v[96:99], 0\n\t"
    "ds_read_b128 v[196:199], v239 offset:4096\n\t"
    "ds_read_b128 v[192:195], v238 offset:4096\n\t"
    "s_waitcnt lgkmcnt(6)\n\t"
    "v_mfma_f32_16x16x32_bf16 v[144:147], v[184:187], v[100:103], v[144:147]\n\t"
    "s_waitcnt lgkmcnt(5)\n\t"
    "v_mfma_f32_16x16x32_bf16 v[144:147], v[188:191], v[104:107], v[144:147]\n\t"
    "s_waitcnt lgkmcnt(4)\n\t"
    "v_mfma_f32_16x16x32_bf16 v[144:147], v[216:219], v[108:111], v[144:147]\n\t"
    "ds_read_b64 v[246:247], v244 offset:56328\n\t"
    "v_mfma_f32_16x16x32_bf16 v[148:151], v[216:219], v[124:127], 0\n\t"
    "ds_read_b64 v[248:249], v244 offset:56320\n\t"
    "v_mfma_f32_16x16x32_bf16 v[148:151], v[188:191], v[120:123], v[148:151]\n\t"
    "v_mfma_f32_16x16x32_bf16 v[148:151], v[184:187], v[116:119], v[148:151]\n\t"
    "v_mfma_f32_16x16x32_bf16 v[148:151], v[180:183], v[112:115], v[148:151]\n\t"
    "v_mfma_f32_16x16x32_bf16 v[152:155], v[180:183], v[128:131], 0\n\t"
    "ds_read_b128 v[180:183], v238 offset:8192\n\t"
    "v_mfma_f32_16x16x32_bf16 v[152:155], v[184:187], v[132:135], v[152:155]\n\t"
    "ds_read_b128 v[184:187], v239 offset:8192\n\t"
    "v_mfma_f32_16x16x32_bf16 v[152:155], v[188:191], v[136:139], v[152:155]\n\t"
    "ds_read_b128 v[188:191], v238 offset:9216\n\t"
    "v_mfma_f32_16x16x32_bf16 v[152:155], v[216:219], v[140:143], v[152:155]\n\t"
    "ds_read_b128 v[216:219], v239 offset:9216\n\t"
    "s_waitcnt lgkmcnt(9)\n\t"
    "v_mfma_f32_16x16x32_bf16 v[164:167], v[204:207], v[140:143], 0\n\t"
    "s_waitcnt lgkmcnt(8)\n\t"
    "v_mfma_f32_16x16x32_bf16 v[164:167], v[200:203], v[136:139], v[164:167]\n\t"
    "s_waitcnt lgkmcnt(7)\n\t"
    "v_mfma_f32_16x16x32_bf16 v[164:167], v[196:199], v[132:135], v[164:167]\n\t"
    "s_waitcnt lgkmcnt(6)\n\t"
    "v_mfma_f32_16x16x32_bf16 v[164:167], v[192:195], v[128:131], v[164:167]\n\t"
    "s_add_u32 s58, s54, 53248\n\t"
    "v_mfma_f32_16x16x32_bf16 v[160:163], v[192:195], v[112:115], 0\n\t"
    "v_mfma_f32_16x16x32_bf16 v[160:163], v[196:199], v[116:119], v[160:163]\n\t"
    "s_waitcnt lgkmcnt(4)\n\t"
    "buffer_store_dwordx4 v[246:249], v243, s[84:87], s58 offen offset:3072 nt\n\t"
    "v_mfma_f32_16x16x32_bf16 v[160:163], v[200:203], v[120:123], v[160:163]\n\t"
    "s_add_u32 m0, s55, 53248\n\t"
    "v_mfma_f32_16x16x32_bf16 v[160:163], v[204:207], v[124:127], v[160:163]\n\t"
    "s_add_u32 s57, s53, 53248\n\t"
    "v_mfma_f32_16x16x32_bf16 v[156:159], v[204:207], v[108:111], 0\n\t"
    "ds_read_b128 v[204:207], v239 offset:13312\n\t"
    "v_mfma_f32_16x16x32_bf16 v[156:159], v[200:203], v[104:107], v[156:159]\n\t"
    "ds_read_b128 v[200:203], v238 offset:13312\n\t"
    "v_mfma_f32_16x16x32_bf16 v[156:159], v[196:199], v[100:103], v[156:159]\n\t"
    "ds_read_b128 v[196:199], v239 offset:12288\n\t"
    "v_mfma_f32_16x16x32_bf16 v[156:159], v[192:195], v[96:99], v[156:159]\n\t"
    "ds_read_b128 v[192:195], v238 offset:12288\n\t"
    "s_waitcnt lgkmcnt(7)\n\t"
    "v_mfma_f32_16x16x32_bf16 v[168:171], v[180:183], v[96:99], 0\n\t"
    "s_waitcnt lgkmcnt(6)\n\t"
    "v_mfma_f32_16x16x32_bf16 v[168:171], v[184:187], v[100:103], v[168:171]\n\t"
    "s_waitcnt lgkmcnt(5)\n\t"
    "v_mfma_f32_16x16x32_bf16 v[168:171], v[188:191], v[104:107], v[168:171]\n\t"
    "s_waitcnt lgkmcnt(4)\n\t"
    "v_mfma_f32_16x16x32_bf16 v[168:171], v[216:219], v[108:111], v[168:171]\n\t"
    "buffer_load_dwordx4 v243, s[44:47], s57 offen offset:3072 lds\n\t"
    "v_mfma_f32_16x16x32_bf16 v[172:175], v[216:219], v[124:127], 0\n\t"
    "v_mfma_f32_16x16x32_bf16 v[172:175], v[188:191], v[120:123], v[172:175]\n\t"
    "v_mfma_f32_16x16x32_bf16 v[172:175], v[184:187], v[116:119], v[172:175]\n\t"
    "v_mfma_f32_16x16x32_bf16 v[172:175], v[180:183], v[112:115], v[172:175]\n\t"
    "v_mfma_f32_16x16x32_bf16 v[176:179], v[180:183], v[128:131], 0\n\t"
    "v_mfma_f32_16x16x32_bf16 v[176:179], v[184:187], v[132:135], v[176:179]\n\t"
    "v_mfma_f32_16x16x32_bf16 v[176:179], v[188:191], v[136:139], v[176:179]\n\t"
    "v_mfma_f32_16x16x32_bf16 v[176:179], v[216:219], v[140:143], v[176:179]\n\t"
    "s_waitcnt lgkmcnt(3)\n\t"
    "v_mfma_f32_16x16x32_bf16 v[188:191], v[204:207], v[140:143], 0\n\t"
    "s_waitcnt lgkmcnt(2)\n\t"
    "v_mfma_f32_16x16x32_bf16 v[188:191], v[200:203], v[136:139], v[188:191]\n\t"
    "s_waitcnt lgkmcnt(1)\n\t"
    "v_mfma_f32_16x16x32_bf16 v[188:191], v[196:199], v[132:135], v[188:191]\n\t"
    "s_waitcnt lgkmcnt(0)\n\t"
    "v_mfma_f32_16x16x32_bf16 v[188:191], v[192:195], v[128:131], v[188:191]\n\t"
    "v_mfma_f32_16x16x32_bf16 v[184:187], v[192:195], v[112:115], 0\n\t"
    "v_mfma_f32_16x16x32_bf16 v[184:187], v[196:199], v[116:119], v[184:187]\n\t"
    "v_mfma_f32_16x16x32_bf16 v[184:187], v[200:203], v[120:123], v[184:187]\n\t"
    "v_mfma_f32_16x16x32_bf16 v[184:187], v[204:207], v[124:127], v[184:187]\n\t"
    "v_mfma_f32_16x16x32_bf16 v[180:183], v[204:207], v[108:111], 0\n\t"
    "v_mfma_f32_16x16x32_bf16 v[180:183], v[200:203], v[104:107], v[180:183]\n\t"
    "v_mfma_f32_16x16x32_bf16 v[180:183], v[196:199], v[100:103], v[180:183]\n\t"
    "v_mfma_f32_16x16x32_bf16 v[180:183], v[192:195], v[96:99], v[180:183]\n\t"
    "s_waitcnt lgkmcnt(0)\n\t"
    "s_barrier\n\t"
    "s_setprio 0\n\t"
    "v_fma_f32 v220, v144, s48, v232\n\t"
    "v_fma_f32 v221, v145, s48, v232\n\t"
    "v_fma_f32 v222, v146, s48, v232\n\t"
    "v_fma_f32 v223, v147, s48, v232\n\t"
    "v_exp_f32 v220, v220\n\t"
    "v_exp_f32 v221, v221\n\t"
    "v_exp_f32 v222, v222\n\t"
    "v_exp_f32 v223, v223\n\t"
    "v_fma_f32 v224, v148, s48, v233\n\t"
    "v_fma_f32 v225, v149, s48, v233\n\t"
    "v_fma_f32 v226, v150, s48, v233\n\t"
    "v_fma_f32 v227, v151, s48, v233\n\t"
    "v_exp_f32 v224, v224\n\t"
    "v_exp_f32 v225, v225\n\t"
    "v_exp_f32 v226, v226\n\t"
    "v_exp_f32 v227, v227\n\t"
    "v_cvt_pk_bf16_f32 v192, v220, v221\n\t"
    "v_cvt_pk_bf16_f32 v193, v222, v223\n\t"
    "v_add_f32 v228, v220, v221\n\t"
    "v_add_f32 v228, v228, v222\n\t"
    "v_add_f32 v228, v228, v223\n\t"
    "v_fma_f32 v220, v152, s48, v234\n\t"
    "v_fma_f32 v221, v153, s48, v234\n\t"
    "v_fma_f32 v222, v154, s48, v234\n\t"
    "v_fma_f32 v223, v155, s48, v234\n\t"
    "v_exp_f32 v220, v220\n\t"
    "v_exp_f32 v221, v221\n\t"
    "v_exp_f32 v222, v222\n\t"
    "v_exp_f32 v223, v223\n\t"
    "v_cvt_pk_bf16_f32 v196, v224, v225\n\t"
    "v_cvt_pk_bf16_f32 v197, v226, v227\n\t"
    "v_add_f32 v229, v224, v225\n\t"
    "v_add_f32 v229, v229, v226\n\t"
    "v_add_f32 v229, v229, v227\n\t"
    "v_fma_f32 v224, v156, s48, v232\n\t"
    "v_fma_f32 v225, v157, s48, v232\n\t"
    "v_fma_f32 v226, v158, s48, v232\n\t"
    "v_fma_f32 v227, v159, s48, v232\n\t"
    "v_exp_f32 v224, v224\n\t"
    "v_exp_f32 v225, v225\n\t"
    "v_exp_f32 v226, v226\n\t"
    "v_exp_f32 v227, v227\n\t"
    "v_cvt_pk_bf16_f32 v200, v220, v221\n\t"
    "v_cvt_pk_bf16_f32 v201, v222, v223\n\t"
    "v_add_f32 v230, v220, v221\n\t"
    "v_add_f32 v230, v230, v222\n\t"
    "v_add_f32 v230, v230, v223\n\t"
    "v_fma_f32 v220, v160, s48, v233\n\t"
    "v_fma_f32 v221, v161, s48, v233\n\t"
    "v_fma_f32 v222, v162, s48, v233\n\t"
    "v_fma_f32 v223, v163, s48, v233\n\t"
    "v_exp_f32 v220, v220\n\t"
    "v_exp_f32 v221, v221\n\t"
    "v_exp_f32 v222, v222\n\t"
    "v_exp_f32 v223, v223\n\t"
    "v_cvt_pk_bf16_f32 v194, v224, v225\n\t"
    "v_cvt_pk_bf16_f32 v195, v226, v227\n\t"
    "v_add_f32 v228, v228, v224\n\t"
    "v_add_f32 v228, v228, v225\n\t"
    "v_add_f32 v228, v228, v226\n\t"
    "v_add_f32 v228, v228, v227\n\t"
    "v_fma_f32 v224, v164, s48, v234\n\t"
    "v_fma_f32 v225, v165, s48, v234\n\t"
    "v_fma_f32 v226, v166, s48, v234\n\t"
    "v_fma_f32 v227, v167, s48, v234\n\t"
    "v_exp_f32 v224, v224\n\t"
    "v_exp_f32 v225, v225\n\t"
    "v_exp_f32 v226, v226\n\t"
    "v_exp_f32 v227, v227\n\t"
    "v_cvt_pk_bf16_f32 v198, v220, v221\n\t"
    "v_cvt_pk_bf16_f32 v199, v222, v223\n\t"
    "v_add_f32 v229, v229, v220\n\t"
    "v_add_f32 v229, v229, v221\n\t"
    "v_add_f32 v229, v229, v222\n\t"
    "v_add_f32 v229, v229, v223\n\t"
    "v_fma_f32 v220, v168, s48, v232\n\t"
    "v_fma_f32 v221, v169, s48, v232\n\t"
    "v_fma_f32 v222, v170, s48, v232\n\t"
    "v_fma_f32 v223, v171, s48, v232\n\t"
    "v_exp_f32 v220, v220\n\t"
    "v_exp_f32 v221, v221\n\t"
    "v_exp_f32 v222, v222\n\t"
    "v_exp_f32 v223, v223\n\t"
    "v_cvt_pk_bf16_f32 v202, v224, v225\n\t"
    "v_cvt_pk_bf16_f32 v203, v226, v227\n\t"
    "v_add_f32 v230, v230, v224\n\t"
    "v_add_f32 v230, v230, v225\n\t"
    "v_add_f32 v230, v230, v226\n\t"
    "v_add_f32 v230, v230, v227\n\t"
    "v_fma_f32 v224, v172, s48, v233\n\t"
    "v_fma_f32 v225, v173, s48, v233\n\t"
    "v_fma_f32 v226, v174, s48, v233\n\t"
    "v_fma_f32 v227, v175, s48, v233\n\t"
    "v_exp_f32 v224, v224\n\t"
    "v_exp_f32 v225, v225\n\t"
    "v_exp_f32 v226, v226\n\t"
    "v_exp_f32 v227, v227\n\t"
    "v_cvt_pk_bf16_f32 v204, v220, v221\n\t"
    "v_cvt_pk_bf16_f32 v205, v222, v223\n\t"
    "v_add_f32 v228, v228, v220\n\t"
    "v_add_f32 v228, v228, v221\n\t"
    "v_add_f32 v228, v228, v222\n\t"
    "v_add_f32 v228, v228, v223\n\t"
    "v_fma_f32 v220, v176, s48, v234\n\t"
    "v_fma_f32 v221, v177, s48, v234\n\t"
    "v_fma_f32 v222, v178, s48, v234\n\t"
    "v_fma_f32 v223, v179, s48, v234\n\t"
    "v_exp_f32 v220, v220\n\t"
    "v_exp_f32 v221, v221\n\t"
    "v_exp_f32 v222, v222\n\t"
    "v_exp_f32 v223, v223\n\t"
    "v_cvt_pk_bf16_f32 v208, v224, v225\n\t"
    "v_cvt_pk_bf16_f32 v209, v226, v227\n\t"
    "v_add_f32 v229, v229, v224\n\t"
    "v_add_f32 v229, v229, v225\n\t"
    "v_add_f32 v229, v229, v226\n\t"
    "v_add_f32 v229, v229, v227\n\t"
    "v_fma_f32 v224, v180, s48, v232\n\t"
    "v_fma_f32 v225, v181, s48, v232\n\t"
    "v_fma_f32 v226, v182, s48, v232\n\t"
    "v_fma_f32 v227, v183, s48, v232\n\t"
    "v_exp_f32 v224, v224\n\t"
    "v_exp_f32 v225, v225\n\t"
    "v_exp_f32 v226, v226\n\t"
    "v_exp_f32 v227, v227\n\t"
    "v_cvt_pk_bf16_f32 v212, v220, v221\n\t"
    "v_cvt_pk_bf16_f32 v213, v222, v223\n\t"
    "v_add_f32 v230, v230, v220\n\t"
    "v_add_f32 v230, v230, v221\n\t"
    "v_add_f32 v230, v230, v222\n\t"
    "v_add_f32 v230, v230, v223\n\t"
    "v_fma_f32 v220, v184, s48, v233\n\t"
    "v_fma_f32 v221, v185, s48, v233\n\t"
    "v_fma_f32 v222, v186, s48, v233\n\t"
    "v_fma_f32 v223, v187, s48, v233\n\t"
    "v_exp_f32 v220, v220\n\t"
    "v_exp_f32 v221, v221\n\t"
    "v_exp_f32 v222, v222\n\t"
    "v_exp_f32 v223, v223\n\t"
    "v_cvt_pk_bf16_f32 v206, v224, v225\n\t"
    "v_cvt_pk_bf16_f32 v207, v226, v227\n\t"
    "v_add_f32 v228, v228, v224\n\t"
    "v_add_f32 v228, v228, v225\n\t"
    "v_add_f32 v228, v228, v226\n\t"
    "v_add_f32 v228, v228, v227\n\t"
    "v_fma_f32 v224, v188, s48, v234\n\t"
    "v_fma_f32 v225, v189, s48, v234\n\t"
    "v_fma_f32 v226, v190, s48, v234\n\t"
    "v_fma_f32 v227, v191, s48, v234\n\t"
    "v_exp_f32 v224, v224\n\t"
    "v_exp_f32 v225, v225\n\t"
    "v_exp_f32 v226, v226\n\t"
    "v_exp_f32 v227, v227\n\t"
    "v_cvt_pk_bf16_f32 v210, v220, v221\n\t"
    "v_cvt_pk_bf16_f32 v211, v222, v223\n\t"
    "v_add_f32 v229, v229, v220\n\t"
    "v_add_f32 v229, v229, v221\n\t"
    "v_add_f32 v229, v229, v222\n\t"
    "v_add_f32 v229, v229, v223\n\t"
    "v_cvt_pk_bf16_f32 v214, v224, v225\n\t"
    "v_cvt_pk_bf16_f32 v215, v226, v227\n\t"
    "v_add_f32 v230, v230, v224\n\t"
    "v_add_f32 v230, v230, v225\n\t"
    "v_add_f32 v230, v230, v226\n\t"
    "v_add_f32 v230, v230, v227\n\t"
    "v_max3_f32 v220, v228, v229, v230\n\t"
    "v_cmp_lt_f32 vcc, s49, v220\n\t"
    "s_cbranch_vccnz LFBB6%=\n\t"
    "LRETB6%=:\n\t"
    "v_add_f32 v235, v235, v228\n\t"
    "v_add_f32 v236, v236, v229\n\t"
    "v_add_f32 v237, v237, v230\n\t"
    "s_waitcnt vmcnt(6)\n\t"
    "s_barrier\n\t"
    "s_setprio 1\n\t"
    "ds_read_b64_tr_b16 v[144:145], v240 offset:32768\n\t"
    "ds_read_b64_tr_b16 v[146:147], v240 offset:36864\n\t"
    "ds_read_b64_tr_b16 v[148:149], v240 offset:40960\n\t"
    "ds_read_b64_tr_b16 v[150:151], v240 offset:45056\n\t"
    "ds_read_b64_tr_b16 v[152:153], v241 offset:40960\n\t"
    "ds_read_b64_tr_b16 v[154:155], v241 offset:45056\n\t"
    "ds_read_b64_tr_b16 v[156:157], v241 offset:32768\n\t"
    "ds_read_b64_tr_b16 v[158:159], v241 offset:36864\n\t"
    "s_waitcnt lgkmcnt(6)\n\t"
    "v_mfma_f32_16x16x32_bf16 v[0:3], v[144:147], v[192:195], v[0:3]\n\t"
    "ds_read_b64_tr_b16 v[160:161], v240 offset:33280\n\t"
    "ds_read_b64_tr_b16 v[162:163], v240 offset:37376\n\t"
    "s_waitcnt lgkmcnt(6)\n\t"
    "v_mfma_f32_16x16x32_bf16 v[0:3], v[148:151], v[204:207], v[0:3]\n\t"
    "ds_read_b64_tr_b16 v[164:165], v240 offset:41472\n\t"
    "ds_read_b64_tr_b16 v[166:167], v240 offset:45568\n\t"
    "v_mfma_f32_16x16x32_bf16 v[4:7], v[148:151], v[208:211], v[4:7]\n\t"
    "ds_read_b64_tr_b16 v[168:169], v241 offset:41472\n\t"
    "ds_read_b64_tr_b16 v[170:171], v241 offset:45568\n\t"
    "v_mfma_f32_16x16x32_bf16 v[4:7], v[144:147], v[196:199], v[4:7]\n\t"
    "ds_read_b64_tr_b16 v[172:173], v241 offset:33280\n\t"
    "ds_read_b64_tr_b16 v[174:175], v241 offset:37376\n\t"
    "v_mfma_f32_16x16x32_bf16 v[8:11], v[144:147], v[200:203], v[8:11]\n\t"
    "ds_read_b128 v[246:249], v244 offset:57344\n\t"
    "v_mfma_f32_16x16x32_bf16 v[8:11], v[148:151], v[212:215], v[8:11]\n\t"
    "s_waitcnt lgkmcnt(11)\n\t"
    "v_mfma_f32_16x16x32_bf16 v[20:23], v[152:155], v[212:215], v[20:23]\n\t"
    "s_waitcnt lgkmcnt(9)\n\t"
    "v_mfma_f32_16x16x32_bf16 v[20:23], v[156:159], v[200:203], v[20:23]\n\t"
    "v_mfma_f32_16x16x32_bf16 v[16:19], v[156:159], v[196:199], v[16:19]\n\t"
    "ds_read_b64_tr_b16 v[176:177], v240 offset:33792\n\t"
    "ds_read_b64_tr_b16 v[178:179], v240 offset:37888\n\t"
    "v_mfma_f32_16x16x32_bf16 v[16:19], v[152:155], v[208:211], v[16:19]\n\t"
    "ds_read_b64_tr_b16 v[144:145], v240 offset:41984\n\t"
    "ds_read_b64_tr_b16 v[146:147], v240 offset:46080\n\t"
    "v_mfma_f32_16x16x32_bf16 v[12:15], v[152:155], v[204:207], v[12:15]\n\t"
    "s_add_u32 s58, s54, 57344\n\t"
    "v_mfma_f32_16x16x32_bf16 v[12:15], v[156:159], v[192:195], v[12:15]\n\t"
    "s_waitcnt lgkmcnt(11)\n\t"
    "v_mfma_f32_16x16x32_bf16 v[24:27], v[160:163], v[192:195], v[24:27]\n\t"
    "s_waitcnt lgkmcnt(9)\n\t"
    "v_mfma_f32_16x16x32_bf16 v[24:27], v[164:167], v[204:207], v[24:27]\n\t"
    "s_waitcnt lgkmcnt(4)\n\t"
    "buffer_store_dwordx4 v[246:249], v242, s[84:87], s58 offen offset:0 nt\n\t"
    "v_mfma_f32_16x16x32_bf16 v[28:31], v[164:167], v[208:211], v[28:31]\n\t"
    "ds_read_b64_tr_b16 v[148:149], v241 offset:41984\n\t"
    "ds_read_b64_tr_b16 v[150:151], v241 offset:46080\n\t"
    "v_mfma_f32_16x16x32_bf16 v[28:31], v[160:163], v[196:199], v[28:31]\n\t"
    "ds_read_b64_tr_b16 v[152:153], v241 offset:33792\n\t"
    "ds_read_b64_tr_b16 v[154:155], v241 offset:37888\n\t"
    "v_mfma_f32_16x16x32_bf16 v[32:35], v[160:163], v[200:203], v[32:35]\n\t"
    "s_add_u32 m0, s55, 57344\n\t"
    "v_mfma_f32_16x16x32_bf16 v[32:35], v[164:167], v[212:215], v[32:35]\n\t"
    "v_mfma_f32_16x16x32_bf16 v[44:47], v[168:171], v[212:215], v[44:47]\n\t"
    "v_mfma_f32_16x16x32_bf16 v[44:47], v[172:175], v[200:203], v[44:47]\n\t"
    "s_add_u32 s57, s53, 57344\n\t"
    "v_mfma_f32_16x16x32_bf16 v[40:43], v[172:175], v[196:199], v[40:43]\n\t"
    "ds_read_b64_tr_b16 v[156:157], v240 offset:34304\n\t"
    "ds_read_b64_tr_b16 v[158:159], v240 offset:38400\n\t"
    "v_mfma_f32_16x16x32_bf16 v[40:43], v[168:171], v[208:211], v[40:43]\n\t"
    "ds_read_b64_tr_b16 v[160:161], v240 offset:42496\n\t"
    "ds_read_b64_tr_b16 v[162:163], v240 offset:46592\n\t"
    "v_mfma_f32_16x16x32_bf16 v[36:39], v[168:171], v[204:207], v[36:39]\n\t"
    "buffer_load_dwordx4 v242, s[44:47], s57 offen offset:0 lds\n\t"
    "v_mfma_f32_16x16x32_bf16 v[36:39], v[172:175], v[192:195], v[36:39]\n\t"
    "s_waitcnt lgkmcnt(10)\n\t"
    "v_mfma_f32_16x16x32_bf16 v[48:51], v[176:179], v[192:195], v[48:51]\n\t"
    "s_waitcnt lgkmcnt(8)\n\t"
    "v_mfma_f32_16x16x32_bf16 v[48:51], v[144:147], v[204:207], v[48:51]\n\t"
    "ds_read_b128 v[246:249], v244 offset:58368\n\t"
    "v_mfma_f32_16x16x32_bf16 v[52:55], v[144:147], v[208:211], v[52:55]\n\t"
    "ds_read_b64_tr_b16 v[164:165], v241 offset:42496\n\t"
    "ds_read_b64_tr_b16 v[166:167], v241 offset:46592\n\t"
    "v_mfma_f32_16x16x32_bf16 v[52:55], v[176:179], v[196:199], v[52:55]\n\t"
    "ds_read_b64_tr_b16 v[168:169], v241 offset:34304\n\t"
    "ds_read_b64_tr_b16 v[170:171], v241 offset:38400\n\t"
    "v_mfma_f32_16x16x32_bf16 v[56:59], v[176:179], v[200:203], v[56:59]\n\t"
    "v_mfma_f32_16x16x32_bf16 v[56:59], v[144:147], v[212:215], v[56:59]\n\t"
    "s_waitcnt lgkmcnt(11)\n\t"
    "v_mfma_f32_16x16x32_bf16 v[68:71], v[148:151], v[212:215], v[68:71]\n\t"
    "s_waitcnt lgkmcnt(9)\n\t"
    "v_mfma_f32_16x16x32_bf16 v[68:71], v[152:155], v[200:203], v[68:71]\n\t"
    "s_add_u32 s58, s54, 57344\n\t"
    "v_mfma_f32_16x16x32_bf16 v[64:67], v[152:155], v[196:199], v[64:67]\n\t"
    "ds_read_b128 v[180:183], v238 offset:16384\n\t"
    "v_mfma_f32_16x16x32_bf16 v[64:67], v[148:151], v[208:211], v[64:67]\n\t"
    "ds_read_b128 v[184:187], v239 offset:16384\n\t"
    "v_mfma_f32_16x16x32_bf16 v[60:63], v[148:151], v[204:207], v[60:63]\n\t"
    "ds_read_b128 v[188:191], v238 offset:17408\n\t"
    "v_mfma_f32_16x16x32_bf16 v[60:63], v[152:155], v[192:195], v[60:63]\n\t"
    "ds_read_b128 v[216:219], v239 offset:17408\n\t"
    "s_waitcnt lgkmcnt(11)\n\t"
    "v_mfma_f32_16x16x32_bf16 v[72:75], v[156:159], v[192:195], v[72:75]\n\t"
    "s_waitcnt lgkmcnt(9)\n\t"
    "v_mfma_f32_16x16x32_bf16 v[72:75], v[160:163], v[204:207], v[72:75]\n\t"
    "s_waitcnt lgkmcnt(8)\n\t"
    "buffer_store_dwordx4 v[246:249], v243, s[84:87], s58 offen offset:1024 nt\n\t"
    "v_mfma_f32_16x16x32_bf16 v[76:79], v[160:163], v[208:211], v[76:79]\n\t"
    "s_add_u32 m0, s55, 57344\n\t"
    "v_mfma_f32_16x16x32_bf16 v[76:79], v[156:159], v[196:199], v[76:79]\n\t"
    "s_add_u32 s57, s53, 57344\n\t"
    "v_mfma_f32_16x16x32_bf16 v[80:83], v[156:159], v[200:203], v[80:83]\n\t"
    "buffer_load_dwordx4 v243, s[44:47], s57 offen offset:1024 lds\n\t"
    "v_mfma_f32_16x16x32_bf16 v[80:83], v[160:163], v[212:215], v[80:83]\n\t"
    "s_waitcnt lgkmcnt(6)\n\t"
    "v_mfma_f32_16x16x32_bf16 v[92:95], v[164:167], v[212:215], v[92:95]\n\t"
    "s_waitcnt lgkmcnt(4)\n\t"
    "v_mfma_f32_16x16x32_bf16 v[92:95], v[168:171], v[200:203], v[92:95]\n\t"
    "ds_read_b64 v[246:247], v244 offset:59400\n\t"
    "v_mfma_f32_16x16x32_bf16 v[88:91], v[168:171], v[196:199], v[88:91]\n\t"
    "ds_read_b64 v[248:249], v244 offset:59392\n\t"
    "v_mfma_f32_16x16x32_bf16 v[88:91], v[164:167], v[208:211], v[88:91]\n\t"
    "v_mfma_f32_16x16x32_bf16 v[84:87], v[164:167], v[204:207], v[84:87]\n\t"
    "v_mfma_f32_16x16x32_bf16 v[84:87], v[168:171], v[192:195], v[84:87]\n\t"
    "ds_read_b128 v[204:207], v239 offset:21504\n\t"
    "ds_read_b128 v[200:203], v238 offset:21504\n\t"
    "s_waitcnt lgkmcnt(7)\n\t"
    "v_mfma_f32_16x16x32_bf16 v[144:147], v[180:183], v[96:99], 0\n\t"
    "ds_read_b128 v[196:199], v239 offset:20480\n\t"
    "ds_read_b128 v[192:195], v238 offset:20480\n\t"
    "s_waitcnt lgkmcnt(8)\n\t"
    "v_mfma_f32_16x16x32_bf16 v[144:147], v[184:187], v[100:103], v[144:147]\n\t"
    "s_waitcnt lgkmcnt(7)\n\t"
    "v_mfma_f32_16x16x32_bf16 v[144:147], v[188:191], v[104:107], v[144:147]\n\t"
    "s_waitcnt lgkmcnt(6)\n\t"
    "v_mfma_f32_16x16x32_bf16 v[144:147], v[216:219], v[108:111], v[144:147]\n\t"
    "s_add_u32 s58, s54, 57344\n\t"
    "v_mfma_f32_16x16x32_bf16 v[148:151], v[216:219], v[124:127], 0\n\t"
    "v_mfma_f32_16x16x32_bf16 v[148:151], v[188:191], v[120:123], v[148:151]\n\t"
    "s_waitcnt lgkmcnt(4)\n\t"
    "buffer_store_dwordx4 v[246:249], v242, s[84:87], s58 offen offset:2048 nt\n\t"
    "v_mfma_f32_16x16x32_bf16 v[148:151], v[184:187], v[116:119], v[148:151]\n\t"
    "s_add_u32 m0, s55, 57344\n\t"
    "v_mfma_f32_16x16x32_bf16 v[148:151], v[180:183], v[112:115], v[148:151]\n\t"
    "s_add_u32 s57, s53, 57344\n\t"
    "v_mfma_f32_16x16x32_bf16 v[152:155], v[180:183], v[128:131], 0\n\t"
    "ds_read_b128 v[180:183], v238 offset:24576\n\t"
    "v_mfma_f32_16x16x32_bf16 v[152:155], v[184:187], v[132:135], v[152:155]\n\t"
    "ds_read_b128 v[184:187], v239 offset:24576\n\t"
    "v_mfma_f32_16x16x32_bf16 v[152:155], v[188:191], v[136:139], v[152:155]\n\t"
    "ds_read_b128 v[188:191], v238 offset:25600\n\t"
    "v_mfma_f32_16x16x32_bf16 v[152:155], v[216:219], v[140:143], v[152:155]\n\t"
    "ds_read_b128 v[216:219], v239 offset:25600\n\t"
    "s_waitcnt lgkmcnt(7)\n\t"
    "v_mfma_f32_16x16x32_bf16 v[164:167], v[204:207], v[140:143], 0\n\t"
    "s_waitcnt lgkmcnt(6)\n\t"
    "v_mfma_f32_16x16x32_bf16 v[164:167], v[200:203], v[136:139], v[164:167]\n\t"
    "s_waitcnt lgkmcnt(5)\n\t"
    "v_mfma_f32_16x16x32_bf16 v[164:167], v[196:199], v[132:135], v[164:167]\n\t"
    "s_waitcnt lgkmcnt(4)\n\t"
    "v_mfma_f32_16x16x32_bf16 v[164:167], v[192:195], v[128:131], v[164:167]\n\t"
    "buffer_load_dwordx4 v242, s[44:47], s57 offen offset:2048 lds\n\t"
    "v_mfma_f32_16x16x32_bf16 v[160:163], v[192:195], v[112:115], 0\n\t"
    "v_mfma_f32_16x16x32_bf16 v[160:163], v[196:199], v[116:119], v[160:163]\n\t"
    "v_mfma_f32_16x16x32_bf16 v[160:163], v[200:203], v[120:123], v[160:163]\n\t"
    "ds_read_b64 v[246:247], v244 offset:60424\n\t"
    "v_mfma_f32_16x16x32_bf16 v[160:163], v[204:207], v[124:127], v[160:163]\n\t"
    "ds_read_b64 v[248:249], v244 offset:60416\n\t"
    "v_mfma_f32_16x16x32_bf16 v[156:159], v[204:207], v[108:111], 0\n\t"
    "ds_read_b128 v[204:207], v239 offset:29696\n\t"
    "v_mfma_f32_16x16x32_bf16 v[156:159], v[200:203], v[104:107], v[156:159]\n\t"
    "ds_read_b128 v[200:203], v238 offset:29696\n\t"
    "v_mfma_f32_16x16x32_bf16 v[156:159], v[196:199], v[100:103], v[156:159]\n\t"
    "ds_read_b128 v[196:199], v239 offset:28672\n\t"
    "v_mfma_f32_16x16x32_bf16 v[156:159], v[192:195], v[96:99], v[156:159]\n\t"
    "ds_read_b128 v[192:195], v238 offset:28672\n\t"
    "s_waitcnt lgkmcnt(9)\n\t"
    "v_mfma_f32_16x16x32_bf16 v[168:171], v[180:183], v[96:99], 0\n\t"
    "s_waitcnt lgkmcnt(8)\n\t"
    "v_mfma_f32_16x16x32_bf16 v[168:171], v[184:187], v[100:103], v[168:171]\n\t"
    "s_waitcnt lgkmcnt(7)\n\t"
    "v_mfma_f32_16x16x32_bf16 v[168:171], v[188:191], v[104:107], v[168:171]\n\t"
    "s_waitcnt lgkmcnt(6)\n\t"
    "v_mfma_f32_16x16x32_bf16 v[168:171], v[216:219], v[108:111], v[168:171]\n\t"
    "s_add_u32 s58, s54, 57344\n\t"
    "v_mfma_f32_16x16x32_bf16 v[172:175], v[216:219], v[124:127], 0\n\t"
    "v_mfma_f32_16x16x32_bf16 v[172:175], v[188:191], v[120:123], v[172:175]\n\t"
    "s_waitcnt lgkmcnt(4)\n\t"
    "buffer_store_dwordx4 v[246:249], v243, s[84:87], s58 offen offset:3072 nt\n\t"
    "v_mfma_f32_16x16x32_bf16 v[172:175], v[184:187], v[116:119], v[172:175]\n\t"
    "s_add_u32 m0, s55, 57344\n\t"
    "v_mfma_f32_16x16x32_bf16 v[172:175], v[180:183], v[112:115], v[172:175]\n\t"
    "s_add_u32 s57, s53, 57344\n\t"
    "v_mfma_f32_16x16x32_bf16 v[176:179], v[180:183], v[128:131], 0\n\t"
    "buffer_load_dwordx4 v243, s[44:47], s57 offen offset:3072 lds\n\t"
    "v_mfma_f32_16x16x32_bf16 v[176:179], v[184:187], v[132:135], v[176:179]\n\t"
    "v_mfma_f32_16x16x32_bf16 v[176:179], v[188:191], v[136:139], v[176:179]\n\t"
    "v_mfma_f32_16x16x32_bf16 v[176:179], v[216:219], v[140:143], v[176:179]\n\t"
    "s_waitcnt lgkmcnt(3)\n\t"
    "v_mfma_f32_16x16x32_bf16 v[188:191], v[204:207], v[140:143], 0\n\t"
    "s_waitcnt lgkmcnt(2)\n\t"
    "v_mfma_f32_16x16x32_bf16 v[188:191], v[200:203], v[136:139], v[188:191]\n\t"
    "s_waitcnt lgkmcnt(1)\n\t"
    "v_mfma_f32_16x16x32_bf16 v[188:191], v[196:199], v[132:135], v[188:191]\n\t"
    "s_waitcnt lgkmcnt(0)\n\t"
    "v_mfma_f32_16x16x32_bf16 v[188:191], v[192:195], v[128:131], v[188:191]\n\t"
    "v_mfma_f32_16x16x32_bf16 v[184:187], v[192:195], v[112:115], 0\n\t"
    "v_mfma_f32_16x16x32_bf16 v[184:187], v[196:199], v[116:119], v[184:187]\n\t"
    "v_mfma_f32_16x16x32_bf16 v[184:187], v[200:203], v[120:123], v[184:187]\n\t"
    "v_mfma_f32_16x16x32_bf16 v[184:187], v[204:207], v[124:127], v[184:187]\n\t"
    "v_mfma_f32_16x16x32_bf16 v[180:183], v[204:207], v[108:111], 0\n\t"
    "v_mfma_f32_16x16x32_bf16 v[180:183], v[200:203], v[104:107], v[180:183]\n\t"
    "v_mfma_f32_16x16x32_bf16 v[180:183], v[196:199], v[100:103], v[180:183]\n\t"
    "v_mfma_f32_16x16x32_bf16 v[180:183], v[192:195], v[96:99], v[180:183]\n\t"
    "s_waitcnt lgkmcnt(0)\n\t"
    "s_barrier\n\t"
    "s_setprio 0\n\t"
    "v_fma_f32 v220, v144, s48, v232\n\t"
    "v_fma_f32 v221, v145, s48, v232\n\t"
    "v_fma_f32 v222, v146, s48, v232\n\t"
    "v_fma_f32 v223, v147, s48, v232\n\t"
    "v_exp_f32 v220, v220\n\t"
    "v_exp_f32 v221, v221\n\t"
    "v_exp_f32 v222, v222\n\t"
    "v_exp_f32 v223, v223\n\t"
    "v_fma_f32 v224, v148, s48, v233\n\t"
    "v_fma_f32 v225, v149, s48, v233\n\t"
    "v_fma_f32 v226, v150, s48, v233\n\t"
    "v_fma_f32 v227, v151, s48, v233\n\t"
    "v_exp_f32 v224, v224\n\t"
    "v_exp_f32 v225, v225\n\t"
    "v_exp_f32 v226, v226\n\t"
    "v_exp_f32 v227, v227\n\t"
    "v_cvt_pk_bf16_f32 v192, v220, v221\n\t"
    "v_cvt_pk_bf16_f32 v193, v222, v223\n\t"
    "v_add_f32 v228, v220, v221\n\t"
    "v_add_f32 v228, v228, v222\n\t"
    "v_add_f32 v228, v228, v223\n\t"
    "v_fma_f32 v220, v152, s48, v234\n\t"
    "v_fma_f32 v221, v153, s48, v234\n\t"
    "v_fma_f32 v222, v154, s48, v234\n\t"
    "v_fma_f32 v223, v155, s48, v234\n\t"
    "v_exp_f32 v220, v220\n\t"
    "v_exp_f32 v221, v221\n\t"
    "v_exp_f32 v222, v222\n\t"
    "v_exp_f32 v223, v223\n\t"
    "v_cvt_pk_bf16_f32 v196, v224, v225\n\t"
    "v_cvt_pk_bf16_f32 v197, v226, v227\n\t"
    "v_add_f32 v229, v224, v225\n\t"
    "v_add_f32 v229, v229, v226\n\t"
    "v_add_f32 v229, v229, v227\n\t"
    "v_fma_f32 v224, v156, s48, v232\n\t"
    "v_fma_f32 v225, v157, s48, v232\n\t"
    "v_fma_f32 v226, v158, s48, v232\n\t"
    "v_fma_f32 v227, v159, s48, v232\n\t"
    "v_exp_f32 v224, v224\n\t"
    "v_exp_f32 v225, v225\n\t"
    "v_exp_f32 v226, v226\n\t"
    "v_exp_f32 v227, v227\n\t"
    "v_cvt_pk_bf16_f32 v200, v220, v221\n\t"
    "v_cvt_pk_bf16_f32 v201, v222, v223\n\t"
    "v_add_f32 v230, v220, v221\n\t"
    "v_add_f32 v230, v230, v222\n\t"
    "v_add_f32 v230, v230, v223\n\t"
    "v_fma_f32 v220, v160, s48, v233\n\t"
    "v_fma_f32 v221, v161, s48, v233\n\t"
    "v_fma_f32 v222, v162, s48, v233\n\t"
    "v_fma_f32 v223, v163, s48, v233\n\t"
    "v_exp_f32 v220, v220\n\t"
    "v_exp_f32 v221, v221\n\t"
    "v_exp_f32 v222, v222\n\t"
    "v_exp_f32 v223, v223\n\t"
    "v_cvt_pk_bf16_f32 v194, v224, v225\n\t"
    "v_cvt_pk_bf16_f32 v195, v226, v227\n\t"
    "v_add_f32 v228, v228, v224\n\t"
    "v_add_f32 v228, v228, v225\n\t"
    "v_add_f32 v228, v228, v226\n\t"
    "v_add_f32 v228, v228, v227\n\t"
    "v_fma_f32 v224, v164, s48, v234\n\t"
    "v_fma_f32 v225, v165, s48, v234\n\t"
    "v_fma_f32 v226, v166, s48, v234\n\t"
    "v_fma_f32 v227, v167, s48, v234\n\t"
    "v_exp_f32 v224, v224\n\t"
    "v_exp_f32 v225, v225\n\t"
    "v_exp_f32 v226, v226\n\t"
    "v_exp_f32 v227, v227\n\t"
    "v_cvt_pk_bf16_f32 v198, v220, v221\n\t"
    "v_cvt_pk_bf16_f32 v199, v222, v223\n\t"
    "v_add_f32 v229, v229, v220\n\t"
    "v_add_f32 v229, v229, v221\n\t"
    "v_add_f32 v229, v229, v222\n\t"
    "v_add_f32 v229, v229, v223\n\t"
    "v_fma_f32 v220, v168, s48, v232\n\t"
    "v_fma_f32 v221, v169, s48, v232\n\t"
    "v_fma_f32 v222, v170, s48, v232\n\t"
    "v_fma_f32 v223, v171, s48, v232\n\t"
    "v_exp_f32 v220, v220\n\t"
    "v_exp_f32 v221, v221\n\t"
    "v_exp_f32 v222, v222\n\t"
    "v_exp_f32 v223, v223\n\t"
    "v_cvt_pk_bf16_f32 v202, v224, v225\n\t"
    "v_cvt_pk_bf16_f32 v203, v226, v227\n\t"
    "v_add_f32 v230, v230, v224\n\t"
    "v_add_f32 v230, v230, v225\n\t"
    "v_add_f32 v230, v230, v226\n\t"
    "v_add_f32 v230, v230, v227\n\t"
    "v_fma_f32 v224, v172, s48, v233\n\t"
    "v_fma_f32 v225, v173, s48, v233\n\t"
    "v_fma_f32 v226, v174, s48, v233\n\t"
    "v_fma_f32 v227, v175, s48, v233\n\t"
    "v_exp_f32 v224, v224\n\t"
    "v_exp_f32 v225, v225\n\t"
    "v_exp_f32 v226, v226\n\t"
    "v_exp_f32 v227, v227\n\t"
    "v_cvt_pk_bf16_f32 v204, v220, v221\n\t"
    "v_cvt_pk_bf16_f32 v205, v222, v223\n\t"
    "v_add_f32 v228, v228, v220\n\t"
    "v_add_f32 v228, v228, v221\n\t"
    "v_add_f32 v228, v228, v222\n\t"
    "v_add_f32 v228, v228, v223\n\t"
    "v_fma_f32 v220, v176, s48, v234\n\t"
    "v_fma_f32 v221, v177, s48, v234\n\t"
    "v_fma_f32 v222, v178, s48, v234\n\t"
    "v_fma_f32 v223, v179, s48, v234\n\t"
    "v_exp_f32 v220, v220\n\t"
    "v_exp_f32 v221, v221\n\t"
    "v_exp_f32 v222, v222\n\t"
    "v_exp_f32 v223, v223\n\t"
    "v_cvt_pk_bf16_f32 v208, v224, v225\n\t"
    "v_cvt_pk_bf16_f32 v209, v226, v227\n\t"
    "v_add_f32 v229, v229, v224\n\t"
    "v_add_f32 v229, v229, v225\n\t"
    "v_add_f32 v229, v229, v226\n\t"
    "v_add_f32 v229, v229, v227\n\t"
    "v_fma_f32 v224, v180, s48, v232\n\t"
    "v_fma_f32 v225, v181, s48, v232\n\t"
    "v_fma_f32 v226, v182, s48, v232\n\t"
    "v_fma_f32 v227, v183, s48, v232\n\t"
    "v_exp_f32 v224, v224\n\t"
    "v_exp_f32 v225, v225\n\t"
    "v_exp_f32 v226, v226\n\t"
    "v_exp_f32 v227, v227\n\t"
    "v_cvt_pk_bf16_f32 v212, v220, v221\n\t"
    "v_cvt_pk_bf16_f32 v213, v222, v223\n\t"
    "v_add_f32 v230, v230, v220\n\t"
    "v_add_f32 v230, v230, v221\n\t"
    "v_add_f32 v230, v230, v222\n\t"
    "v_add_f32 v230, v230, v223\n\t"
    "v_fma_f32 v220, v184, s48, v233\n\t"
    "v_fma_f32 v221, v185, s48, v233\n\t"
    "v_fma_f32 v222, v186, s48, v233\n\t"
    "v_fma_f32 v223, v187, s48, v233\n\t"
    "v_exp_f32 v220, v220\n\t"
    "v_exp_f32 v221, v221\n\t"
    "v_exp_f32 v222, v222\n\t"
    "v_exp_f32 v223, v223\n\t"
    "v_cvt_pk_bf16_f32 v206, v224, v225\n\t"
    "v_cvt_pk_bf16_f32 v207, v226, v227\n\t"
    "v_add_f32 v228, v228, v224\n\t"
    "v_add_f32 v228, v228, v225\n\t"
    "v_add_f32 v228, v228, v226\n\t"
    "v_add_f32 v228, v228, v227\n\t"
    "v_fma_f32 v224, v188, s48, v234\n\t"
    "v_fma_f32 v225, v189, s48, v234\n\t"
    "v_fma_f32 v226, v190, s48, v234\n\t"
    "v_fma_f32 v227, v191, s48, v234\n\t"
    "v_exp_f32 v224, v224\n\t"
    "v_exp_f32 v225, v225\n\t"
    "v_exp_f32 v226, v226\n\t"
    "v_exp_f32 v227, v227\n\t"
    "v_cvt_pk_bf16_f32 v210, v220, v221\n\t"
    "v_cvt_pk_bf16_f32 v211, v222, v223\n\t"
    "v_add_f32 v229, v229, v220\n\t"
    "v_add_f32 v229, v229, v221\n\t"
    "v_add_f32 v229, v229, v222\n\t"
    "v_add_f32 v229, v229, v223\n\t"
    "v_cvt_pk_bf16_f32 v214, v224, v225\n\t"
    "v_cvt_pk_bf16_f32 v215, v226, v227\n\t"
    "v_add_f32 v230, v230, v224\n\t"
    "v_add_f32 v230, v230, v225\n\t"
    "v_add_f32 v230, v230, v226\n\t"
    "v_add_f32 v230, v230, v227\n\t"
    "v_max3_f32 v220, v228, v229, v230\n\t"
    "v_cmp_lt_f32 vcc, s49, v220\n\t"
    "s_cbranch_vccnz LFBB7%=\n\t"
    "LRETB7%=:\n\t"
    "v_add_f32 v235, v235, v228\n\t"
    "v_add_f32 v236, v236, v229\n\t"
    "v_add_f32 v237, v237, v230\n\t"
    "s_cmp_eq_u32 s56, 3\n\t"
    "s_cbranch_scc1 LNBB%=\n\t"
    "s_barrier\n\t"
    "LNBB%=:\n\t"
    "s_cmp_eq_u32 s56, 3\n\t"
    "s_cbranch_scc1 LTAILB%=\n\t"
    "s_waitcnt vmcnt(8)\n\t"
    "v_xor_b32 v254, 64, v231\n\t"
    "s_setprio 1\n\t"
    "ds_read_b64_tr_b16 v[144:145], v240 offset:49152\n\t"
    "ds_read_b64_tr_b16 v[146:147], v240 offset:53248\n\t"
    "ds_read_b64_tr_b16 v[148:149], v240 offset:57344\n\t"
    "ds_read_b64_tr_b16 v[150:151], v240 offset:61440\n\t"
    "ds_read_b64_tr_b16 v[152:153], v241 offset:57344\n\t"
    "ds_read_b64_tr_b16 v[154:155], v241 offset:61440\n\t"
    "ds_read_b64_tr_b16 v[156:157], v241 offset:49152\n\t"
    "ds_read_b64_tr_b16 v[158:159], v241 offset:53248\n\t"
    "s_waitcnt lgkmcnt(6)\n\t"
    "v_mfma_f32_16x16x32_bf16 v[0:3], v[144:147], v[192:195], v[0:3]\n\t"
    "ds_read_b64_tr_b16 v[160:161], v240 offset:49664\n\t"
    "ds_read_b64_tr_b16 v[162:163], v240 offset:53760\n\t"
    "s_waitcnt lgkmcnt(6)\n\t"
    "v_mfma_f32_16x16x32_bf16 v[0:3], v[148:151], v[204:207], v[0:3]\n\t"
    "ds_read_b64_tr_b16 v[164:165], v240 offset:57856\n\t"
    "ds_read_b64_tr_b16 v[166:167], v240 offset:61952\n\t"
    "v_mfma_f32_16x16x32_bf16 v[4:7], v[148:151], v[208:211], v[4:7]\n\t"
    "ds_read_b128 v[96:99], v231 offset:0\n\t"
    "v_mfma_f32_16x16x32_bf16 v[4:7], v[144:147], v[196:199], v[4:7]\n\t"
    "v_mfma_f32_16x16x32_bf16 v[8:11], v[144:147], v[200:203], v[8:11]\n\t"
    "v_mfma_f32_16x16x32_bf16 v[8:11], v[148:151], v[212:215], v[8:11]\n\t"
    "ds_read_b128 v[100:103], v254 offset:0\n\t"
    "s_waitcnt lgkmcnt(8)\n\t"
    "v_mfma_f32_16x16x32_bf16 v[20:23], v[152:155], v[212:215], v[20:23]\n\t"
    "ds_read_b64_tr_b16 v[168:169], v241 offset:57856\n\t"
    "ds_read_b64_tr_b16 v[170:171], v241 offset:61952\n\t"
    "s_waitcnt lgkmcnt(8)\n\t"
    "v_mfma_f32_16x16x32_bf16 v[20:23], v[156:159], v[200:203], v[20:23]\n\t"
    "ds_read_b64_tr_b16 v[172:173], v241 offset:49664\n\t"
    "ds_read_b64_tr_b16 v[174:175], v241 offset:53760\n\t"
    "v_mfma_f32_16x16x32_bf16 v[16:19], v[156:159], v[196:199], v[16:19]\n\t"
    "ds_read_b128 v[104:107], v231 offset:128\n\t"
    "v_mfma_f32_16x16x32_bf16 v[16:19], v[152:155], v[208:211], v[16:19]\n\t"
    "v_mfma_f32_16x16x32_bf16 v[12:15], v[152:155], v[204:207], v[12:15]\n\t"
    "v_mfma_f32_16x16x32_bf16 v[12:15], v[156:159], v[192:195], v[12:15]\n\t"
    "ds_read_b128 v[108:111], v254 offset:128\n\t"
    "s_waitcnt lgkmcnt(10)\n\t"
    "v_mfma_f32_16x16x32_bf16 v[24:27], v[160:163], v[192:195], v[24:27]\n\t"
    "ds_read_b64_tr_b16 v[176:177], v240 offset:50176\n\t"
    "ds_read_b64_tr_b16 v[178:179], v240 offset:54272\n\t"
    "s_waitcnt lgkmcnt(10)\n\t"
    "v_mfma_f32_16x16x32_bf16 v[24:27], v[164:167], v[204:207], v[24:27]\n\t"
    "ds_read_b64_tr_b16 v[144:145], v240 offset:58368\n\t"
    "ds_read_b64_tr_b16 v[146:147], v240 offset:62464\n\t"
    "v_mfma_f32_16x16x32_bf16 v[28:31], v[164:167], v[208:211], v[28:31]\n\t"
    "ds_read_b128 v[112:115], v231 offset:4096\n\t"
    "v_mfma_f32_16x16x32_bf16 v[28:31], v[160:163], v[196:199], v[28:31]\n\t"
    "v_mfma_f32_16x16x32_bf16 v[32:35], v[160:163], v[200:203], v[32:35]\n\t"
    "v_mfma_f32_16x16x32_bf16 v[32:35], v[164:167], v[212:215], v[32:35]\n\t"
    "ds_read_b128 v[116:119], v254 offset:4096\n\t"
    "s_waitcnt lgkmcnt(10)\n\t"
    "v_mfma_f32_16x16x32_bf16 v[44:47], v[168:171], v[212:215], v[44:47]\n\t"
    "ds_read_b64_tr_b16 v[148:149], v241 offset:58368\n\t"
    "ds_read_b64_tr_b16 v[150:151], v241 offset:62464\n\t"
    "s_waitcnt lgkmcnt(10)\n\t"
    "v_mfma_f32_16x16x32_bf16 v[44:47], v[172:175], v[200:203], v[44:47]\n\t"
    "ds_read_b64_tr_b16 v[152:153], v241 offset:50176\n\t"
    "ds_read_b64_tr_b16 v[154:155], v241 offset:54272\n\t"
    "v_mfma_f32_16x16x32_bf16 v[40:43], v[172:175], v[196:199], v[40:43]\n\t"
    "ds_read_b128 v[120:123], v231 offset:4224\n\t"
    "v_mfma_f32_16x16x32_bf16 v[40:43], v[168:171], v[208:211], v[40:43]\n\t"
    "v_mfma_f32_16x16x32_bf16 v[36:39], v[168:171], v[204:207], v[36:39]\n\t"
    "v_mfma_f32_16x16x32_bf16 v[36:39], v[172:175], v[192:195], v[36:39]\n\t"
    "ds_read_b128 v[124:127], v254 offset:4224\n\t"
    "s_waitcnt lgkmcnt(10)\n\t"
    "v_mfma_f32_16x16x32_bf16 v[48:51], v[176:179], v[192:195], v[48:51]\n\t"
    "ds_read_b64_tr_b16 v[156:157], v240 offset:50688\n\t"
    "ds_read_b64_tr_b16 v[158:159], v240 offset:54784\n\t"
    "s_waitcnt lgkmcnt(10)\n\t"
    "v_mfma_f32_16x16x32_bf16 v[48:51], v[144:147], v[204:207], v[48:51]\n\t"
    "ds_read_b64_tr_b16 v[160:161], v240 offset:58880\n\t"
    "ds_read_b64_tr_b16 v[162:163], v240 offset:62976\n\t"
    "v_mfma_f32_16x16x32_bf16 v[52:55], v[144:147], v[208:211], v[52:55]\n\t"
    "s_waitcnt vmcnt(0)\n\t"
    "v_mfma_f32_16x16x32_bf16 v[52:55], v[176:179], v[196:199], v[52:55]\n\t"
    "v_mfma_f32_16x16x32_bf16 v[56:59], v[176:179], v[200:203], v[56:59]\n\t"
    "v_mfma_f32_16x16x32_bf16 v[56:59], v[144:147], v[212:215], v[56:59]\n\t"
    "ds_read_b128 v[128:131], v231 offset:8192\n\t"
    "s_waitcnt lgkmcnt(9)\n\t"
    "v_mfma_f32_16x16x32_bf16 v[68:71], v[148:151], v[212:215], v[68:71]\n\t"
    "ds_read_b64_tr_b16 v[164:165], v241 offset:58880\n\t"
    "ds_read_b64_tr_b16 v[166:167], v241 offset:62976\n\t"
    "s_waitcnt lgkmcnt(9)\n\t"
    "v_mfma_f32_16x16x32_bf16 v[68:71], v[152:155], v[200:203], v[68:71]\n\t"
    "ds_read_b64_tr_b16 v[168:169], v241 offset:50688\n\t"
    "ds_read_b64_tr_b16 v[170:171], v241 offset:54784\n\t"
    "v_mfma_f32_16x16x32_bf16 v[64:67], v[152:155], v[196:199], v[64:67]\n\t"
    "ds_read_b128 v[132:135], v254 offset:8192\n\t"
    "v_mfma_f32_16x16x32_bf16 v[64:67], v[148:151], v[208:211], v[64:67]\n\t"
    "v_mfma_f32_16x16x32_bf16 v[60:63], v[148:151], v[204:207], v[60:63]\n\t"
    "v_mfma_f32_16x16x32_bf16 v[60:63], v[152:155], v[192:195], v[60:63]\n\t"
    "ds_read_b128 v[136:139], v231 offset:8320\n\t"
    "s_waitcnt lgkmcnt(9)\n\t"
    "v_mfma_f32_16x16x32_bf16 v[72:75], v[156:159], v[192:195], v[72:75]\n\t"
    "ds_read_b128 v[180:183], v238 offset:0\n\t"
    "s_waitcnt lgkmcnt(8)\n\t"
    "v_mfma_f32_16x16x32_bf16 v[72:75], v[160:163], v[204:207], v[72:75]\n\t"
    "ds_read_b128 v[184:187], v239 offset:0\n\t"
    "v_mfma_f32_16x16x32_bf16 v[76:79], v[160:163], v[208:211], v[76:79]\n\t"
    "ds_read_b128 v[188:191], v238 offset:1024\n\t"
    "ds_read_b128 v[140:143], v254 offset:8320\n\t"
    "v_mfma_f32_16x16x32_bf16 v[76:79], v[156:159], v[196:199], v[76:79]\n\t"
    "ds_read_b128 v[216:219], v239 offset:1024\n\t"
    "v_mfma_f32_16x16x32_bf16 v[80:83], v[156:159], v[200:203], v[80:83]\n\t"
    "v_mfma_f32_16x16x32_bf16 v[80:83], v[160:163], v[212:215], v[80:83]\n\t"
    "s_waitcnt lgkmcnt(9)\n\t"
    "v_mfma_f32_16x16x32_bf16 v[92:95], v[164:167], v[212:215], v[92:95]\n\t"
    "s_waitcnt lgkmcnt(7)\n\t"
    "v_mfma_f32_16x16x32_bf16 v[92:95], v[168:171], v[200:203], v[92:95]\n\t"
    "v_mfma_f32_16x16x32_bf16 v[88:91], v[168:171], v[196:199], v[88:91]\n\t"
    "v_mfma_f32_16x16x32_bf16 v[88:91], v[164:167], v[208:211], v[88:91]\n\t"
    "v_mfma_f32_16x16x32_bf16 v[84:87], v[164:167], v[204:207], v[84:87]\n\t"
    "v_mfma_f32_16x16x32_bf16 v[84:87], v[168:171], v[192:195], v[84:87]\n\t"
    "s_waitcnt lgkmcnt(1)\n\t"
    "v_mfma_f32_16x16x32_bf16 v[144:147], v[180:183], v[96:99], 0\n\t"
    "ds_read_b128 v[204:207], v239 offset:5120\n\t"
    "v_mfma_f32_16x16x32_bf16 v[144:147], v[184:187], v[100:103], v[144:147]\n\t"
    "ds_read_b128 v[200:203], v238 offset:5120\n\t"
    "v_mfma_f32_16x16x32_bf16 v[144:147], v[188:191], v[104:107], v[144:147]\n\t"
    "ds_read_b128 v[196:199], v239 offset:4096\n\t"
    "s_waitcnt lgkmcnt(3)\n\t"
    "v_mfma_f32_16x16x32_bf16 v[144:147], v[216:219], v[108:111], v[144:147]\n\t"
    "ds_read_b128 v[192:195], v238 offset:4096\n\t"
    "v_mfma_f32_16x16x32_bf16 v[148:151], v[216:219], v[124:127], 0\n\t"
    "v_mfma_f32_16x16x32_bf16 v[148:151], v[188:191], v[120:123], v[148:151]\n\t"
    "v_mfma_f32_16x16x32_bf16 v[148:151], v[184:187], v[116:119], v[148:151]\n\t"
    "v_mfma_f32_16x16x32_bf16 v[148:151], v[180:183], v[112:115], v[148:151]\n\t"
    "v_mfma_f32_16x16x32_bf16 v[152:155], v[180:183], v[128:131], 0\n\t"
    "v_mfma_f32_16x16x32_bf16 v[152:155], v[184:187], v[132:135], v[152:155]\n\t"
    "v_mfma_f32_16x16x32_bf16 v[152:155], v[188:191], v[136:139], v[152:155]\n\t"
    "v_mfma_f32_16x16x32_bf16 v[152:155], v[216:219], v[140:143], v[152:155]\n\t"
    "s_waitcnt lgkmcnt(3)\n\t"
    "v_mfma_f32_16x16x32_bf16 v[164:167], v[204:207], v[140:143], 0\n\t"
    "ds_read_b128 v[180:183], v238 offset:8192\n\t"
    "s_waitcnt lgkmcnt(3)\n\t"
    "v_mfma_f32_16x16x32_bf16 v[164:167], v[200:203], v[136:139], v[164:167]\n\t"
    "ds_read_b128 v[184:187], v239 offset:8192\n\t"
    "s_waitcnt lgkmcnt(3)\n\t"
    "v_mfma_f32_16x16x32_bf16 v[164:167], v[196:199], v[132:135], v[164:167]\n\t"
    "ds_read_b128 v[188:191], v238 offset:9216\n\t"
    "s_waitcnt lgkmcnt(3)\n\t"
    "v_mfma_f32_16x16x32_bf16 v[164:167], v[192:195], v[128:131], v[164:167]\n\t"
    "ds_read_b128 v[216:219], v239 offset:9216\n\t"
    "v_mfma_f32_16x16x32_bf16 v[160:163], v[192:195], v[112:115], 0\n\t"
    "v_mfma_f32_16x16x32_bf16 v[160:163], v[196:199], v[116:119], v[160:163]\n\t"
    "v_mfma_f32_16x16x32_bf16 v[160:163], v[200:203], v[120:123], v[160:163]\n\t"
    "v_mfma_f32_16x16x32_bf16 v[160:163], v[204:207], v[124:127], v[160:163]\n\t"
    "v_mfma_f32_16x16x32_bf16 v[156:159], v[204:207], v[108:111], 0\n\t"
    "v_mfma_f32_16x16x32_bf16 v[156:159], v[200:203], v[104:107], v[156:159]\n\t"
    "v_mfma_f32_16x16x32_bf16 v[156:159], v[196:199], v[100:103], v[156:159]\n\t"
    "v_mfma_f32_16x16x32_bf16 v[156:159], v[192:195], v[96:99], v[156:159]\n\t"
    "s_waitcnt lgkmcnt(3)\n\t"
    "v_mfma_f32_16x16x32_bf16 v[168:171], v[180:183], v[96:99], 0\n\t"
    "ds_read_b128 v[204:207], v239 offset:13312\n\t"
    "s_waitcnt lgkmcnt(3)\n\t"
    "v_mfma_f32_16x16x32_bf16 v[168:171], v[184:187], v[100:103], v[168:171]\n\t"
    "ds_read_b128 v[200:203], v238 offset:13312\n\t"
    "s_waitcnt lgkmcnt(3)\n\t"
    "v_mfma_f32_16x16x32_bf16 v[168:171], v[188:191], v[104:107], v[168:171]\n\t"
    "ds_read_b128 v[196:199], v239 offset:12288\n\t"
    "s_waitcnt lgkmcnt(3)\n\t"
    "v_mfma_f32_16x16x32_bf16 v[168:171], v[216:219], v[108:111], v[168:171]\n\t"
    "ds_read_b128 v[192:195], v238 offset:12288\n\t"
    "v_mfma_f32_16x16x32_bf16 v[172:175], v[216:219], v[124:127], 0\n\t"
    "v_mfma_f32_16x16x32_bf16 v[172:175], v[188:191], v[120:123], v[172:175]\n\t"
    "v_mfma_f32_16x16x32_bf16 v[172:175], v[184:187], v[116:119], v[172:175]\n\t"
    "v_mfma_f32_16x16x32_bf16 v[172:175], v[180:183], v[112:115], v[172:175]\n\t"
    "v_mfma_f32_16x16x32_bf16 v[176:179], v[180:183], v[128:131], 0\n\t"
    "v_mfma_f32_16x16x32_bf16 v[176:179], v[184:187], v[132:135], v[176:179]\n\t"
    "v_mfma_f32_16x16x32_bf16 v[176:179], v[188:191], v[136:139], v[176:179]\n\t"
    "v_mfma_f32_16x16x32_bf16 v[176:179], v[216:219], v[140:143], v[176:179]\n\t"
    "s_waitcnt lgkmcnt(3)\n\t"
    "v_mfma_f32_16x16x32_bf16 v[188:191], v[204:207], v[140:143], 0\n\t"
    "s_waitcnt lgkmcnt(2)\n\t"
    "v_mfma_f32_16x16x32_bf16 v[188:191], v[200:203], v[136:139], v[188:191]\n\t"
    "s_waitcnt lgkmcnt(1)\n\t"
    "v_mfma_f32_16x16x32_bf16 v[188:191], v[196:199], v[132:135], v[188:191]\n\t"
    "s_waitcnt lgkmcnt(0)\n\t"
    "v_mfma_f32_16x16x32_bf16 v[188:191], v[192:195], v[128:131], v[188:191]\n\t"
    "v_mfma_f32_16x16x32_bf16 v[184:187], v[192:195], v[112:115], 0\n\t"
    "v_mfma_f32_16x16x32_bf16 v[184:187], v[196:199], v[116:119], v[184:187]\n\t"
    "v_mfma_f32_16x16x32_bf16 v[184:187], v[200:203], v[120:123], v[184:187]\n\t"
    "v_mfma_f32_16x16x32_bf16 v[184:187], v[204:207], v[124:127], v[184:187]\n\t"
    "v_mfma_f32_16x16x32_bf16 v[180:183], v[204:207], v[108:111], 0\n\t"
    "v_mfma_f32_16x16x32_bf16 v[180:183], v[200:203], v[104:107], v[180:183]\n\t"
    "v_mfma_f32_16x16x32_bf16 v[180:183], v[196:199], v[100:103], v[180:183]\n\t"
    "v_mfma_f32_16x16x32_bf16 v[180:183], v[192:195], v[96:99], v[180:183]\n\t"
    "s_waitcnt lgkmcnt(0)\n\t"
    "s_setprio 0\n\t"
    "v_mov_b32 v220, v235\n\t"
    "v_mov_b32 v221, v236\n\t"
    "v_mov_b32 v222, v237\n\t"
    "v_permlane16_swap_b32 v235, v220\n\t"
    "v_permlane16_swap_b32 v236, v221\n\t"
    "v_permlane16_swap_b32 v237, v222\n\t"
    "v_add_f32 v235, v235, v220\n\t"
    "v_add_f32 v236, v236, v221\n\t"
    "v_add_f32 v237, v237, v222\n\t"
    "v_mov_b32 v220, v235\n\t"
    "v_mov_b32 v221, v236\n\t"
    "v_mov_b32 v222, v237\n\t"
    "v_permlane32_swap_b32 v235, v220\n\t"
    "v_permlane32_swap_b32 v236, v221\n\t"
    "v_permlane32_swap_b32 v237, v222\n\t"
    "v_add_f32 v235, v235, v220\n\t"
    "v_add_f32 v236, v236, v221\n\t"
    "v_add_f32 v237, v237, v222\n\t"
    "v_rcp_f32 v245, v235\n\t"
    "v_rcp_f32 v254, v236\n\t"
    "v_rcp_f32 v255, v237\n\t"
    "v_mov_b32 v220, v245\n\t"
    "v_mov_b32 v222, v254\n\t"
    "v_mov_b32 v224, v255\n\t"
    "v_pk_mul_f32 v[0:1], v[0:1], v[220:221] op_sel_hi:[1,0]\n\t"
    "v_pk_mul_f32 v[2:3], v[2:3], v[220:221] op_sel_hi:[1,0]\n\t"
    "v_pk_mul_f32 v[4:5], v[4:5], v[222:223] op_sel_hi:[1,0]\n\t"
    "v_pk_mul_f32 v[6:7], v[6:7], v[222:223] op_sel_hi:[1,0]\n\t"
    "v_cvt_pk_bf16_f32 v0, v0, v1\n\t"
    "v_cvt_pk_bf16_f32 v1, v2, v3\n\t"
    "v_pk_mul_f32 v[8:9], v[8:9], v[224:225] op_sel_hi:[1,0]\n\t"
    "v_pk_mul_f32 v[10:11], v[10:11], v[224:225] op_sel_hi:[1,0]\n\t"
    "v_cvt_pk_bf16_f32 v4, v4, v5\n\t"
    "v_cvt_pk_bf16_f32 v5, v6, v7\n\t"
    "ds_write_b64 v250, v[0:1] offset:0\n\t"
    "v_pk_mul_f32 v[12:13], v[12:13], v[220:221] op_sel_hi:[1,0]\n\t"
    "v_pk_mul_f32 v[14:15], v[14:15], v[220:221] op_sel_hi:[1,0]\n\t"
    "v_cvt_pk_bf16_f32 v8, v8, v9\n\t"
    "v_cvt_pk_bf16_f32 v9, v10, v11\n\t"
    "ds_write_b64 v250, v[4:5] offset:4096\n\t"
    "v_pk_mul_f32 v[16:17], v[16:17], v[222:223] op_sel_hi:[1,0]\n\t"
    "v_pk_mul_f32 v[18:19], v[18:19], v[222:223] op_sel_hi:[1,0]\n\t"
    "v_cvt_pk_bf16_f32 v12, v12, v13\n\t"
    "v_cvt_pk_bf16_f32 v13, v14, v15\n\t"
    "ds_write_b64 v250, v[8:9] offset:8192\n\t"
    "v_pk_mul_f32 v[20:21], v[20:21], v[224:225] op_sel_hi:[1,0]\n\t"
    "v_pk_mul_f32 v[22:23], v[22:23], v[224:225] op_sel_hi:[1,0]\n\t"
    "v_cvt_pk_bf16_f32 v16, v16, v17\n\t"
    "v_cvt_pk_bf16_f32 v17, v18, v19\n\t"
    "ds_write_b64 v251, v[12:13] offset:0\n\t"
    "v_pk_mul_f32 v[24:25], v[24:25], v[220:221] op_sel_hi:[1,0]\n\t"
    "v_pk_mul_f32 v[26:27], v[26:27], v[220:221] op_sel_hi:[1,0]\n\t"
    "v_cvt_pk_bf16_f32 v20, v20, v21\n\t"
    "v_cvt_pk_bf16_f32 v21, v22, v23\n\t"
    "ds_write_b64 v251, v[16:17] offset:4096\n\t"
    "v_pk_mul_f32 v[28:29], v[28:29], v[222:223] op_sel_hi:[1,0]\n\t"
    "v_pk_mul_f32 v[30:31], v[30:31], v[222:223] op_sel_hi:[1,0]\n\t"
    "v_cvt_pk_bf16_f32 v24, v24, v25\n\t"
    "v_cvt_pk_bf16_f32 v25, v26, v27\n\t"
    "ds_write_b64 v251, v[20:21] offset:8192\n\t"
    "v_pk_mul_f32 v[32:33], v[32:33], v[224:225] op_sel_hi:[1,0]\n\t"
    "v_pk_mul_f32 v[34:35], v[34:35], v[224:225] op_sel_hi:[1,0]\n\t"
    "v_cvt_pk_bf16_f32 v28, v28, v29\n\t"
    "v_cvt_pk_bf16_f32 v29, v30, v31\n\t"
    "ds_write_b64 v252, v[24:25] offset:0\n\t"
    "v_pk_mul_f32 v[36:37], v[36:37], v[220:221] op_sel_hi:[1,0]\n\t"
    "v_pk_mul_f32 v[38:39], v[38:39], v[220:221] op_sel_hi:[1,0]\n\t"
    "v_cvt_pk_bf16_f32 v32, v32, v33\n\t"
    "v_cvt_pk_bf16_f32 v33, v34, v35\n\t"
    "ds_write_b64 v252, v[28:29] offset:4096\n\t"
    "v_pk_mul_f32 v[40:41], v[40:41], v[222:223] op_sel_hi:[1,0]\n\t"
    "v_pk_mul_f32 v[42:43], v[42:43], v[222:223] op_sel_hi:[1,0]\n\t"
    "v_cvt_pk_bf16_f32 v36, v36, v37\n\t"
    "v_cvt_pk_bf16_f32 v37, v38, v39\n\t"
    "ds_write_b64 v252, v[32:33] offset:8192\n\t"
    "v_pk_mul_f32 v[44:45], v[44:45], v[224:225] op_sel_hi:[1,0]\n\t"
    "v_pk_mul_f32 v[46:47], v[46:47], v[224:225] op_sel_hi:[1,0]\n\t"
    "v_cvt_pk_bf16_f32 v40, v40, v41\n\t"
    "v_cvt_pk_bf16_f32 v41, v42, v43\n\t"
    "ds_write_b64 v253, v[36:37] offset:0\n\t"
    "v_pk_mul_f32 v[48:49], v[48:49], v[220:221] op_sel_hi:[1,0]\n\t"
    "v_pk_mul_f32 v[50:51], v[50:51], v[220:221] op_sel_hi:[1,0]\n\t"
    "v_cvt_pk_bf16_f32 v44, v44, v45\n\t"
    "v_cvt_pk_bf16_f32 v45, v46, v47\n\t"
    "ds_write_b64 v253, v[40:41] offset:4096\n\t"
    "v_pk_mul_f32 v[52:53], v[52:53], v[222:223] op_sel_hi:[1,0]\n\t"
    "v_pk_mul_f32 v[54:55], v[54:55], v[222:223] op_sel_hi:[1,0]\n\t"
    "v_cvt_pk_bf16_f32 v48, v48, v49\n\t"
    "v_cvt_pk_bf16_f32 v49, v50, v51\n\t"
    "ds_write_b64 v253, v[44:45] offset:8192\n\t"
    "v_pk_mul_f32 v[56:57], v[56:57], v[224:225] op_sel_hi:[1,0]\n\t"
    "v_pk_mul_f32 v[58:59], v[58:59], v[224:225] op_sel_hi:[1,0]\n\t"
    "v_cvt_pk_bf16_f32 v52, v52, v53\n\t"
    "v_cvt_pk_bf16_f32 v53, v54, v55\n\t"
    "s_waitcnt lgkmcnt(8)\n\t"
    "ds_write_b64 v250, v[48:49] offset:128\n\t"
    "v_pk_mul_f32 v[60:61], v[60:61], v[220:221] op_sel_hi:[1,0]\n\t"
    "v_pk_mul_f32 v[62:63], v[62:63], v[220:221] op_sel_hi:[1,0]\n\t"
    "v_cvt_pk_bf16_f32 v56, v56, v57\n\t"
    "v_cvt_pk_bf16_f32 v57, v58, v59\n\t"
    "ds_write_b64 v250, v[52:53] offset:4224\n\t"
    "v_pk_mul_f32 v[64:65], v[64:65], v[222:223] op_sel_hi:[1,0]\n\t"
    "v_pk_mul_f32 v[66:67], v[66:67], v[222:223] op_sel_hi:[1,0]\n\t"
    "v_cvt_pk_bf16_f32 v60, v60, v61\n\t"
    "v_cvt_pk_bf16_f32 v61, v62, v63\n\t"
    "ds_write_b64 v250, v[56:57] offset:8320\n\t"
    "v_pk_mul_f32 v[68:69], v[68:69], v[224:225] op_sel_hi:[1,0]\n\t"
    "v_pk_mul_f32 v[70:71], v[70:71], v[224:225] op_sel_hi:[1,0]\n\t"
    "v_cvt_pk_bf16_f32 v64, v64, v65\n\t"
    "v_cvt_pk_bf16_f32 v65, v66, v67\n\t"
    "ds_write_b64 v251, v[60:61] offset:128\n\t"
    "v_pk_mul_f32 v[72:73], v[72:73], v[220:221] op_sel_hi:[1,0]\n\t"
    "v_pk_mul_f32 v[74:75], v[74:75], v[220:221] op_sel_hi:[1,0]\n\t"
    "v_cvt_pk_bf16_f32 v68, v68, v69\n\t"
    "v_cvt_pk_bf16_f32 v69, v70, v71\n\t"
    "s_waitcnt lgkmcnt(8)\n\t"
    "ds_write_b64 v251, v[64:65] offset:4224\n\t"
    "v_pk_mul_f32 v[76:77], v[76:77], v[222:223] op_sel_hi:[1,0]\n\t"
    "v_pk_mul_f32 v[78:79], v[78:79], v[222:223] op_sel_hi:[1,0]\n\t"
    "v_cvt_pk_bf16_f32 v72, v72, v73\n\t"
    "v_cvt_pk_bf16_f32 v73, v74, v75\n\t"
    "ds_write_b64 v251, v[68:69] offset:8320\n\t"
    "v_pk_mul_f32 v[80:81], v[80:81], v[224:225] op_sel_hi:[1,0]\n\t"
    "v_pk_mul_f32 v[82:83], v[82:83], v[224:225] op_sel_hi:[1,0]\n\t"
    "v_cvt_pk_bf16_f32 v76, v76, v77\n\t"
    "v_cvt_pk_bf16_f32 v77, v78, v79\n\t"
    "ds_write_b64 v252, v[72:73] offset:128\n\t"
    "v_pk_mul_f32 v[84:85], v[84:85], v[220:221] op_sel_hi:[1,0]\n\t"
    "v_pk_mul_f32 v[86:87], v[86:87], v[220:221] op_sel_hi:[1,0]\n\t"
    "v_cvt_pk_bf16_f32 v80, v80, v81\n\t"
    "v_cvt_pk_bf16_f32 v81, v82, v83\n\t"
    "ds_write_b64 v252, v[76:77] offset:4224\n\t"
    "v_pk_mul_f32 v[88:89], v[88:89], v[222:223] op_sel_hi:[1,0]\n\t"
    "v_pk_mul_f32 v[90:91], v[90:91], v[222:223] op_sel_hi:[1,0]\n\t"
    "v_cvt_pk_bf16_f32 v84, v84, v85\n\t"
    "v_cvt_pk_bf16_f32 v85, v86, v87\n\t"
    "s_waitcnt lgkmcnt(8)\n\t"
    "ds_write_b64 v252, v[80:81] offset:8320\n\t"
    "v_pk_mul_f32 v[92:93], v[92:93], v[224:225] op_sel_hi:[1,0]\n\t"
    "v_pk_mul_f32 v[94:95], v[94:95], v[224:225] op_sel_hi:[1,0]\n\t"
    "v_cvt_pk_bf16_f32 v88, v88, v89\n\t"
    "v_cvt_pk_bf16_f32 v89, v90, v91\n\t"
    "ds_write_b64 v253, v[84:85] offset:128\n\t"
    "v_cvt_pk_bf16_f32 v92, v92, v93\n\t"
    "v_cvt_pk_bf16_f32 v93, v94, v95\n\t"
    "ds_write_b64 v253, v[88:89] offset:4224\n\t"
    "ds_write_b64 v253, v[92:93] offset:8320\n\t"
    "s_waitcnt lgkmcnt(0)\n\t"
    "s_barrier\n\t"
    "s_add_u32 s56, s56, 1\n\t"
    "s_add_u32 s64, s64, 25165824\n\t"
    "s_sub_u32 s54, s64, 25214976\n\t"
    "s_add_u32 s53, s53, 25165824\n\t"
    "s_cmp_eq_u32 s56, 3\n\t"
    "s_cselect_b32 s53, 2130706432, s53\n\t"
    "s_branch LBLKB%=\n\t"
    "LTAILB%=:\n\t"
    "s_waitcnt vmcnt(0)\n\t"
    "v_mov_b32 v220, v235\n\t"
    "v_mov_b32 v221, v236\n\t"
    "v_mov_b32 v222, v237\n\t"
    "v_permlane16_swap_b32 v235, v220\n\t"
    "v_permlane16_swap_b32 v236, v221\n\t"
    "v_permlane16_swap_b32 v237, v222\n\t"
    "v_add_f32 v235, v235, v220\n\t"
    "v_add_f32 v236, v236, v221\n\t"
    "v_add_f32 v237, v237, v222\n\t"
    "v_mov_b32 v220, v235\n\t"
    "v_mov_b32 v221, v236\n\t"
    "v_mov_b32 v222, v237\n\t"
    "v_permlane32_swap_b32 v235, v220\n\t"
    "v_permlane32_swap_b32 v236, v221\n\t"
    "v_permlane32_swap_b32 v237, v222\n\t"
    "v_add_f32 v235, v235, v220\n\t"
    "v_add_f32 v236, v236, v221\n\t"
    "v_add_f32 v237, v237, v222\n\t"
    "v_rcp_f32 v245, v235\n\t"
    "v_rcp_f32 v254, v236\n\t"
    "v_rcp_f32 v255, v237\n\t"
    "v_mbcnt_lo_u32_b32 v233, -1, 0\n\t"
    "v_mbcnt_hi_u32_b32 v233, -1, v233\n\t"
    "v_lshlrev_b32 v232, 4, v233\n\t"
    "v_add_u32 v232, s63, v232\n\t"
    "s_setprio 1\n\t"
    "ds_read_b64_tr_b16 v[144:145], v240 offset:49152\n\t"
    "ds_read_b64_tr_b16 v[146:147], v240 offset:53248\n\t"
    "ds_read_b64_tr_b16 v[148:149], v240 offset:57344\n\t"
    "ds_read_b64_tr_b16 v[150:151], v240 offset:61440\n\t"
    "ds_read_b64_tr_b16 v[152:153], v241 offset:57344\n\t"
    "ds_read_b64_tr_b16 v[154:155], v241 offset:61440\n\t"
    "ds_read_b64_tr_b16 v[156:157], v241 offset:49152\n\t"
    "ds_read_b64_tr_b16 v[158:159], v241 offset:53248\n\t"
    "s_waitcnt lgkmcnt(6)\n\t"
    "v_mfma_f32_16x16x32_bf16 v[0:3], v[144:147], v[192:195], v[0:3]\n\t"
    "ds_read_b64_tr_b16 v[160:161], v240 offset:49664\n\t"
    "ds_read_b64_tr_b16 v[162:163], v240 offset:53760\n\t"
    "ds_read_b64_tr_b16 v[164:165], v240 offset:57856\n\t"
    "ds_read_b64_tr_b16 v[166:167], v240 offset:61952\n\t"
    "s_waitcnt lgkmcnt(8)\n\t"
    "v_mfma_f32_16x16x32_bf16 v[0:3], v[148:151], v[204:207], v[0:3]\n\t"
    "ds_read_b64_tr_b16 v[168:169], v241 offset:57856\n\t"
    "ds_read_b64_tr_b16 v[170:171], v241 offset:61952\n\t"
    "ds_read_b64_tr_b16 v[172:173], v241 offset:49664\n\t"
    "ds_read_b64_tr_b16 v[174:175], v241 offset:53760\n\t"
    "s_waitcnt lgkmcnt(10)\n\t"
    "v_mfma_f32_16x16x32_bf16 v[12:15], v[152:155], v[204:207], v[12:15]\n\t"
    "ds_read_b64_tr_b16 v[176:177], v240 offset:50176\n\t"
    "ds_read_b64_tr_b16 v[178:179], v240 offset:54272\n\t"
    "s_waitcnt lgkmcnt(8)\n\t"
    "ds_read_b64_tr_b16 v[180:181], v240 offset:58368\n\t"
    "ds_read_b64_tr_b16 v[182:183], v240 offset:62464\n\t"
    "v_mfma_f32_16x16x32_bf16 v[12:15], v[156:159], v[192:195], v[12:15]\n\t"
    "ds_read_b64_tr_b16 v[184:185], v241 offset:58368\n\t"
    "ds_read_b64_tr_b16 v[186:187], v241 offset:62464\n\t"
    "s_waitcnt lgkmcnt(8)\n\t"
    "ds_read_b64_tr_b16 v[188:189], v241 offset:50176\n\t"
    "ds_read_b64_tr_b16 v[190:191], v241 offset:54272\n\t"
    "v_mfma_f32_16x16x32_bf16 v[24:27], v[160:163], v[192:195], v[24:27]\n\t"
    "ds_read_b64_tr_b16 v[128:129], v240 offset:50688\n\t"
    "ds_read_b64_tr_b16 v[130:131], v240 offset:54784\n\t"
    "s_waitcnt lgkmcnt(8)\n\t"
    "ds_read_b64_tr_b16 v[132:133], v240 offset:58880\n\t"
    "ds_read_b64_tr_b16 v[134:135], v240 offset:62976\n\t"
    "v_mfma_f32_16x16x32_bf16 v[24:27], v[164:167], v[204:207], v[24:27]\n\t"
    "ds_read_b64_tr_b16 v[136:137], v241 offset:58880\n\t"
    "ds_read_b64_tr_b16 v[138:139], v241 offset:62976\n\t"
    "v_mfma_f32_16x16x32_bf16 v[36:39], v[168:171], v[204:207], v[36:39]\n\t"
    "s_waitcnt lgkmcnt(8)\n\t"
    "ds_read_b64_tr_b16 v[140:141], v241 offset:50688\n\t"
    "ds_read_b64_tr_b16 v[142:143], v241 offset:54784\n\t"
    "v_mfma_f32_16x16x32_bf16 v[36:39], v[172:175], v[192:195], v[36:39]\n\t"
    "v_mul_f32 v0, v0, v245\n\t"
    "v_mul_f32 v1, v1, v245\n\t"
    "v_mul_f32 v2, v2, v245\n\t"
    "v_mul_f32 v3, v3, v245\n\t"
    "v_mfma_f32_16x16x32_bf16 v[48:51], v[176:179], v[192:195], v[48:51]\n\t"
    "v_cvt_pk_bf16_f32 v0, v0, v1\n\t"
    "v_cvt_pk_bf16_f32 v1, v2, v3\n\t"
    "v_mfma_f32_16x16x32_bf16 v[48:51], v[180:183], v[204:207], v[48:51]\n\t"
    "ds_write_b64 v250, v[0:1] offset:0\n\t"
    "v_mul_f32 v12, v12, v245\n\t"
    "v_mul_f32 v13, v13, v245\n\t"
    "v_mul_f32 v14, v14, v245\n\t"
    "v_mul_f32 v15, v15, v245\n\t"
    "v_mfma_f32_16x16x32_bf16 v[60:63], v[184:187], v[204:207], v[60:63]\n\t"
    "v_cvt_pk_bf16_f32 v12, v12, v13\n\t"
    "v_cvt_pk_bf16_f32 v13, v14, v15\n\t"
    "s_waitcnt lgkmcnt(9)\n\t"
    "v_mfma_f32_16x16x32_bf16 v[60:63], v[188:191], v[192:195], v[60:63]\n\t"
    "ds_write_b64 v251, v[12:13] offset:0\n\t"
    "v_mul_f32 v24, v24, v245\n\t"
    "v_mul_f32 v25, v25, v245\n\t"
    "v_mul_f32 v26, v26, v245\n\t"
    "v_mul_f32 v27, v27, v245\n\t"
    "s_waitcnt lgkmcnt(8)\n\t"
    "v_mfma_f32_16x16x32_bf16 v[72:75], v[128:131], v[192:195], v[72:75]\n\t"
    "v_cvt_pk_bf16_f32 v24, v24, v25\n\t"
    "v_cvt_pk_bf16_f32 v25, v26, v27\n\t"
    "s_waitcnt lgkmcnt(6)\n\t"
    "v_mfma_f32_16x16x32_bf16 v[72:75], v[132:135], v[204:207], v[72:75]\n\t"
    "ds_write_b64 v252, v[24:25] offset:0\n\t"
    "v_mul_f32 v36, v36, v245\n\t"
    "v_mul_f32 v37, v37, v245\n\t"
    "v_mul_f32 v38, v38, v245\n\t"
    "v_mul_f32 v39, v39, v245\n\t"
    "s_waitcnt lgkmcnt(5)\n\t"
    "v_mfma_f32_16x16x32_bf16 v[84:87], v[136:139], v[204:207], v[84:87]\n\t"
    "v_cvt_pk_bf16_f32 v36, v36, v37\n\t"
    "v_cvt_pk_bf16_f32 v37, v38, v39\n\t"
    "s_waitcnt lgkmcnt(3)\n\t"
    "v_mfma_f32_16x16x32_bf16 v[84:87], v[140:143], v[192:195], v[84:87]\n\t"
    "ds_write_b64 v253, v[36:37] offset:0\n\t"
    "v_mul_f32 v48, v48, v245\n\t"
    "v_mul_f32 v49, v49, v245\n\t"
    "v_mul_f32 v50, v50, v245\n\t"
    "v_mul_f32 v51, v51, v245\n\t"
    "v_mfma_f32_16x16x32_bf16 v[88:91], v[140:143], v[196:199], v[88:91]\n\t"
    "v_cvt_pk_bf16_f32 v48, v48, v49\n\t"
    "v_cvt_pk_bf16_f32 v49, v50, v51\n\t"
    "v_mfma_f32_16x16x32_bf16 v[88:91], v[136:139], v[208:211], v[88:91]\n\t"
    "ds_write_b64 v250, v[48:49] offset:128\n\t"
    "v_mul_f32 v60, v60, v245\n\t"
    "v_mul_f32 v61, v61, v245\n\t"
    "v_mul_f32 v62, v62, v245\n\t"
    "v_mul_f32 v63, v63, v245\n\t"
    "v_mfma_f32_16x16x32_bf16 v[76:79], v[132:135], v[208:211], v[76:79]\n\t"
    "v_cvt_pk_bf16_f32 v60, v60, v61\n\t"
    "v_cvt_pk_bf16_f32 v61, v62, v63\n\t"
    "v_mfma_f32_16x16x32_bf16 v[76:79], v[128:131], v[196:199], v[76:79]\n\t"
    "ds_write_b64 v251, v[60:61] offset:128\n\t"
    "v_mul_f32 v72, v72, v245\n\t"
    "v_mul_f32 v73, v73, v245\n\t"
    "v_mul_f32 v74, v74, v245\n\t"
    "v_mul_f32 v75, v75, v245\n\t"
    "v_mfma_f32_16x16x32_bf16 v[64:67], v[188:191], v[196:199], v[64:67]\n\t"
    "v_cvt_pk_bf16_f32 v72, v72, v73\n\t"
    "v_cvt_pk_bf16_f32 v73, v74, v75\n\t"
    "v_mfma_f32_16x16x32_bf16 v[64:67], v[184:187], v[208:211], v[64:67]\n\t"
    "ds_write_b64 v252, v[72:73] offset:128\n\t"
    "v_mul_f32 v84, v84, v245\n\t"
    "v_mul_f32 v85, v85, v245\n\t"
    "v_mul_f32 v86, v86, v245\n\t"
    "v_mul_f32 v87, v87, v245\n\t"
    "v_mfma_f32_16x16x32_bf16 v[52:55], v[180:183], v[208:211], v[52:55]\n\t"
    "v_cvt_pk_bf16_f32 v84, v84, v85\n\t"
    "v_cvt_pk_bf16_f32 v85, v86, v87\n\t"
    "v_mfma_f32_16x16x32_bf16 v[52:55], v[176:179], v[196:199], v[52:55]\n\t"
    "ds_write_b64 v253, v[84:85] offset:128\n\t"
    "v_mul_f32 v88, v88, v254\n\t"
    "v_mul_f32 v89, v89, v254\n\t"
    "v_mul_f32 v90, v90, v254\n\t"
    "v_mul_f32 v91, v91, v254\n\t"
    "v_mfma_f32_16x16x32_bf16 v[40:43], v[172:175], v[196:199], v[40:43]\n\t"
    "v_cvt_pk_bf16_f32 v88, v88, v89\n\t"
    "v_cvt_pk_bf16_f32 v89, v90, v91\n\t"
    "ds_read_b128 v[96:99], v232 offset:0\n\t"
    "v_mfma_f32_16x16x32_bf16 v[40:43], v[168:171], v[208:211], v[40:43]\n\t"
    "v_mul_f32 v76, v76, v254\n\t"
    "v_mul_f32 v77, v77, v254\n\t"
    "v_mul_f32 v78, v78, v254\n\t"
    "v_mul_f32 v79, v79, v254\n\t"
    "ds_write_b64 v253, v[88:89] offset:4224\n\t"
    "ds_read_b128 v[100:103], v232 offset:1024\n\t"
    "v_mfma_f32_16x16x32_bf16 v[28:31], v[164:167], v[208:211], v[28:31]\n\t"
    "v_cvt_pk_bf16_f32 v76, v76, v77\n\t"
    "v_cvt_pk_bf16_f32 v77, v78, v79\n\t"
    "ds_read_b64 v[104:105], v232 offset:2056\n\t"
    "ds_read_b64 v[106:107], v232 offset:2048\n\t"
    "v_mfma_f32_16x16x32_bf16 v[28:31], v[160:163], v[196:199], v[28:31]\n\t"
    "v_mul_f32 v64, v64, v254\n\t"
    "v_mul_f32 v65, v65, v254\n\t"
    "v_mul_f32 v66, v66, v254\n\t"
    "v_mul_f32 v67, v67, v254\n\t"
    "ds_write_b64 v252, v[76:77] offset:4224\n\t"
    "s_waitcnt lgkmcnt(10)\n\t"
    "ds_read_b64 v[108:109], v232 offset:3080\n\t"
    "ds_read_b64 v[110:111], v232 offset:3072\n\t"
    "v_mfma_f32_16x16x32_bf16 v[16:19], v[156:159], v[196:199], v[16:19]\n\t"
    "v_cvt_pk_bf16_f32 v64, v64, v65\n\t"
    "v_cvt_pk_bf16_f32 v65, v66, v67\n\t"
    "v_mfma_f32_16x16x32_bf16 v[16:19], v[152:155], v[208:211], v[16:19]\n\t"
    "v_mul_f32 v52, v52, v254\n\t"
    "v_mul_f32 v53, v53, v254\n\t"
    "v_mul_f32 v54, v54, v254\n\t"
    "v_mul_f32 v55, v55, v254\n\t"
    "ds_write_b64 v251, v[64:65] offset:4224\n\t"
    "s_add_u32 s58, s64, 0\n\t"
    "s_waitcnt lgkmcnt(8)\n\t"
    "buffer_store_dwordx4 v[96:99], v242, s[84:87], s58 offen offset:0 nt\n\t"
    "v_mfma_f32_16x16x32_bf16 v[4:7], v[148:151], v[208:211], v[4:7]\n\t"
    "v_cvt_pk_bf16_f32 v52, v52, v53\n\t"
    "v_cvt_pk_bf16_f32 v53, v54, v55\n\t"
    "s_add_u32 s58, s64, 0\n\t"
    "s_waitcnt lgkmcnt(6)\n\t"
    "buffer_store_dwordx4 v[100:103], v243, s[84:87], s58 offen offset:1024 nt\n\t"
    "v_mfma_f32_16x16x32_bf16 v[4:7], v[144:147], v[196:199], v[4:7]\n\t"
    "v_mul_f32 v40, v40, v254\n\t"
    "v_mul_f32 v41, v41, v254\n\t"
    "v_mul_f32 v42, v42, v254\n\t"
    "v_mul_f32 v43, v43, v254\n\t"
    "ds_write_b64 v250, v[52:53] offset:4224\n\t"
    "s_add_u32 s58, s64, 0\n\t"
    "s_waitcnt lgkmcnt(5)\n\t"
    "buffer_store_dwordx4 v[104:107], v242, s[84:87], s58 offen offset:2048 nt\n\t"
    "v_mfma_f32_16x16x32_bf16 v[8:11], v[144:147], v[200:203], v[8:11]\n\t"
    "v_cvt_pk_bf16_f32 v40, v40, v41\n\t"
    "v_cvt_pk_bf16_f32 v41, v42, v43\n\t"
    "s_add_u32 s58, s64, 0\n\t"
    "s_waitcnt lgkmcnt(2)\n\t"
    "buffer_store_dwordx4 v[108:111], v243, s[84:87], s58 offen offset:3072 nt\n\t"
    "v_mfma_f32_16x16x32_bf16 v[8:11], v[148:151], v[212:215], v[8:11]\n\t"
    "v_mul_f32 v28, v28, v254\n\t"
    "v_mul_f32 v29, v29, v254\n\t"
    "v_mul_f32 v30, v30, v254\n\t"
    "v_mul_f32 v31, v31, v254\n\t"
    "ds_write_b64 v253, v[40:41] offset:4096\n\t"
    "v_mfma_f32_16x16x32_bf16 v[20:23], v[152:155], v[212:215], v[20:23]\n\t"
    "v_cvt_pk_bf16_f32 v28, v28, v29\n\t"
    "v_cvt_pk_bf16_f32 v29, v30, v31\n\t"
    "v_mfma_f32_16x16x32_bf16 v[20:23], v[156:159], v[200:203], v[20:23]\n\t"
    "v_mul_f32 v16, v16, v254\n\t"
    "v_mul_f32 v17, v17, v254\n\t"
    "v_mul_f32 v18, v18, v254\n\t"
    "v_mul_f32 v19, v19, v254\n\t"
    "ds_write_b64 v252, v[28:29] offset:4096\n\t"
    "v_mfma_f32_16x16x32_bf16 v[32:35], v[160:163], v[200:203], v[32:35]\n\t"
    "v_cvt_pk_bf16_f32 v16, v16, v17\n\t"
    "v_cvt_pk_bf16_f32 v17, v18, v19\n\t"
    "v_mfma_f32_16x16x32_bf16 v[32:35], v[164:167], v[212:215], v[32:35]\n\t"
    "v_mul_f32 v4, v4, v254\n\t"
    "v_mul_f32 v5, v5, v254\n\t"
    "v_mul_f32 v6, v6, v254\n\t"
    "v_mul_f32 v7, v7, v254\n\t"
    "ds_write_b64 v251, v[16:17] offset:4096\n\t"
    "v_mfma_f32_16x16x32_bf16 v[44:47], v[168:171], v[212:215], v[44:47]\n\t"
    "v_cvt_pk_bf16_f32 v4, v4, v5\n\t"
    "v_cvt_pk_bf16_f32 v5, v6, v7\n\t"
    "v_mfma_f32_16x16x32_bf16 v[44:47], v[172:175], v[200:203], v[44:47]\n\t"
    "ds_write_b64 v250, v[4:5] offset:4096\n\t"
    "v_mul_f32 v8, v8, v255\n\t"
    "v_mul_f32 v9, v9, v255\n\t"
    "v_mul_f32 v10, v10, v255\n\t"
    "v_mul_f32 v11, v11, v255\n\t"
    "v_mfma_f32_16x16x32_bf16 v[56:59], v[176:179], v[200:203], v[56:59]\n\t"
    "v_cvt_pk_bf16_f32 v8, v8, v9\n\t"
    "v_cvt_pk_bf16_f32 v9, v10, v11\n\t"
    "ds_read_b128 v[112:115], v232 offset:4096\n\t"
    "v_mfma_f32_16x16x32_bf16 v[56:59], v[180:183], v[212:215], v[56:59]\n\t"
    "ds_write_b64 v250, v[8:9] offset:8192\n\t"
    "v_mul_f32 v20, v20, v255\n\t"
    "v_mul_f32 v21, v21, v255\n\t"
    "v_mul_f32 v22, v22, v255\n\t"
    "v_mul_f32 v23, v23, v255\n\t"
    "ds_read_b128 v[116:119], v232 offset:5120\n\t"
    "v_mfma_f32_16x16x32_bf16 v[68:71], v[184:187], v[212:215], v[68:71]\n\t"
    "v_cvt_pk_bf16_f32 v20, v20, v21\n\t"
    "v_cvt_pk_bf16_f32 v21, v22, v23\n\t"
    "ds_read_b64 v[120:121], v232 offset:6152\n\t"
    "ds_read_b64 v[122:123], v232 offset:6144\n\t"
    "v_mfma_f32_16x16x32_bf16 v[68:71], v[188:191], v[200:203], v[68:71]\n\t"
    "ds_write_b64 v251, v[20:21] offset:8192\n\t"
    "v_mul_f32 v32, v32, v255\n\t"
    "v_mul_f32 v33, v33, v255\n\t"
    "v_mul_f32 v34, v34, v255\n\t"
    "v_mul_f32 v35, v35, v255\n\t"
    "ds_read_b64 v[124:125], v232 offset:7176\n\t"
    "ds_read_b64 v[126:127], v232 offset:7168\n\t"
    "v_mfma_f32_16x16x32_bf16 v[80:83], v[128:131], v[200:203], v[80:83]\n\t"
    "v_cvt_pk_bf16_f32 v32, v32, v33\n\t"
    "v_cvt_pk_bf16_f32 v33, v34, v35\n\t"
    "v_mfma_f32_16x16x32_bf16 v[80:83], v[132:135], v[212:215], v[80:83]\n\t"
    "ds_write_b64 v252, v[32:33] offset:8192\n\t"
    "v_mul_f32 v44, v44, v255\n\t"
    "v_mul_f32 v45, v45, v255\n\t"
    "v_mul_f32 v46, v46, v255\n\t"
    "v_mul_f32 v47, v47, v255\n\t"
    "s_add_u32 s58, s64, 4096\n\t"
    "s_waitcnt lgkmcnt(8)\n\t"
    "buffer_store_dwordx4 v[112:115], v242, s[84:87], s58 offen offset:0 nt\n\t"
    "v_mfma_f32_16x16x32_bf16 v[92:95], v[136:139], v[212:215], v[92:95]\n\t"
    "v_cvt_pk_bf16_f32 v44, v44, v45\n\t"
    "v_cvt_pk_bf16_f32 v45, v46, v47\n\t"
    "s_add_u32 s58, s64, 4096\n\t"
    "s_waitcnt lgkmcnt(6)\n\t"
    "buffer_store_dwordx4 v[116:119], v243, s[84:87], s58 offen offset:1024 nt\n\t"
    "v_mfma_f32_16x16x32_bf16 v[92:95], v[140:143], v[200:203], v[92:95]\n\t"
    "ds_write_b64 v253, v[44:45] offset:8192\n\t"
    "v_mul_f32 v56, v56, v255\n\t"
    "v_mul_f32 v57, v57, v255\n\t"
    "v_mul_f32 v58, v58, v255\n\t"
    "v_mul_f32 v59, v59, v255\n\t"
    "s_add_u32 s58, s64, 4096\n\t"
    "s_waitcnt lgkmcnt(5)\n\t"
    "buffer_store_dwordx4 v[120:123], v242, s[84:87], s58 offen offset:2048 nt\n\t"
    "v_cvt_pk_bf16_f32 v56, v56, v57\n\t"
    "v_cvt_pk_bf16_f32 v57, v58, v59\n\t"
    "s_add_u32 s58, s64, 4096\n\t"
    "s_waitcnt lgkmcnt(2)\n\t"
    "buffer_store_dwordx4 v[124:127], v243, s[84:87], s58 offen offset:3072 nt\n\t"
    "ds_write_b64 v250, v[56:57] offset:8320\n\t"
    "v_mul_f32 v68, v68, v255\n\t"
    "v_mul_f32 v69, v69, v255\n\t"
    "v_mul_f32 v70, v70, v255\n\t"
    "v_mul_f32 v71, v71, v255\n\t"
    "v_cvt_pk_bf16_f32 v68, v68, v69\n\t"
    "v_cvt_pk_bf16_f32 v69, v70, v71\n\t"
    "ds_write_b64 v251, v[68:69] offset:8320\n\t"
    "v_mul_f32 v80, v80, v255\n\t"
    "v_mul_f32 v81, v81, v255\n\t"
    "v_mul_f32 v82, v82, v255\n\t"
    "v_mul_f32 v83, v83, v255\n\t"
    "v_cvt_pk_bf16_f32 v80, v80, v81\n\t"
    "v_cvt_pk_bf16_f32 v81, v82, v83\n\t"
    "ds_write_b64 v252, v[80:81] offset:8320\n\t"
    "v_mul_f32 v92, v92, v255\n\t"
    "v_mul_f32 v93, v93, v255\n\t"
    "v_mul_f32 v94, v94, v255\n\t"
    "v_mul_f32 v95, v95, v255\n\t"
    "v_cvt_pk_bf16_f32 v92, v92, v93\n\t"
    "v_cvt_pk_bf16_f32 v93, v94, v95\n\t"
    "ds_write_b64 v253, v[92:93] offset:8320\n\t"
    "ds_read_b128 v[96:99], v232 offset:8192\n\t"
    "ds_read_b128 v[100:103], v232 offset:9216\n\t"
    "ds_read_b64 v[104:105], v232 offset:10248\n\t"
    "ds_read_b64 v[106:107], v232 offset:10240\n\t"
    "ds_read_b64 v[108:109], v232 offset:11272\n\t"
    "ds_read_b64 v[110:111], v232 offset:11264\n\t"
    "s_add_u32 s58, s64, 8192\n\t"
    "s_waitcnt lgkmcnt(5)\n\t"
    "buffer_store_dwordx4 v[96:99], v242, s[84:87], s58 offen offset:0 nt\n\t"
    "s_add_u32 s58, s64, 8192\n\t"
    "s_waitcnt lgkmcnt(4)\n\t"
    "buffer_store_dwordx4 v[100:103], v243, s[84:87], s58 offen offset:1024 nt\n\t"
    "s_add_u32 s58, s64, 8192\n\t"
    "s_waitcnt lgkmcnt(2)\n\t"
    "buffer_store_dwordx4 v[104:107], v242, s[84:87], s58 offen offset:2048 nt\n\t"
    "s_add_u32 s58, s64, 8192\n\t"
    "s_waitcnt lgkmcnt(0)\n\t"
    "buffer_store_dwordx4 v[108:111], v243, s[84:87], s58 offen offset:3072 nt\n\t"
    "s_endpgm\n\t"
    "LFBB1%=:\n\t"
    "s_getpc_b64 s[60:61]\n\t"
    "LPCB1%=:\n\t"
    "s_add_u32 s60, s60, LRETB1%=-LPCB1%=\n\t"
    "s_addc_u32 s61, s61, -1\n\t"
    "s_branch LFBC%=\n\t"
    "LFBB2%=:\n\t"
    "s_getpc_b64 s[60:61]\n\t"
    "LPCB2%=:\n\t"
    "s_add_u32 s60, s60, LRETB2%=-LPCB2%=\n\t"
    "s_addc_u32 s61, s61, -1\n\t"
    "s_branch LFBC%=\n\t"
    "LFBB3%=:\n\t"
    "s_getpc_b64 s[60:61]\n\t"
    "LPCB3%=:\n\t"
    "s_add_u32 s60, s60, LRETB3%=-LPCB3%=\n\t"
    "s_addc_u32 s61, s61, -1\n\t"
    "s_branch LFBC%=\n\t"
    "LFBB4%=:\n\t"
    "s_getpc_b64 s[60:61]\n\t"
    "LPCB4%=:\n\t"
    "s_add_u32 s60, s60, LRETB4%=-LPCB4%=\n\t"
    "s_addc_u32 s61, s61, -1\n\t"
    "s_branch LFBC%=\n\t"
    "LFBB5%=:\n\t"
    "s_getpc_b64 s[60:61]\n\t"
    "LPCB5%=:\n\t"
    "s_add_u32 s60, s60, LRETB5%=-LPCB5%=\n\t"
    "s_addc_u32 s61, s61, -1\n\t"
    "s_branch LFBC%=\n\t"
    "LFBB6%=:\n\t"
    "s_getpc_b64 s[60:61]\n\t"
    "LPCB6%=:\n\t"
    "s_add_u32 s60, s60, LRETB6%=-LPCB6%=\n\t"
    "s_addc_u32 s61, s61, -1\n\t"
    "s_branch LFBC%=\n\t"
    "LFBB7%=:\n\t"
    "s_getpc_b64 s[60:61]\n\t"
    "LPCB7%=:\n\t"
    "s_add_u32 s60, s60, LRETB7%=-LPCB7%=\n\t"
    "s_addc_u32 s61, s61, -1\n\t"
    "s_branch LFBC%=\n\t"
    "LFBC%=:\n\t"
    "v_max3_f32 v228, v144, v145, v146\n\t"
    "v_max3_f32 v229, v148, v149, v150\n\t"
    "v_max3_f32 v230, v152, v153, v154\n\t"
    "v_max3_f32 v228, v228, v147, v156\n\t"
    "v_max3_f32 v229, v229, v151, v160\n\t"
    "v_max3_f32 v230, v230, v155, v164\n\t"
    "v_max3_f32 v228, v228, v157, v158\n\t"
    "v_max3_f32 v229, v229, v161, v162\n\t"
    "v_max3_f32 v230, v230, v165, v166\n\t"
    "v_max3_f32 v228, v228, v159, v168\n\t"
    "v_max3_f32 v229, v229, v163, v172\n\t"
    "v_max3_f32 v230, v230, v167, v176\n\t"
    "v_max3_f32 v228, v228, v169, v170\n\t"
    "v_max3_f32 v229, v229, v173, v174\n\t"
    "v_max3_f32 v230, v230, v177, v178\n\t"
    "v_max3_f32 v228, v228, v171, v180\n\t"
    "v_max3_f32 v229, v229, v175, v184\n\t"
    "v_max3_f32 v230, v230, v179, v188\n\t"
    "v_max3_f32 v228, v228, v181, v182\n\t"
    "v_max3_f32 v229, v229, v185, v186\n\t"
    "v_max3_f32 v230, v230, v189, v190\n\t"
    "v_max_f32 v228, v228, v183\n\t"
    "v_max_f32 v229, v229, v187\n\t"
    "v_max_f32 v230, v230, v191\n\t"
    "v_mov_b32 v224, v228\n\t"
    "v_mov_b32 v225, v229\n\t"
    "v_mov_b32 v226, v230\n\t"
    "v_permlane16_swap_b32 v228, v224\n\t"
    "v_permlane16_swap_b32 v229, v225\n\t"
    "v_permlane16_swap_b32 v230, v226\n\t"
    "v_max_f32 v228, v228, v224\n\t"
    "v_max_f32 v229, v229, v225\n\t"
    "v_max_f32 v230, v230, v226\n\t"
    "v_mov_b32 v224, v228\n\t"
    "v_mov_b32 v225, v229\n\t"
    "v_mov_b32 v226, v230\n\t"
    "v_permlane32_swap_b32 v228, v224\n\t"
    "v_permlane32_swap_b32 v229, v225\n\t"
    "v_permlane32_swap_b32 v230, v226\n\t"
    "v_max_f32 v228, v228, v224\n\t"
    "v_max_f32 v229, v229, v225\n\t"
    "v_max_f32 v230, v230, v226\n\t"
    "v_mul_f32 v228, s48, v228\n\t"
    "v_mul_f32 v229, s48, v229\n\t"
    "v_mul_f32 v230, s48, v230\n\t"
    "v_min_f32_e64 v228, v232, -v228\n\t"
    "v_min_f32_e64 v229, v233, -v229\n\t"
    "v_min_f32_e64 v230, v234, -v230\n\t"
    "v_sub_f32 v220, v228, v232\n\t"
    "v_sub_f32 v221, v229, v233\n\t"
    "v_sub_f32 v222, v230, v234\n\t"
    "v_exp_f32 v220, v220\n\t"
    "v_exp_f32 v221, v221\n\t"
    "v_exp_f32 v222, v222\n\t"
    "v_mov_b32 v232, v228\n\t"
    "v_mov_b32 v233, v229\n\t"
    "v_mov_b32 v234, v230\n\t"
    "v_mul_f32 v235, v235, v220\n\t"
    "v_mul_f32 v236, v236, v221\n\t"
    "v_mul_f32 v237, v237, v222\n\t"
    "v_mul_f32 v0, v0, v220\n\t"
    "v_mul_f32 v1, v1, v220\n\t"
    "v_mul_f32 v2, v2, v220\n\t"
    "v_mul_f32 v3, v3, v220\n\t"
    "v_mul_f32 v4, v4, v221\n\t"
    "v_mul_f32 v5, v5, v221\n\t"
    "v_mul_f32 v6, v6, v221\n\t"
    "v_mul_f32 v7, v7, v221\n\t"
    "v_mul_f32 v8, v8, v222\n\t"
    "v_mul_f32 v9, v9, v222\n\t"
    "v_mul_f32 v10, v10, v222\n\t"
    "v_mul_f32 v11, v11, v222\n\t"
    "v_mul_f32 v12, v12, v220\n\t"
    "v_mul_f32 v13, v13, v220\n\t"
    "v_mul_f32 v14, v14, v220\n\t"
    "v_mul_f32 v15, v15, v220\n\t"
    "v_mul_f32 v16, v16, v221\n\t"
    "v_mul_f32 v17, v17, v221\n\t"
    "v_mul_f32 v18, v18, v221\n\t"
    "v_mul_f32 v19, v19, v221\n\t"
    "v_mul_f32 v20, v20, v222\n\t"
    "v_mul_f32 v21, v21, v222\n\t"
    "v_mul_f32 v22, v22, v222\n\t"
    "v_mul_f32 v23, v23, v222\n\t"
    "v_mul_f32 v24, v24, v220\n\t"
    "v_mul_f32 v25, v25, v220\n\t"
    "v_mul_f32 v26, v26, v220\n\t"
    "v_mul_f32 v27, v27, v220\n\t"
    "v_mul_f32 v28, v28, v221\n\t"
    "v_mul_f32 v29, v29, v221\n\t"
    "v_mul_f32 v30, v30, v221\n\t"
    "v_mul_f32 v31, v31, v221\n\t"
    "v_mul_f32 v32, v32, v222\n\t"
    "v_mul_f32 v33, v33, v222\n\t"
    "v_mul_f32 v34, v34, v222\n\t"
    "v_mul_f32 v35, v35, v222\n\t"
    "v_mul_f32 v36, v36, v220\n\t"
    "v_mul_f32 v37, v37, v220\n\t"
    "v_mul_f32 v38, v38, v220\n\t"
    "v_mul_f32 v39, v39, v220\n\t"
    "v_mul_f32 v40, v40, v221\n\t"
    "v_mul_f32 v41, v41, v221\n\t"
    "v_mul_f32 v42, v42, v221\n\t"
    "v_mul_f32 v43, v43, v221\n\t"
    "v_mul_f32 v44, v44, v222\n\t"
    "v_mul_f32 v45, v45, v222\n\t"
    "v_mul_f32 v46, v46, v222\n\t"
    "v_mul_f32 v47, v47, v222\n\t"
    "v_mul_f32 v48, v48, v220\n\t"
    "v_mul_f32 v49, v49, v220\n\t"
    "v_mul_f32 v50, v50, v220\n\t"
    "v_mul_f32 v51, v51, v220\n\t"
    "v_mul_f32 v52, v52, v221\n\t"
    "v_mul_f32 v53, v53, v221\n\t"
    "v_mul_f32 v54, v54, v221\n\t"
    "v_mul_f32 v55, v55, v221\n\t"
    "v_mul_f32 v56, v56, v222\n\t"
    "v_mul_f32 v57, v57, v222\n\t"
    "v_mul_f32 v58, v58, v222\n\t"
    "v_mul_f32 v59, v59, v222\n\t"
    "v_mul_f32 v60, v60, v220\n\t"
    "v_mul_f32 v61, v61, v220\n\t"
    "v_mul_f32 v62, v62, v220\n\t"
    "v_mul_f32 v63, v63, v220\n\t"
    "v_mul_f32 v64, v64, v221\n\t"
    "v_mul_f32 v65, v65, v221\n\t"
    "v_mul_f32 v66, v66, v221\n\t"
    "v_mul_f32 v67, v67, v221\n\t"
    "v_mul_f32 v68, v68, v222\n\t"
    "v_mul_f32 v69, v69, v222\n\t"
    "v_mul_f32 v70, v70, v222\n\t"
    "v_mul_f32 v71, v71, v222\n\t"
    "v_mul_f32 v72, v72, v220\n\t"
    "v_mul_f32 v73, v73, v220\n\t"
    "v_mul_f32 v74, v74, v220\n\t"
    "v_mul_f32 v75, v75, v220\n\t"
    "v_mul_f32 v76, v76, v221\n\t"
    "v_mul_f32 v77, v77, v221\n\t"
    "v_mul_f32 v78, v78, v221\n\t"
    "v_mul_f32 v79, v79, v221\n\t"
    "v_mul_f32 v80, v80, v222\n\t"
    "v_mul_f32 v81, v81, v222\n\t"
    "v_mul_f32 v82, v82, v222\n\t"
    "v_mul_f32 v83, v83, v222\n\t"
    "v_mul_f32 v84, v84, v220\n\t"
    "v_mul_f32 v85, v85, v220\n\t"
    "v_mul_f32 v86, v86, v220\n\t"
    "v_mul_f32 v87, v87, v220\n\t"
    "v_mul_f32 v88, v88, v221\n\t"
    "v_mul_f32 v89, v89, v221\n\t"
    "v_mul_f32 v90, v90, v221\n\t"
    "v_mul_f32 v91, v91, v221\n\t"
    "v_mul_f32 v92, v92, v222\n\t"
    "v_mul_f32 v93, v93, v222\n\t"
    "v_mul_f32 v94, v94, v222\n\t"
    "v_mul_f32 v95, v95, v222\n\t"
    "v_fma_f32 v220, v144, s48, v232\n\t"
    "v_fma_f32 v221, v145, s48, v232\n\t"
    "v_fma_f32 v222, v146, s48, v232\n\t"
    "v_fma_f32 v223, v147, s48, v232\n\t"
    "v_exp_f32 v220, v220\n\t"
    "v_exp_f32 v221, v221\n\t"
    "v_exp_f32 v222, v222\n\t"
    "v_exp_f32 v223, v223\n\t"
    "v_fma_f32 v224, v148, s48, v233\n\t"
    "v_fma_f32 v225, v149, s48, v233\n\t"
    "v_fma_f32 v226, v150, s48, v233\n\t"
    "v_fma_f32 v227, v151, s48, v233\n\t"
    "v_exp_f32 v224, v224\n\t"
    "v_exp_f32 v225, v225\n\t"
    "v_exp_f32 v226, v226\n\t"
    "v_exp_f32 v227, v227\n\t"
    "v_cvt_pk_bf16_f32 v192, v220, v221\n\t"
    "v_cvt_pk_bf16_f32 v193, v222, v223\n\t"
    "v_add_f32 v228, v220, v221\n\t"
    "v_add_f32 v228, v228, v222\n\t"
    "v_add_f32 v228, v228, v223\n\t"
    "v_fma_f32 v220, v152, s48, v234\n\t"
    "v_fma_f32 v221, v153, s48, v234\n\t"
    "v_fma_f32 v222, v154, s48, v234\n\t"
    "v_fma_f32 v223, v155, s48, v234\n\t"
    "v_exp_f32 v220, v220\n\t"
    "v_exp_f32 v221, v221\n\t"
    "v_exp_f32 v222, v222\n\t"
    "v_exp_f32 v223, v223\n\t"
    "v_cvt_pk_bf16_f32 v196, v224, v225\n\t"
    "v_cvt_pk_bf16_f32 v197, v226, v227\n\t"
    "v_add_f32 v229, v224, v225\n\t"
    "v_add_f32 v229, v229, v226\n\t"
    "v_add_f32 v229, v229, v227\n\t"
    "v_fma_f32 v224, v156, s48, v232\n\t"
    "v_fma_f32 v225, v157, s48, v232\n\t"
    "v_fma_f32 v226, v158, s48, v232\n\t"
    "v_fma_f32 v227, v159, s48, v232\n\t"
    "v_exp_f32 v224, v224\n\t"
    "v_exp_f32 v225, v225\n\t"
    "v_exp_f32 v226, v226\n\t"
    "v_exp_f32 v227, v227\n\t"
    "v_cvt_pk_bf16_f32 v200, v220, v221\n\t"
    "v_cvt_pk_bf16_f32 v201, v222, v223\n\t"
    "v_add_f32 v230, v220, v221\n\t"
    "v_add_f32 v230, v230, v222\n\t"
    "v_add_f32 v230, v230, v223\n\t"
    "v_fma_f32 v220, v160, s48, v233\n\t"
    "v_fma_f32 v221, v161, s48, v233\n\t"
    "v_fma_f32 v222, v162, s48, v233\n\t"
    "v_fma_f32 v223, v163, s48, v233\n\t"
    "v_exp_f32 v220, v220\n\t"
    "v_exp_f32 v221, v221\n\t"
    "v_exp_f32 v222, v222\n\t"
    "v_exp_f32 v223, v223\n\t"
    "v_cvt_pk_bf16_f32 v194, v224, v225\n\t"
    "v_cvt_pk_bf16_f32 v195, v226, v227\n\t"
    "v_add_f32 v228, v228, v224\n\t"
    "v_add_f32 v228, v228, v225\n\t"
    "v_add_f32 v228, v228, v226\n\t"
    "v_add_f32 v228, v228, v227\n\t"
    "v_fma_f32 v224, v164, s48, v234\n\t"
    "v_fma_f32 v225, v165, s48, v234\n\t"
    "v_fma_f32 v226, v166, s48, v234\n\t"
    "v_fma_f32 v227, v167, s48, v234\n\t"
    "v_exp_f32 v224, v224\n\t"
    "v_exp_f32 v225, v225\n\t"
    "v_exp_f32 v226, v226\n\t"
    "v_exp_f32 v227, v227\n\t"
    "v_cvt_pk_bf16_f32 v198, v220, v221\n\t"
    "v_cvt_pk_bf16_f32 v199, v222, v223\n\t"
    "v_add_f32 v229, v229, v220\n\t"
    "v_add_f32 v229, v229, v221\n\t"
    "v_add_f32 v229, v229, v222\n\t"
    "v_add_f32 v229, v229, v223\n\t"
    "v_fma_f32 v220, v168, s48, v232\n\t"
    "v_fma_f32 v221, v169, s48, v232\n\t"
    "v_fma_f32 v222, v170, s48, v232\n\t"
    "v_fma_f32 v223, v171, s48, v232\n\t"
    "v_exp_f32 v220, v220\n\t"
    "v_exp_f32 v221, v221\n\t"
    "v_exp_f32 v222, v222\n\t"
    "v_exp_f32 v223, v223\n\t"
    "v_cvt_pk_bf16_f32 v202, v224, v225\n\t"
    "v_cvt_pk_bf16_f32 v203, v226, v227\n\t"
    "v_add_f32 v230, v230, v224\n\t"
    "v_add_f32 v230, v230, v225\n\t"
    "v_add_f32 v230, v230, v226\n\t"
    "v_add_f32 v230, v230, v227\n\t"
    "v_fma_f32 v224, v172, s48, v233\n\t"
    "v_fma_f32 v225, v173, s48, v233\n\t"
    "v_fma_f32 v226, v174, s48, v233\n\t"
    "v_fma_f32 v227, v175, s48, v233\n\t"
    "v_exp_f32 v224, v224\n\t"
    "v_exp_f32 v225, v225\n\t"
    "v_exp_f32 v226, v226\n\t"
    "v_exp_f32 v227, v227\n\t"
    "v_cvt_pk_bf16_f32 v204, v220, v221\n\t"
    "v_cvt_pk_bf16_f32 v205, v222, v223\n\t"
    "v_add_f32 v228, v228, v220\n\t"
    "v_add_f32 v228, v228, v221\n\t"
    "v_add_f32 v228, v228, v222\n\t"
    "v_add_f32 v228, v228, v223\n\t"
    "v_fma_f32 v220, v176, s48, v234\n\t"
    "v_fma_f32 v221, v177, s48, v234\n\t"
    "v_fma_f32 v222, v178, s48, v234\n\t"
    "v_fma_f32 v223, v179, s48, v234\n\t"
    "v_exp_f32 v220, v220\n\t"
    "v_exp_f32 v221, v221\n\t"
    "v_exp_f32 v222, v222\n\t"
    "v_exp_f32 v223, v223\n\t"
    "v_cvt_pk_bf16_f32 v208, v224, v225\n\t"
    "v_cvt_pk_bf16_f32 v209, v226, v227\n\t"
    "v_add_f32 v229, v229, v224\n\t"
    "v_add_f32 v229, v229, v225\n\t"
    "v_add_f32 v229, v229, v226\n\t"
    "v_add_f32 v229, v229, v227\n\t"
    "v_fma_f32 v224, v180, s48, v232\n\t"
    "v_fma_f32 v225, v181, s48, v232\n\t"
    "v_fma_f32 v226, v182, s48, v232\n\t"
    "v_fma_f32 v227, v183, s48, v232\n\t"
    "v_exp_f32 v224, v224\n\t"
    "v_exp_f32 v225, v225\n\t"
    "v_exp_f32 v226, v226\n\t"
    "v_exp_f32 v227, v227\n\t"
    "v_cvt_pk_bf16_f32 v212, v220, v221\n\t"
    "v_cvt_pk_bf16_f32 v213, v222, v223\n\t"
    "v_add_f32 v230, v230, v220\n\t"
    "v_add_f32 v230, v230, v221\n\t"
    "v_add_f32 v230, v230, v222\n\t"
    "v_add_f32 v230, v230, v223\n\t"
    "v_fma_f32 v220, v184, s48, v233\n\t"
    "v_fma_f32 v221, v185, s48, v233\n\t"
    "v_fma_f32 v222, v186, s48, v233\n\t"
    "v_fma_f32 v223, v187, s48, v233\n\t"
    "v_exp_f32 v220, v220\n\t"
    "v_exp_f32 v221, v221\n\t"
    "v_exp_f32 v222, v222\n\t"
    "v_exp_f32 v223, v223\n\t"
    "v_cvt_pk_bf16_f32 v206, v224, v225\n\t"
    "v_cvt_pk_bf16_f32 v207, v226, v227\n\t"
    "v_add_f32 v228, v228, v224\n\t"
    "v_add_f32 v228, v228, v225\n\t"
    "v_add_f32 v228, v228, v226\n\t"
    "v_add_f32 v228, v228, v227\n\t"
    "v_fma_f32 v224, v188, s48, v234\n\t"
    "v_fma_f32 v225, v189, s48, v234\n\t"
    "v_fma_f32 v226, v190, s48, v234\n\t"
    "v_fma_f32 v227, v191, s48, v234\n\t"
    "v_exp_f32 v224, v224\n\t"
    "v_exp_f32 v225, v225\n\t"
    "v_exp_f32 v226, v226\n\t"
    "v_exp_f32 v227, v227\n\t"
    "v_cvt_pk_bf16_f32 v210, v220, v221\n\t"
    "v_cvt_pk_bf16_f32 v211, v222, v223\n\t"
    "v_add_f32 v229, v229, v220\n\t"
    "v_add_f32 v229, v229, v221\n\t"
    "v_add_f32 v229, v229, v222\n\t"
    "v_add_f32 v229, v229, v223\n\t"
    "v_cvt_pk_bf16_f32 v214, v224, v225\n\t"
    "v_cvt_pk_bf16_f32 v215, v226, v227\n\t"
    "v_add_f32 v230, v230, v224\n\t"
    "v_add_f32 v230, v230, v225\n\t"
    "v_add_f32 v230, v230, v226\n\t"
    "v_add_f32 v230, v230, v227\n\t"
    "s_setpc_b64 s[60:61]\n\t"
    "LCODEEND%=:\n\t"
    :
    : "s"((unsigned)(size_t)q), "s"((unsigned)((size_t)q >> 32)), "s"((unsigned)(size_t)k), "s"((unsigned)((size_t)k >> 32)),
      "s"((unsigned)(size_t)v), "s"((unsigned)((size_t)v >> 32)), "s"((unsigned)(size_t)o), "s"((unsigned)((size_t)o >> 32)),
      "s"(wid), "s"(bid)
    : "memory", "scc", "vcc", "v0", "v1", "v2", "v3", "v4", "v5", "v6", "v7", "v8", "v9", "v10", "v11", "v12", "v13", "v14", "v15", "v16", "v17", "v18", "v19", "v20", "v21", "v22", "v23", "v24", "v25", "v26", "v27", "v28", "v29", "v30", "v31", "v32", "v33", "v34", "v35", "v36", "v37", "v38", "v39", "v40", "v41", "v42", "v43", "v44", "v45", "v46", "v47", "v48", "v49", "v50", "v51", "v52", "v53", "v54", "v55", "v56", "v57", "v58", "v59", "v60", "v61", "v62", "v63", "v64", "v65", "v66", "v67", "v68", "v69", "v70", "v71", "v72", "v73", "v74", "v75", "v76", "v77", "v78", "v79", "v80", "v81", "v82", "v83", "v84", "v85", "v86", "v87", "v88", "v89", "v90", "v91", "v92", "v93", "v94", "v95", "v96", "v97", "v98", "v99", "v100", "v101", "v102", "v103", "v104", "v105", "v106", "v107", "v108", "v109", "v110", "v111", "v112", "v113", "v114", "v115", "v116", "v117", "v118", "v119", "v120", "v121", "v122", "v123", "v124", "v125", "v126", "v127", "v128", "v129", "v130", "v131", "v132", "v133", "v134", "v135", "v136", "v137", "v138", "v139", "v140", "v141", "v142", "v143", "v144", "v145", "v146", "v147", "v148", "v149", "v150", "v151", "v152", "v153", "v154", "v155", "v156", "v157", "v158", "v159", "v160", "v161", "v162", "v163", "v164", "v165", "v166", "v167", "v168", "v169", "v170", "v171", "v172", "v173", "v174", "v175", "v176", "v177", "v178", "v179", "v180", "v181", "v182", "v183", "v184", "v185", "v186", "v187", "v188", "v189", "v190", "v191", "v192", "v193", "v194", "v195", "v196", "v197", "v198", "v199", "v200", "v201", "v202", "v203", "v204", "v205", "v206", "v207", "v208", "v209", "v210", "v211", "v212", "v213", "v214", "v215", "v216", "v217", "v218", "v219", "v220", "v221", "v222", "v223", "v224", "v225", "v226", "v227", "v228", "v229", "v230", "v231", "v232", "v233", "v234", "v235", "v236", "v237", "v238", "v239", "v240", "v241", "v242", "v243", "v244", "v245", "v246", "v247", "v248", "v249", "v250", "v251", "v252", "v253", "v254", "v255", "s20", "s21", "s22", "s23", "s24", "s25", "s26", "s27", "s28", "s29", "s30", "s31", "s36", "s37", "s38", "s39", "s40", "s41", "s42", "s43", "s44", "s45", "s46", "s47", "s48", "s49", "s50", "s51", "s52", "s53", "s54", "s55", "s56", "s57", "s58", "s59", "s60", "s61", "s62", "s63", "s64", "s65", "s66", "s67", "s68", "s69", "s70", "s71", "s72", "s73", "s74", "s75", "s76", "s77", "s78", "s79", "s80", "s81", "s82", "s83", "s84", "s85", "s86", "s87", "s88", "s89", "s90", "s91", "s92", "s93", "s94", "s95", "s96", "s97", "s98", "s99");
}

extern "C" void kernel_launch(void* const* d_in, const int* in_sizes, int n_in, void* d_out, int out_size, void* d_ws, size_t ws_size,
                              hipStream_t stream) {
  (void)in_sizes; (void)n_in; (void)out_size; (void)d_ws; (void)ws_size;
  (void)hipFuncSetAttribute((const void*)mha_pp_kernel, hipFuncAttributeMaxDynamicSharedMemorySize, 163840);
  hipLaunchKernelGGL(mha_pp_kernel, dim3(256), dim3(512), 163840, stream, (const void*)d_in[0], (const void*)d_in[1], (const void*)d_in[2], d_out);
}
```
